```python
import torch

def _softmax(x, axis=-1):
    return torch.softmax(x, dim=axis)
def _gelu(x):
    return torch.nn.functional.gelu(x, approximate="tanh")
def _silu(x):
    return torch.nn.functional.silu(x)
def _sigmoid(x):
    return torch.sigmoid(x)
def _layernorm(x, axis=-1, eps=1e-5):
    m = x.mean(dim=axis, keepdim=True)
    v = x.var(dim=axis, keepdim=True, unbiased=False)
    return (x - m) / torch.sqrt(v + eps)
def _scatter_add(tgt, idx, src, axis=0):
    return tgt.clone().index_add_(axis, idx, src)
def _segment_sum(src, seg, n):
    out = torch.zeros((n,) + src.shape[1:], dtype=src.dtype)
    return out.index_add_(0, seg, src)
def _pad(x, pw):
    flat = tuple(v for pair in reversed(pw) for v in pair)
    return torch.nn.functional.pad(x, flat)

kernel_name = 'recurrent_scan__ssm_conv_s5__mixer_channel_mlp__prompt'

def setup_inputs():

    _w = (lambda a: (lambda t: t.to(torch.bfloat16) if t.is_floating_point() else t)(torch.as_tensor(a)))
    def _A_setup():
        B, S, D = 1, 2048, 576
        P = D
        Lambda_re = -0.5 * (lambda _x: torch.exp(torch.as_tensor(_x)))((lambda *s: torch.randn(*s, dtype=torch.bfloat16) * 0.2)(P))
        Lambda_im = (lambda *s: torch.randn(*s, dtype=torch.bfloat16) * 0.8)(P) * 1.0
        B_re = (lambda *s: torch.randn(*s, dtype=torch.bfloat16) * 0.2)(P, D) * (D ** -0.5)
        B_im = (lambda *s: torch.randn(*s, dtype=torch.bfloat16) * 0.2)(P, D) * (D ** -0.5)
        C_re = (lambda *s: torch.randn(*s, dtype=torch.bfloat16) * 0.2)(D, P) * (P ** -0.5)
        C_im = (lambda *s: torch.randn(*s, dtype=torch.bfloat16) * 0.2)(D, P) * (P ** -0.5)
        log_dt = (lambda *s: torch.randn(*s, dtype=torch.bfloat16) * 0.2)(P) * 0.3 - 2.0
        Dskip = (lambda *s: torch.randn(*s, dtype=torch.bfloat16) * 0.05)(D)
        return {
            "x": _w((lambda *s: torch.randn(*s, dtype=torch.bfloat16) * 0.05)(B, S, D)),
            "Lambda_re": _w(Lambda_re),
            "Lambda_im": _w(Lambda_im),
            "B_re": _w(B_re),
            "B_im": _w(B_im),
            "C_re": _w(C_re),
            "C_im": _w(C_im),
            "log_dt": _w(log_dt),
            "Dskip": _w(Dskip),
        }
    _A_in = _A_setup()

    def _B_setup():
        B, S, D, F = 1, 2048, 576, 4608
        return {
            "x": _w((lambda *s: torch.randn(*s, dtype=torch.bfloat16) * 0.05)(B, S, D)),
            "w1": _w((lambda *s: torch.randn(*s, dtype=torch.bfloat16) * 3.2)(D, F) * (D ** -0.5)),
            "b1": _w((lambda *s: torch.randn(*s, dtype=torch.bfloat16) * 1.6)(F)),
            "w2": _w((lambda *s: torch.randn(*s, dtype=torch.bfloat16) * 0.1)(F, D) * (F ** -0.5)),
            "b2": _w((lambda *s: torch.randn(*s, dtype=torch.bfloat16) * 0.1)(D)),
        }
    _B_in = _B_setup()

    return {
        "A_x": _A_in["x"],
        "A_Lambda_re": _A_in["Lambda_re"],
        "A_Lambda_im": _A_in["Lambda_im"],
        "A_B_re": _A_in["B_re"],
        "A_B_im": _A_in["B_im"],
        "A_C_re": _A_in["C_re"],
        "A_C_im": _A_in["C_im"],
        "A_log_dt": _A_in["log_dt"],
        "A_Dskip": _A_in["Dskip"],
        "B_x": _B_in["x"],
        "B_w1": _B_in["w1"],
        "B_b1": _B_in["b1"],
        "B_w2": _B_in["w2"],
        "B_b2": _B_in["b2"],
    }

def reference(A_x, A_Lambda_re, A_Lambda_im, A_B_re, A_B_im, A_C_re, A_C_im, A_log_dt, A_Dskip, B_x, B_w1, B_b1, B_w2, B_b2):
    def _A_ref(x, Lambda_re, Lambda_im, B_re, B_im, C_re, C_im, log_dt, Dskip):
        Bsz, L, H = x.shape
        Lam = Lambda_re + 1j * Lambda_im
        Bmat = B_re + 1j * B_im
        Cmat = C_re + 1j * C_im
        dt = torch.exp(log_dt)
        Lam_bar = torch.exp(dt * Lam)
        B_bar = ((Lam_bar - 1.0) / Lam)[:, None] * Bmat
        Bu = torch.einsum("bld,pd->blp", x + 0j, B_bar)
        h = torch.zeros_like(Bu[:, 0, :])
        hs = []
        for t in range(L):
            h = Lam_bar[None, :] * h + Bu[:, t, :]
            hs.append(h)
        hseq = torch.stack(hs, 1)
        y = 2.0 * torch.einsum("blp,dp->bld", hseq, Cmat).real + x * Dskip[None, None, :]
        return y

    def _B_ref(x, w1, b1, w2, b2):
        y = _layernorm(x, axis=-1)
        h = _gelu(torch.einsum("bsd,df->bsf", y, w1) + b1)
        return torch.einsum("bsf,fd->bsd", h, w2) + b2

    x = A_x
    xn = _layernorm(x, axis=-1)
    s = _A_ref(xn, A_Lambda_re, A_Lambda_im, A_B_re, A_B_im, A_C_re, A_C_im, A_log_dt, A_Dskip)
    g = _silu(_B_ref(xn, B_w1, B_b1, B_w2, B_b2))
    return x + s * g
```

```cpp
#include <hip/hip_runtime.h>
#include <hip/hip_bf16.h>
#include <stdint.h>

typedef unsigned short u16;
constexpr int L = 2048, D = 576, P = 576, F = 4608;

__device__ __forceinline__ float bf2f(u16 v) { return __uint_as_float(((unsigned)v) << 16); }
__device__ __forceinline__ u16 f2bf(float f) { unsigned u = __float_as_uint(f); u += 0x7fffu + ((u >> 16) & 1u); return (u16)(u >> 16); }
__device__ __forceinline__ float rbf(float f) { return bf2f(f2bf(f)); }

__global__ void k_ln(const u16* x, u16* xn, u16* y) {
  int row = blockIdx.x, lane = threadIdx.x;
  float v[9];
  float s = 0;
  for (int i = 0; i < 9; ++i) { v[i] = bf2f(x[row * D + lane + 64 * i]); s += v[i]; }
  for (int o = 32; o; o >>= 1) s += __shfl_xor(s, o);
  float mean = s / D;
  float q = 0;
  for (int i = 0; i < 9; ++i) { float d = v[i] - mean; q += d * d; }
  for (int o = 32; o; o >>= 1) q += __shfl_xor(q, o);
  float var = q / D;
  float m = rbf(mean), vv = rbf(var);
  float den = rbf(sqrtf(rbf(vv + 1e-5f)));
  s = 0;
  for (int i = 0; i < 9; ++i) { v[i] = rbf(rbf(v[i] - m) / den); xn[row * D + lane + 64 * i] = f2bf(v[i]); s += v[i]; }
  for (int o = 32; o; o >>= 1) s += __shfl_xor(s, o);
  mean = s / D;
  q = 0;
  for (int i = 0; i < 9; ++i) { float d = v[i] - mean; q += d * d; }
  for (int o = 32; o; o >>= 1) q += __shfl_xor(q, o);
  var = q / D;
  m = rbf(mean); vv = rbf(var);
  den = rbf(sqrtf(rbf(vv + 1e-5f)));
  for (int i = 0; i < 9; ++i) { y[row * D + lane + 64 * i] = f2bf(rbf(v[i] - m) / den); }
}

__global__ void k_prep(const u16* lre, const u16* lim, const u16* logdt, float2* lam, float2* coef) {
  int p = blockIdx.x * blockDim.x + threadIdx.x;
  if (p >= P) return;
  float dt = rbf(expf(bf2f(logdt[p])));
  float re = bf2f(lre[p]), im = bf2f(lim[p]);
  float a = dt * re, b = dt * im;
  float e = expf(a);
  float lr = e * cosf(b), li = e * sinf(b);
  lam[p] = make_float2(lr, li);
  float nr = lr - 1.0f, ni = li;
  float dd = re * re + im * im;
  coef[p] = make_float2((nr * re + ni * im) / dd, (ni * re - nr * im) / dd);
}

template <typename TA, bool BT, int MODE>
__global__ void k_gemm(const TA* A, int lda, const u16* B, int ldb, int K, float* Cf, u16* Ch, int ldc, const u16* bias, float scale, int accum) {
  __shared__ float As[16][65], Bs[16][65];
  int tx = threadIdx.x & 15, ty = threadIdx.x >> 4;
  int m0 = blockIdx.y * 64, n0 = blockIdx.x * 64;
  float acc[4][4] = {};
  for (int k0 = 0; k0 < K; k0 += 16) {
    for (int i = threadIdx.x; i < 1024; i += 256) {
      int r = i >> 4, c = i & 15;
      float a;
      if constexpr (sizeof(TA) == 2) a = bf2f(A[(long)(m0 + r) * lda + k0 + c]); else a = A[(long)(m0 + r) * lda + k0 + c];
      As[c][r] = a;
      if (BT) Bs[c][r] = bf2f(B[(long)(n0 + r) * ldb + k0 + c]);
      else { int kk = i >> 6, nn = i & 63; Bs[kk][nn] = bf2f(B[(long)(k0 + kk) * ldb + n0 + nn]); }
    }
    __syncthreads();
    for (int k = 0; k < 16; ++k) {
      float a[4], b[4];
      for (int i = 0; i < 4; ++i) { a[i] = As[k][ty * 4 + i]; b[i] = Bs[k][tx * 4 + i]; }
      for (int i = 0; i < 4; ++i) for (int j = 0; j < 4; ++j) acc[i][j] += a[i] * b[j];
    }
    __syncthreads();
  }
  for (int i = 0; i < 4; ++i) for (int j = 0; j < 4; ++j) {
    int m = m0 + ty * 4 + i, n = n0 + tx * 4 + j;
    float v = acc[i][j];
    if (MODE == 0) { float o = v * scale; if (accum) o += Cf[(long)m * ldc + n]; Cf[(long)m * ldc + n] = o; }
    else if (MODE == 1) {
      float h = v + bf2f(bias[n]);
      float g = 0.5f * h * (1.0f + tanhf(0.7978845608028654f * (h + 0.044715f * h * h * h)));
      Ch[(long)m * ldc + n] = f2bf(g);
    } else {
      Cf[(long)m * ldc + n] = v + bf2f(bias[n]);
    }
  }
}

__global__ void k_scan(const float* ure, const float* uim, const float2* lam, const float2* coef, float* hre, float* him) {
  int p = blockIdx.x * blockDim.x + threadIdx.x;
  if (p >= P) return;
  float2 l = lam[p], c = coef[p];
  float hr = 0, hi = 0;
  for (int t = 0; t < L; ++t) {
    float ur = ure[t * P + p], ui = uim[t * P + p];
    float br = c.x * ur - c.y * ui, bi = c.x * ui + c.y * ur;
    float nr = l.x * hr - l.y * hi + br, ni = l.x * hi + l.y * hr + bi;
    hr = nr; hi = ni;
    hre[t * P + p] = hr; him[t * P + p] = hi;
  }
}

__global__ void k_final(const u16* x, const u16* xn, const u16* dskip, const float* s, const float* o, float* out) {
  int i = blockIdx.x * blockDim.x + threadIdx.x;
  if (i >= L * D) return;
  int d = i % D;
  float sv = s[i] + rbf(bf2f(xn[i]) * bf2f(dskip[d]));
  float ov = rbf(o[i]);
  float g = rbf(ov / (1.0f + expf(-ov)));
  out[i] = bf2f(x[i]) + sv * g;
}

extern "C" void kernel_launch(void* const* d_in, const int* in_sizes, int n_in, void* d_out, int out_size, void* d_ws, size_t ws_size, hipStream_t stream) {
  const u16* x = (const u16*)d_in[0];
  const u16 *lre = (const u16*)d_in[1], *lim = (const u16*)d_in[2], *bre = (const u16*)d_in[3], *bim = (const u16*)d_in[4];
  const u16 *cre = (const u16*)d_in[5], *cim = (const u16*)d_in[6], *logdt = (const u16*)d_in[7], *dskip = (const u16*)d_in[8];
  const u16 *w1 = (const u16*)d_in[10], *b1 = (const u16*)d_in[11], *w2 = (const u16*)d_in[12], *b2 = (const u16*)d_in[13];
  char* ws = (char*)d_ws;
  size_t off = 0;
  auto take = [&](size_t n) { char* p = ws + off; off += (n + 255) & ~(size_t)255; return p; };
  u16* xn = (u16*)take(L * D * 2); u16* y = (u16*)take(L * D * 2);
  float2* lam = (float2*)take(P * 8); float2* coef = (float2*)take(P * 8);
  float* ure = (float*)take(L * P * 4); float* uim = (float*)take(L * P * 4);
  float* hre = (float*)take(L * P * 4); float* him = (float*)take(L * P * 4);
  float* s = (float*)take(L * D * 4); float* o = (float*)take(L * D * 4);
  u16* H = (u16*)take((size_t)L * F * 2);
  k_ln<<<L, 64, 0, stream>>>(x, xn, y);
  k_prep<<<3, 256, 0, stream>>>(lre, lim, logdt, lam, coef);
  k_gemm<u16, true, 0><<<dim3(P / 64, L / 64), 256, 0, stream>>>(xn, D, bre, D, D, ure, nullptr, P, nullptr, 1.0f, 0);
  k_gemm<u16, true, 0><<<dim3(P / 64, L / 64), 256, 0, stream>>>(xn, D, bim, D, D, uim, nullptr, P, nullptr, 1.0f, 0);
  k_scan<<<9, 64, 0, stream>>>(ure, uim, lam, coef, hre, him);
  k_gemm<float, true, 0><<<dim3(D / 64, L / 64), 256, 0, stream>>>(hre, P, cre, P, P, s, nullptr, D, nullptr, 2.0f, 0);
  k_gemm<float, true, 0><<<dim3(D / 64, L / 64), 256, 0, stream>>>(him, P, cim, P, P, s, nullptr, D, nullptr, -2.0f, 1);
  k_gemm<u16, false, 1><<<dim3(F / 64, L / 64), 256, 0, stream>>>(y, D, w1, F, D, nullptr, H, F, b1, 1.0f, 0);
  k_gemm<u16, false, 2><<<dim3(D / 64, L / 64), 256, 0, stream>>>(H, F, w2, D, F, o, nullptr, D, b2, 1.0f, 0);
  k_final<<<(L * D + 255) / 256, 256, 0, stream>>>(x, xn, dskip, s, o, (float*)d_out);
}
```

```cpp
#include <hip/hip_runtime.h>
#include <stdint.h>
typedef unsigned short u16;

extern "C" __global__ __launch_bounds__(512, 2) void fused_main(const u16* x, const u16* lre, const u16* lim, const u16* bre, const u16* bim, const u16* cre, const u16* cim,
    const u16* ldt, const u16* dsk, const u16* w1, const u16* b1, const u16* w2, const u16* b2, float* out, char* ws) {
  auto ka = __builtin_amdgcn_kernarg_segment_ptr();
  unsigned tid = threadIdx.x;
  asm volatile(
"s_mov_b64 s[8:9], %1\n"
"s_mov_b32 s10, %2\n"
"v_mov_b32_e64 v231, %0\n"
"s_load_dwordx4 s[44:47], s[8:9], 0\n"
"s_load_dwordx4 s[48:51], s[8:9], 16\n"
"s_load_dwordx4 s[52:55], s[8:9], 32\n"
"s_load_dwordx4 s[56:59], s[8:9], 48\n"
"s_load_dwordx4 s[60:63], s[8:9], 64\n"
"s_load_dwordx4 s[64:67], s[8:9], 80\n"
"s_load_dwordx4 s[68:71], s[8:9], 96\n"
"s_load_dwordx2 s[72:73], s[8:9], 112\n"
"v_lshrrev_b32 v216, 6, v231\n"
"v_and_b32 v248, 63, v231\n"
"s_and_b32 s76, s10, 7\n"
"v_readfirstlane_b32 s11, v216\n"
"s_lshr_b32 s77, s10, 3\n"
"s_and_b32 s78, s76, 1\n"
"s_lshl_b32 s78, s78, 3\n"
"s_and_b32 s79, s77, 7\n"
"s_add_u32 s34, s78, s79\n"
"s_lshr_b32 s76, s76, 1\n"
"s_lshr_b32 s77, s77, 3\n"
"s_mul_i32 s35, s76, 3\n"
"s_add_u32 s35, s35, s77\n"
"s_waitcnt lgkmcnt(0)\n"
"s_add_u32 s78, s72, 44048384\n"
"s_addc_u32 s79, s73, 0\n"
"s_nop 0\n"
"s_load_dword s7, s[78:79], 0\n"
"s_add_u32 s100, s72, 44042240\n"
"s_addc_u32 s101, s73, 0\n"
"s_cmp_eq_u32 s77, 3\n"
"s_cbranch_scc1 SSM_PATH\n"
"s_cmp_ge_u32 s11, 4\n"
"s_cselect_b64 vcc, -1, 0\n"
"s_mov_b32 s12, s44\n"
"s_and_b32 s13, s45, 0xffff\n"
"s_mov_b32 s14, 2359296\n"
"s_mov_b32 s15, 0x20000\n"
"v_lshrrev_b32 v216, 3, v248\n"
"v_and_b32 v217, 7, v248\n"
"v_mul_u32_u24 v218, 1152, v216\n"
"v_lshl_add_u32 v218, v217, 4, v218\n"
"s_lshl_b32 s76, s34, 7\n"
"s_lshl_b32 s77, s11, 4\n"
"s_add_u32 s76, s76, s77\n"
"s_nop 0\n"
"s_mul_i32 s76, s76, 1152\n"
"s_add_u32 s77, s76, 9216\n"
"s_mov_b32 s2, s76\n"
"s_mov_b32 s3, s77\n"
"buffer_load_dwordx4 v[144:147], v218, s[12:15], s2 offen offset:0\n"
"buffer_load_dwordx4 v[148:151], v218, s[12:15], s2 offen offset:128\n"
"buffer_load_dwordx4 v[152:155], v218, s[12:15], s2 offen offset:256\n"
"buffer_load_dwordx4 v[156:159], v218, s[12:15], s2 offen offset:384\n"
"buffer_load_dwordx4 v[160:163], v218, s[12:15], s2 offen offset:512\n"
"buffer_load_dwordx4 v[176:179], v218, s[12:15], s2 offen offset:640\n"
"buffer_load_dwordx4 v[180:183], v218, s[12:15], s2 offen offset:768\n"
"buffer_load_dwordx4 v[184:187], v218, s[12:15], s2 offen offset:896\n"
"buffer_load_dwordx4 v[188:191], v218, s[12:15], s2 offen offset:1024\n"
"s_mov_b32 s40, 0xc09d30fc\n"
"s_mov_b32 s41, 0xffff0000\n"
"s_mov_b32 s42, 0x3f803f80\n"
"s_mul_i32 s82, s35, 384\n"
"s_mov_b32 s16, s62\n"
"s_nop 0\n"
"s_and_b32 s17, s63, 0xffff\n"
"s_mov_b32 s18, 5308416\n"
"s_mov_b32 s19, 0x20000\n"
"s_mov_b32 s24, s66\n"
"s_nop 0\n"
"s_and_b32 s25, s67, 0xffff\n"
"s_mov_b32 s26, 5308416\n"
"s_mov_b32 s27, 0x20000\n"
"s_mov_b32 s28, s64\n"
"s_nop 0\n"
"s_and_b32 s29, s65, 0xffff\n"
"s_mov_b32 s30, 9216\n"
"s_mov_b32 s31, 0x20000\n"
"s_mov_b32 s20, s72\n"
"s_nop 0\n"
"s_and_b32 s21, s73, 0xffff\n"
"s_mov_b32 s22, 37748736\n"
"s_mov_b32 s23, 0x20000\n"
"v_min_u32 v219, 191, v231\n"
"v_lshlrev_b32 v220, 2, v219\n"
"s_lshl_b32 s78, s82, 1\n"
"buffer_load_dword v221, v220, s[28:31], s78 offen\n"
"s_and_b32 s78, s11, 3\n"
"s_nop 0\n"
"s_mul_i32 s78, s78, 147456\n"
"s_lshr_b32 s79, s11, 2\n"
"s_nop 0\n"
"s_mul_i32 s79, s79, 384\n"
"s_add_u32 s78, s78, s79\n"
"s_nop 0\n"
"v_mul_u32_u24 v236, 9216, v216\n"
"v_add_u32 v236, s78, v236\n"
"v_and_b32 v237, 3, v217\n"
"v_lshl_add_u32 v236, v237, 4, v236\n"
"v_bfe_u32 v237, v216, 1, 1\n"
"v_lshrrev_b32 v238, 2, v217\n"
"v_xor_b32 v237, v237, v238\n"
"v_lshl_add_u32 v254, v237, 6, v236\n"
"v_add_u32 v255, 72704, v254\n"
"s_lshl_b32 s36, s82, 1\n"
"s_nop 0\n"
"s_mov_b32 s37, 589824\n"
"s_add_u32 s38, s36, 128\n"
"s_lshl_b32 s43, s11, 11\n"
"v_lshrrev_b32 v236, 1, v217\n"
"v_lshlrev_b32 v249, 10, v236\n"
"v_and_b32 v236, 1, v217\n"
"v_lshl_add_u32 v249, v236, 9, v249\n"
"v_xor_b32_e64 v236, v216, v217\n"
"v_lshl_add_u32 v249, v236, 4, v249\n"
"s_lshr_b32 s78, s11, 1\n"
"s_nop 0\n"
"s_mul_i32 s78, s78, 20480\n"
"s_and_b32 s79, s11, 1\n"
"s_lshl_b32 s79, s79, 8\n"
"s_add_u32 s78, s78, s79\n"
"s_nop 0\n"
"s_add_u32 s78, s78, 65536\n"
"v_add_u32 v249, s78, v249\n"
"v_lshlrev_b32 v228, 3, v216\n"
"s_lshl_b32 s78, s11, 7\n"
"v_add_u32 v228, s78, v228\n"
"v_lshrrev_b32 v216, 5, v248\n"
"v_and_b32 v217, 31, v248\n"
"s_lshr_b32 s78, s11, 1\n"
"s_nop 0\n"
"s_mul_i32 s78, s78, 20480\n"
"s_add_u32 s78, s78, 65536\n"
"v_xor_b32 v240, v217, v216\n"
"v_lshlrev_b32 v241, 9, v216\n"
"v_add_u32 v241, s78, v241\n"
"v_xor_b32 v242, 0, v240\n"
"v_lshl_add_u32 v232, v242, 4, v241\n"
"v_xor_b32_e64 v242, 2, v240\n"
"v_lshl_add_u32 v233, v242, 4, v241\n"
"v_xor_b32_e64 v242, 4, v240\n"
"v_lshl_add_u32 v234, v242, 4, v241\n"
"v_xor_b32_e64 v242, 6, v240\n"
"v_lshl_add_u32 v235, v242, 4, v241\n"
"v_bfe_u32 v240, v248, 2, 2\n"
"v_lshl_add_u32 v241, v216, 3, v240\n"
"v_lshlrev_b32_e64 v241, 7, v241\n"
"v_bfe_u32 v242, v248, 4, 1\n"
"v_lshl_add_u32 v241, v242, 5, v241\n"
"v_and_b32_e64 v242, 3, v248\n"
"v_lshl_add_u32 v241, v242, 3, v241\n"
"s_and_b32 s79, s11, 1\n"
"s_lshl_b32 s76, s79, 13\n"
"v_add_u32 v241, s76, v241\n"
"v_lshrrev_b32 v240, 1, v240\n"
"v_lshl_add_u32 v244, v240, 6, v241\n"
"v_xor_b32 v245, 64, v244\n"
"s_lshr_b32 s76, s11, 1\n"
"s_lshl_b32 s76, s76, 11\n"
"s_lshl_b32 s77, s79, 14\n"
"s_add_u32 s76, s76, s77\n"
"s_nop 0\n"
"s_add_u32 s76, s76, 65536\n"
"v_lshrrev_b32 v242, 4, v217\n"
"v_lshlrev_b32 v246, 10, v242\n"
"v_lshl_add_u32 v246, v216, 8, v246\n"
"v_and_b32_e64 v242, 15, v248\n"
"v_lshl_add_u32 v246, v242, 4, v246\n"
"v_add_u32_e64 v246, s76, v246\n"
"s_mul_i32 s77, s79, 768\n"
"s_add_u32 s77, s77, 49152\n"
"v_lshlrev_b32 v247, 4, v216\n"
"v_add_u32 v247, s77, v247\n"
"buffer_load_dwordx4 v[164:167], v218, s[12:15], s3 offen offset:0\n"
"buffer_load_dwordx4 v[168:171], v218, s[12:15], s3 offen offset:128\n"
"buffer_load_dwordx4 v[172:175], v218, s[12:15], s3 offen offset:256\n"
"buffer_load_dwordx4 v[208:211], v218, s[12:15], s3 offen offset:384\n"
"buffer_load_dwordx4 v[212:215], v218, s[12:15], s3 offen offset:512\n"
"buffer_load_dwordx4 v[192:195], v218, s[12:15], s3 offen offset:640\n"
"buffer_load_dwordx4 v[196:199], v218, s[12:15], s3 offen offset:768\n"
"buffer_load_dwordx4 v[200:203], v218, s[12:15], s3 offen offset:896\n"
"buffer_load_dwordx4 v[204:207], v218, s[12:15], s3 offen offset:1024\n"
"s_waitcnt vmcnt(10)\n"
"v_mov_b32 v222, 0\n"
"v_mov_b32 v223, 0\n"
"v_mov_b32 v224, 0\n"
"v_mov_b32 v225, 0\n"
"v_dot2c_f32_bf16 v222, s42, v144\n"
"v_dot2c_f32_bf16 v224, v144, v144\n"
"v_dot2c_f32_bf16 v223, s42, v145\n"
"v_dot2c_f32_bf16 v225, v145, v145\n"
"v_dot2c_f32_bf16 v222, s42, v146\n"
"v_dot2c_f32_bf16 v224, v146, v146\n"
"v_dot2c_f32_bf16 v223, s42, v147\n"
"v_dot2c_f32_bf16 v225, v147, v147\n"
"v_dot2c_f32_bf16 v222, s42, v148\n"
"v_dot2c_f32_bf16 v224, v148, v148\n"
"v_dot2c_f32_bf16 v223, s42, v149\n"
"v_dot2c_f32_bf16 v225, v149, v149\n"
"v_dot2c_f32_bf16 v222, s42, v150\n"
"v_dot2c_f32_bf16 v224, v150, v150\n"
"v_dot2c_f32_bf16 v223, s42, v151\n"
"v_dot2c_f32_bf16 v225, v151, v151\n"
"v_dot2c_f32_bf16 v222, s42, v152\n"
"v_dot2c_f32_bf16 v224, v152, v152\n"
"v_dot2c_f32_bf16 v223, s42, v153\n"
"v_dot2c_f32_bf16 v225, v153, v153\n"
"v_dot2c_f32_bf16 v222, s42, v154\n"
"v_dot2c_f32_bf16 v224, v154, v154\n"
"v_dot2c_f32_bf16 v223, s42, v155\n"
"v_dot2c_f32_bf16 v225, v155, v155\n"
"v_dot2c_f32_bf16 v222, s42, v156\n"
"v_dot2c_f32_bf16 v224, v156, v156\n"
"v_dot2c_f32_bf16 v223, s42, v157\n"
"v_dot2c_f32_bf16 v225, v157, v157\n"
"v_dot2c_f32_bf16 v222, s42, v158\n"
"v_dot2c_f32_bf16 v224, v158, v158\n"
"v_dot2c_f32_bf16 v223, s42, v159\n"
"v_dot2c_f32_bf16 v225, v159, v159\n"
"v_dot2c_f32_bf16 v222, s42, v160\n"
"v_dot2c_f32_bf16 v224, v160, v160\n"
"v_dot2c_f32_bf16 v223, s42, v161\n"
"v_dot2c_f32_bf16 v225, v161, v161\n"
"v_dot2c_f32_bf16 v222, s42, v162\n"
"v_dot2c_f32_bf16 v224, v162, v162\n"
"v_dot2c_f32_bf16 v223, s42, v163\n"
"v_dot2c_f32_bf16 v225, v163, v163\n"
"v_dot2c_f32_bf16 v222, s42, v176\n"
"v_dot2c_f32_bf16 v224, v176, v176\n"
"v_dot2c_f32_bf16 v223, s42, v177\n"
"v_dot2c_f32_bf16 v225, v177, v177\n"
"v_dot2c_f32_bf16 v222, s42, v178\n"
"v_dot2c_f32_bf16 v224, v178, v178\n"
"v_dot2c_f32_bf16 v223, s42, v179\n"
"v_dot2c_f32_bf16 v225, v179, v179\n"
"v_dot2c_f32_bf16 v222, s42, v180\n"
"v_dot2c_f32_bf16 v224, v180, v180\n"
"v_dot2c_f32_bf16 v223, s42, v181\n"
"v_dot2c_f32_bf16 v225, v181, v181\n"
"v_dot2c_f32_bf16 v222, s42, v182\n"
"v_dot2c_f32_bf16 v224, v182, v182\n"
"v_dot2c_f32_bf16 v223, s42, v183\n"
"v_dot2c_f32_bf16 v225, v183, v183\n"
"v_dot2c_f32_bf16 v222, s42, v184\n"
"v_dot2c_f32_bf16 v224, v184, v184\n"
"v_dot2c_f32_bf16 v223, s42, v185\n"
"v_dot2c_f32_bf16 v225, v185, v185\n"
"v_dot2c_f32_bf16 v222, s42, v186\n"
"v_dot2c_f32_bf16 v224, v186, v186\n"
"v_dot2c_f32_bf16 v223, s42, v187\n"
"v_dot2c_f32_bf16 v225, v187, v187\n"
"v_dot2c_f32_bf16 v222, s42, v188\n"
"v_dot2c_f32_bf16 v224, v188, v188\n"
"v_dot2c_f32_bf16 v223, s42, v189\n"
"v_dot2c_f32_bf16 v225, v189, v189\n"
"v_dot2c_f32_bf16 v222, s42, v190\n"
"v_dot2c_f32_bf16 v224, v190, v190\n"
"v_dot2c_f32_bf16 v223, s42, v191\n"
"v_dot2c_f32_bf16 v225, v191, v191\n"
"s_nop 2\n"
"v_add_f32 v222, v222, v223\n"
"v_add_f32 v224, v224, v225\n"
"s_nop 1\n"
"s_nop 0\n"
"v_add_f32_dpp v222, v222, v222 quad_perm:[1,0,3,2] row_mask:0xf bank_mask:0xf\n"
"v_add_f32_dpp v224, v224, v224 quad_perm:[1,0,3,2] row_mask:0xf bank_mask:0xf\n"
"s_nop 1\n"
"s_nop 0\n"
"v_add_f32_dpp v222, v222, v222 quad_perm:[2,3,0,1] row_mask:0xf bank_mask:0xf\n"
"v_add_f32_dpp v224, v224, v224 quad_perm:[2,3,0,1] row_mask:0xf bank_mask:0xf\n"
"s_nop 1\n"
"s_nop 0\n"
"v_add_f32_dpp v222, v222, v222 row_half_mirror row_mask:0xf bank_mask:0xf\n"
"v_add_f32_dpp v224, v224, v224 row_half_mirror row_mask:0xf bank_mask:0xf\n"
"v_mul_f32 v222, 0x3ae38e39, v222\n"
"v_mul_f32 v224, 0x3ae38e39, v224\n"
"v_fma_f32 v224, -v222, v222, v224\n"
"v_max_f32_e64 v224, 0, v224\n"
"v_add_f32 v225, 0x3727c5ac, v224\n"
"v_rsq_f32 v225, v225\n"
"v_mov_b32 v226, v222\n"
"v_mov_b32 v223, 0x2edbe6ff\n"
"v_fmac_f32 v223, 0x3f800054, v224\n"
"v_mov_b32 v227, v225\n"
"v_rsq_f32 v223, v223\n"
"ds_write_b64 v228, v[226:227] offset:51200\n"
"v_mul_f32 v223, 0x3eeff558, v223\n"
"v_cvt_pk_bf16_f32 v251, v223, v223\n"
"v_and_b32 v250, 0xffff, v251\n"
"v_and_b32 v251, s41, v251\n"
"v_mul_f32 v252, v222, v251\n"
"v_sub_f32_e64 v252, 0, v252\n"
"v_dot2_f32_bf16 v236, v144, v250, v252\n"
"v_dot2_f32_bf16 v237, v144, v251, v252\n"
"v_dot2_f32_bf16 v238, v145, v250, v252\n"
"v_dot2_f32_bf16 v239, v145, v251, v252\n"
"s_nop 1\n"
"s_nop 0\n"
"v_cvt_pk_bf16_f32 v144, v236, v237\n"
"v_cvt_pk_bf16_f32 v145, v238, v239\n"
"v_dot2_f32_bf16 v236, v146, v250, v252\n"
"v_dot2_f32_bf16 v237, v146, v251, v252\n"
"v_dot2_f32_bf16 v238, v147, v250, v252\n"
"v_dot2_f32_bf16 v239, v147, v251, v252\n"
"s_nop 1\n"
"s_nop 0\n"
"v_cvt_pk_bf16_f32 v146, v236, v237\n"
"v_cvt_pk_bf16_f32 v147, v238, v239\n"
"ds_write_b128 v249, v[144:147] offset:0\n"
"v_dot2_f32_bf16 v236, v148, v250, v252\n"
"v_dot2_f32_bf16 v237, v148, v251, v252\n"
"v_dot2_f32_bf16 v238, v149, v250, v252\n"
"v_dot2_f32_bf16 v239, v149, v251, v252\n"
"s_nop 1\n"
"s_nop 0\n"
"v_cvt_pk_bf16_f32 v148, v236, v237\n"
"v_cvt_pk_bf16_f32 v149, v238, v239\n"
"v_dot2_f32_bf16 v236, v150, v250, v252\n"
"v_dot2_f32_bf16 v237, v150, v251, v252\n"
"v_dot2_f32_bf16 v238, v151, v250, v252\n"
"v_dot2_f32_bf16 v239, v151, v251, v252\n"
"s_nop 1\n"
"s_nop 0\n"
"v_cvt_pk_bf16_f32 v150, v236, v237\n"
"v_cvt_pk_bf16_f32 v151, v238, v239\n"
"ds_write_b128 v249, v[148:151] offset:4096\n"
"v_dot2_f32_bf16 v236, v152, v250, v252\n"
"v_dot2_f32_bf16 v237, v152, v251, v252\n"
"v_dot2_f32_bf16 v238, v153, v250, v252\n"
"v_dot2_f32_bf16 v239, v153, v251, v252\n"
"s_nop 1\n"
"s_nop 0\n"
"v_cvt_pk_bf16_f32 v152, v236, v237\n"
"v_cvt_pk_bf16_f32 v153, v238, v239\n"
"v_dot2_f32_bf16 v236, v154, v250, v252\n"
"v_dot2_f32_bf16 v237, v154, v251, v252\n"
"v_dot2_f32_bf16 v238, v155, v250, v252\n"
"v_dot2_f32_bf16 v239, v155, v251, v252\n"
"s_nop 1\n"
"s_nop 0\n"
"v_cvt_pk_bf16_f32 v154, v236, v237\n"
"v_cvt_pk_bf16_f32 v155, v238, v239\n"
"ds_write_b128 v249, v[152:155] offset:8192\n"
"v_dot2_f32_bf16 v236, v156, v250, v252\n"
"v_dot2_f32_bf16 v237, v156, v251, v252\n"
"v_dot2_f32_bf16 v238, v157, v250, v252\n"
"v_dot2_f32_bf16 v239, v157, v251, v252\n"
"s_nop 1\n"
"s_nop 0\n"
"v_cvt_pk_bf16_f32 v156, v236, v237\n"
"v_cvt_pk_bf16_f32 v157, v238, v239\n"
"v_dot2_f32_bf16 v236, v158, v250, v252\n"
"v_dot2_f32_bf16 v237, v158, v251, v252\n"
"v_dot2_f32_bf16 v238, v159, v250, v252\n"
"v_dot2_f32_bf16 v239, v159, v251, v252\n"
"s_nop 1\n"
"s_nop 0\n"
"v_cvt_pk_bf16_f32 v158, v236, v237\n"
"v_cvt_pk_bf16_f32 v159, v238, v239\n"
"ds_write_b128 v249, v[156:159] offset:12288\n"
"v_dot2_f32_bf16 v236, v160, v250, v252\n"
"v_dot2_f32_bf16 v237, v160, v251, v252\n"
"v_dot2_f32_bf16 v238, v161, v250, v252\n"
"v_dot2_f32_bf16 v239, v161, v251, v252\n"
"s_nop 1\n"
"s_nop 0\n"
"v_cvt_pk_bf16_f32 v160, v236, v237\n"
"v_cvt_pk_bf16_f32 v161, v238, v239\n"
"v_dot2_f32_bf16 v236, v162, v250, v252\n"
"v_dot2_f32_bf16 v237, v162, v251, v252\n"
"v_dot2_f32_bf16 v238, v163, v250, v252\n"
"v_dot2_f32_bf16 v239, v163, v251, v252\n"
"s_nop 1\n"
"s_nop 0\n"
"v_cvt_pk_bf16_f32 v162, v236, v237\n"
"v_cvt_pk_bf16_f32 v163, v238, v239\n"
"ds_write_b128 v249, v[160:163] offset:16384\n"
"s_waitcnt vmcnt(0)\n"
"v_mov_b32 v222, 0\n"
"v_mov_b32 v223, 0\n"
"v_mov_b32 v224, 0\n"
"v_mov_b32 v225, 0\n"
"v_dot2c_f32_bf16 v222, s42, v164\n"
"v_dot2c_f32_bf16 v224, v164, v164\n"
"v_dot2c_f32_bf16 v223, s42, v165\n"
"v_dot2c_f32_bf16 v225, v165, v165\n"
"v_dot2c_f32_bf16 v222, s42, v166\n"
"v_dot2c_f32_bf16 v224, v166, v166\n"
"v_dot2c_f32_bf16 v223, s42, v167\n"
"v_dot2c_f32_bf16 v225, v167, v167\n"
"s_mov_b32 m0, s43\n"
"s_nop 0\n"
"s_nop 0\n"
"buffer_load_dwordx4 v254, s[16:19], s36 offen lds\n"
"v_dot2c_f32_bf16 v222, s42, v168\n"
"v_dot2c_f32_bf16 v224, v168, v168\n"
"v_dot2c_f32_bf16 v223, s42, v169\n"
"v_dot2c_f32_bf16 v225, v169, v169\n"
"v_dot2c_f32_bf16 v222, s42, v170\n"
"v_dot2c_f32_bf16 v224, v170, v170\n"
"v_dot2c_f32_bf16 v223, s42, v171\n"
"v_dot2c_f32_bf16 v225, v171, v171\n"
"buffer_load_dwordx4 v255, s[16:19], s36 offen offset:1024 lds\n"
"s_add_u32 s36, s36, s37\n"
"v_dot2c_f32_bf16 v222, s42, v172\n"
"v_dot2c_f32_bf16 v224, v172, v172\n"
"v_dot2c_f32_bf16 v223, s42, v173\n"
"v_dot2c_f32_bf16 v225, v173, v173\n"
"v_dot2c_f32_bf16 v222, s42, v174\n"
"v_dot2c_f32_bf16 v224, v174, v174\n"
"v_dot2c_f32_bf16 v223, s42, v175\n"
"v_dot2c_f32_bf16 v225, v175, v175\n"
"s_nop 0\n"
"s_add_u32 m0, s43, 16384\n"
"s_nop 0\n"
"s_nop 0\n"
"buffer_load_dwordx4 v254, s[16:19], s36 offen lds\n"
"v_dot2c_f32_bf16 v222, s42, v208\n"
"v_dot2c_f32_bf16 v224, v208, v208\n"
"v_dot2c_f32_bf16 v223, s42, v209\n"
"v_dot2c_f32_bf16 v225, v209, v209\n"
"v_dot2c_f32_bf16 v222, s42, v210\n"
"v_dot2c_f32_bf16 v224, v210, v210\n"
"v_dot2c_f32_bf16 v223, s42, v211\n"
"v_dot2c_f32_bf16 v225, v211, v211\n"
"buffer_load_dwordx4 v255, s[16:19], s36 offen offset:1024 lds\n"
"s_add_u32 s36, s36, s37\n"
"v_dot2c_f32_bf16 v222, s42, v212\n"
"v_dot2c_f32_bf16 v224, v212, v212\n"
"v_dot2c_f32_bf16 v223, s42, v213\n"
"v_dot2c_f32_bf16 v225, v213, v213\n"
"v_dot2c_f32_bf16 v222, s42, v214\n"
"v_dot2c_f32_bf16 v224, v214, v214\n"
"v_dot2c_f32_bf16 v223, s42, v215\n"
"v_dot2c_f32_bf16 v225, v215, v215\n"
"s_nop 0\n"
"s_add_u32 m0, s43, 32768\n"
"s_nop 0\n"
"s_nop 0\n"
"buffer_load_dwordx4 v254, s[16:19], s36 offen lds\n"
"v_dot2c_f32_bf16 v222, s42, v192\n"
"v_dot2c_f32_bf16 v224, v192, v192\n"
"v_dot2c_f32_bf16 v223, s42, v193\n"
"v_dot2c_f32_bf16 v225, v193, v193\n"
"v_dot2c_f32_bf16 v222, s42, v194\n"
"v_dot2c_f32_bf16 v224, v194, v194\n"
"v_dot2c_f32_bf16 v223, s42, v195\n"
"v_dot2c_f32_bf16 v225, v195, v195\n"
"buffer_load_dwordx4 v255, s[16:19], s36 offen offset:1024 lds\n"
"s_add_u32 s36, s36, s37\n"
"v_dot2c_f32_bf16 v222, s42, v196\n"
"v_dot2c_f32_bf16 v224, v196, v196\n"
"v_dot2c_f32_bf16 v223, s42, v197\n"
"v_dot2c_f32_bf16 v225, v197, v197\n"
"v_dot2c_f32_bf16 v222, s42, v198\n"
"v_dot2c_f32_bf16 v224, v198, v198\n"
"v_dot2c_f32_bf16 v223, s42, v199\n"
"v_dot2c_f32_bf16 v225, v199, v199\n"
"v_dot2c_f32_bf16 v222, s42, v200\n"
"v_dot2c_f32_bf16 v224, v200, v200\n"
"v_dot2c_f32_bf16 v223, s42, v201\n"
"v_dot2c_f32_bf16 v225, v201, v201\n"
"v_dot2c_f32_bf16 v222, s42, v202\n"
"v_dot2c_f32_bf16 v224, v202, v202\n"
"v_dot2c_f32_bf16 v223, s42, v203\n"
"v_dot2c_f32_bf16 v225, v203, v203\n"
"v_dot2c_f32_bf16 v222, s42, v204\n"
"v_dot2c_f32_bf16 v224, v204, v204\n"
"v_dot2c_f32_bf16 v223, s42, v205\n"
"v_dot2c_f32_bf16 v225, v205, v205\n"
"v_dot2c_f32_bf16 v222, s42, v206\n"
"v_dot2c_f32_bf16 v224, v206, v206\n"
"v_dot2c_f32_bf16 v223, s42, v207\n"
"v_dot2c_f32_bf16 v225, v207, v207\n"
"s_nop 2\n"
"v_add_f32 v222, v222, v223\n"
"v_add_f32 v224, v224, v225\n"
"s_nop 1\n"
"s_nop 0\n"
"v_add_f32_dpp v222, v222, v222 quad_perm:[1,0,3,2] row_mask:0xf bank_mask:0xf\n"
"v_add_f32_dpp v224, v224, v224 quad_perm:[1,0,3,2] row_mask:0xf bank_mask:0xf\n"
"s_nop 1\n"
"s_nop 0\n"
"v_add_f32_dpp v222, v222, v222 quad_perm:[2,3,0,1] row_mask:0xf bank_mask:0xf\n"
"v_add_f32_dpp v224, v224, v224 quad_perm:[2,3,0,1] row_mask:0xf bank_mask:0xf\n"
"s_nop 1\n"
"s_nop 0\n"
"v_add_f32_dpp v222, v222, v222 row_half_mirror row_mask:0xf bank_mask:0xf\n"
"v_add_f32_dpp v224, v224, v224 row_half_mirror row_mask:0xf bank_mask:0xf\n"
"v_mul_f32 v222, 0x3ae38e39, v222\n"
"v_mul_f32 v224, 0x3ae38e39, v224\n"
"v_fma_f32 v224, -v222, v222, v224\n"
"v_max_f32_e64 v224, 0, v224\n"
"v_add_f32 v225, 0x3727c5ac, v224\n"
"v_rsq_f32 v225, v225\n"
"v_mov_b32 v226, v222\n"
"v_mov_b32 v223, 0x2edbe6ff\n"
"v_fmac_f32 v223, 0x3f800054, v224\n"
"v_mov_b32 v227, v225\n"
"v_rsq_f32 v223, v223\n"
"ds_write_b64 v228, v[226:227] offset:51264\n"
"v_mul_f32 v223, 0x3eeff558, v223\n"
"v_cvt_pk_bf16_f32 v240, v223, v223\n"
"v_and_b32 v253, 0xffff, v240\n"
"v_and_b32 v240, s41, v240\n"
"v_mul_f32 v241, v222, v240\n"
"v_sub_f32_e64 v241, 0, v241\n"
"v_dot2_f32_bf16 v236, v164, v253, v241\n"
"v_dot2_f32_bf16 v237, v164, v240, v241\n"
"v_dot2_f32_bf16 v238, v165, v253, v241\n"
"v_dot2_f32_bf16 v239, v165, v240, v241\n"
"s_nop 1\n"
"s_nop 0\n"
"v_cvt_pk_bf16_f32 v164, v236, v237\n"
"v_cvt_pk_bf16_f32 v165, v238, v239\n"
"v_dot2_f32_bf16 v236, v166, v253, v241\n"
"v_dot2_f32_bf16 v237, v166, v240, v241\n"
"v_dot2_f32_bf16 v238, v167, v253, v241\n"
"v_dot2_f32_bf16 v239, v167, v240, v241\n"
"s_nop 1\n"
"s_nop 0\n"
"v_cvt_pk_bf16_f32 v166, v236, v237\n"
"v_cvt_pk_bf16_f32 v167, v238, v239\n"
"ds_write_b128 v249, v[164:167] offset:128\n"
"v_dot2_f32_bf16 v236, v168, v253, v241\n"
"v_dot2_f32_bf16 v237, v168, v240, v241\n"
"v_dot2_f32_bf16 v238, v169, v253, v241\n"
"v_dot2_f32_bf16 v239, v169, v240, v241\n"
"s_nop 1\n"
"s_nop 0\n"
"v_cvt_pk_bf16_f32 v168, v236, v237\n"
"v_cvt_pk_bf16_f32 v169, v238, v239\n"
"v_dot2_f32_bf16 v236, v170, v253, v241\n"
"v_dot2_f32_bf16 v237, v170, v240, v241\n"
"v_dot2_f32_bf16 v238, v171, v253, v241\n"
"v_dot2_f32_bf16 v239, v171, v240, v241\n"
"s_nop 1\n"
"s_nop 0\n"
"v_cvt_pk_bf16_f32 v170, v236, v237\n"
"v_cvt_pk_bf16_f32 v171, v238, v239\n"
"ds_write_b128 v249, v[168:171] offset:4224\n"
"v_dot2_f32_bf16 v236, v172, v253, v241\n"
"v_dot2_f32_bf16 v237, v172, v240, v241\n"
"v_dot2_f32_bf16 v238, v173, v253, v241\n"
"v_dot2_f32_bf16 v239, v173, v240, v241\n"
"s_nop 1\n"
"s_nop 0\n"
"v_cvt_pk_bf16_f32 v172, v236, v237\n"
"v_cvt_pk_bf16_f32 v173, v238, v239\n"
"v_dot2_f32_bf16 v236, v174, v253, v241\n"
"v_dot2_f32_bf16 v237, v174, v240, v241\n"
"v_dot2_f32_bf16 v238, v175, v253, v241\n"
"v_dot2_f32_bf16 v239, v175, v240, v241\n"
"s_nop 1\n"
"s_nop 0\n"
"v_cvt_pk_bf16_f32 v174, v236, v237\n"
"v_cvt_pk_bf16_f32 v175, v238, v239\n"
"ds_write_b128 v249, v[172:175] offset:8320\n"
"v_dot2_f32_bf16 v236, v208, v253, v241\n"
"v_dot2_f32_bf16 v237, v208, v240, v241\n"
"v_dot2_f32_bf16 v238, v209, v253, v241\n"
"v_dot2_f32_bf16 v239, v209, v240, v241\n"
"s_nop 1\n"
"s_nop 0\n"
"v_cvt_pk_bf16_f32 v208, v236, v237\n"
"v_cvt_pk_bf16_f32 v209, v238, v239\n"
"v_dot2_f32_bf16 v236, v210, v253, v241\n"
"v_dot2_f32_bf16 v237, v210, v240, v241\n"
"v_dot2_f32_bf16 v238, v211, v253, v241\n"
"v_dot2_f32_bf16 v239, v211, v240, v241\n"
"s_nop 1\n"
"s_nop 0\n"
"v_cvt_pk_bf16_f32 v210, v236, v237\n"
"v_cvt_pk_bf16_f32 v211, v238, v239\n"
"ds_write_b128 v249, v[208:211] offset:12416\n"
"v_dot2_f32_bf16 v236, v212, v253, v241\n"
"v_dot2_f32_bf16 v237, v212, v240, v241\n"
"v_dot2_f32_bf16 v238, v213, v253, v241\n"
"v_dot2_f32_bf16 v239, v213, v240, v241\n"
"s_nop 1\n"
"s_nop 0\n"
"v_cvt_pk_bf16_f32 v212, v236, v237\n"
"v_cvt_pk_bf16_f32 v213, v238, v239\n"
"v_dot2_f32_bf16 v236, v214, v253, v241\n"
"v_dot2_f32_bf16 v237, v214, v240, v241\n"
"v_dot2_f32_bf16 v238, v215, v253, v241\n"
"v_dot2_f32_bf16 v239, v215, v240, v241\n"
"s_nop 1\n"
"s_nop 0\n"
"v_cvt_pk_bf16_f32 v214, v236, v237\n"
"v_cvt_pk_bf16_f32 v215, v238, v239\n"
"ds_write_b128 v249, v[212:215] offset:16512\n"
"v_lshlrev_b32 v236, 16, v221\n"
"v_and_b32 v237, s41, v221\n"
"v_mul_f32 v236, 0x3eeff558, v236\n"
"v_mul_f32 v237, 0x3eeff558, v237\n"
"v_lshlrev_b32_e64 v219, 3, v219\n"
"ds_write_b64 v219, v[236:237] offset:49152\n"
"s_and_b32 s76, s34, 7\n"
"s_lshl_b32 s76, s76, 3\n"
"s_add_u32 s76, s76, s11\n"
"s_mul_i32 s76, s76, 54\n"
"v_min_u32 v134, 53, v248\n"
"v_add_u32 v134, s76, v134\n"
"v_mul_u32_u24 v135, 43691, v134\n"
"v_lshrrev_b32_e64 v135, 18, v135\n"
"v_mul_u32_u24 v133, 8448, v135\n"
"v_lshl_add_u32 v133, v134, 7, v133\n"
"s_mul_i32 s77, s35, 768\n"
"v_add_u32_e64 v133, s77, v133\n"
"global_load_dword v136, v133, s[62:63]\n"
"s_mul_i32 s77, s35, 442368\n"
"v_lshlrev_b32 v133, 7, v134\n"
"v_add_u32 v133, s77, v133\n"
"global_load_dword v137, v133, s[66:67]\n"
"s_waitcnt vmcnt(6) lgkmcnt(0)\n"
"s_barrier\n"
"s_add_u32 s4, s7, 1\n"
"s_nop 0\n"
"s_xor_b32 s5, s4, 0xa5a5a5a5\n"
"ds_read_b64_tr_b16 v[208:209], v244 offset:0\n"
"ds_read_b64_tr_b16 v[210:211], v244 offset:512\n"
"ds_read_b64_tr_b16 v[212:213], v245 offset:0\n"
"ds_read_b64_tr_b16 v[214:215], v245 offset:512\n"
"ds_read_b64_tr_b16 v[216:217], v244 offset:2048\n"
"ds_read_b64_tr_b16 v[218:219], v244 offset:2560\n"
"ds_read_b64_tr_b16 v[220:221], v245 offset:2048\n"
"ds_read_b64_tr_b16 v[222:223], v245 offset:2560\n"
"ds_read_b128 v[144:147], v247 offset:0\n"
"ds_read_b128 v[148:151], v247 offset:32\n"
"ds_read_b128 v[152:155], v247 offset:64\n"
"ds_read_b128 v[156:159], v247 offset:96\n"
"ds_read_b128 v[160:163], v247 offset:128\n"
"ds_read_b128 v[164:167], v247 offset:160\n"
"ds_read_b128 v[168:171], v247 offset:192\n"
"ds_read_b128 v[172:175], v247 offset:224\n"
"v_add_u32 v247, 256, v247\n"
"ds_read_b128 v[0:3], v232 offset:0\n"
"ds_read_b128 v[4:7], v233 offset:1024\n"
"ds_read_b128 v[8:11], v234 offset:2048\n"
"ds_read_b128 v[12:15], v235 offset:3072\n"
"ds_read_b128 v[16:19], v232 offset:4096\n"
"ds_read_b128 v[20:23], v233 offset:5120\n"
"ds_read_b128 v[24:27], v234 offset:6144\n"
"ds_read_b128 v[28:31], v235 offset:7168\n"
"ds_read_b128 v[32:35], v232 offset:8192\n"
"ds_read_b128 v[36:39], v233 offset:9216\n"
"ds_read_b128 v[40:43], v234 offset:10240\n"
"ds_read_b128 v[44:47], v235 offset:11264\n"
"ds_read_b128 v[48:51], v232 offset:12288\n"
"ds_read_b128 v[52:55], v233 offset:13312\n"
"ds_read_b128 v[56:59], v234 offset:14336\n"
"ds_read_b128 v[60:63], v235 offset:15360\n"
"ds_read_b128 v[64:67], v232 offset:16384\n"
"ds_read_b128 v[68:71], v233 offset:17408\n"
"ds_read_b128 v[72:75], v234 offset:18432\n"
"ds_read_b128 v[76:79], v235 offset:19456\n"
".p2align 3\n"
"s_waitcnt lgkmcnt(15)\n"
"v_lshlrev_b32 v236, 16, v176\n"
"v_mfma_f32_32x32x16_bf16 v[144:159], v[208:211], v[0:3], v[144:159]\n"
"ds_read_b64_tr_b16 v[224:225], v244 offset:4096\n"
"ds_read_b64_tr_b16 v[226:227], v244 offset:4608\n"
"ds_read_b64_tr_b16 v[228:229], v245 offset:4096\n"
"ds_read_b64_tr_b16 v[230:231], v245 offset:4608\n"
"v_and_b32 v237, 0xffff0000, v176\n"
"v_lshlrev_b32_e64 v238, 16, v177\n"
"v_and_b32 v239, 0xffff0000, v177\n"
"v_mfma_f32_32x32x16_bf16 v[160:175], v[212:215], v[0:3], v[160:175]\n"
"v_fma_f32 v236, v236, v251, v252\n"
"v_fma_f32 v237, v237, v251, v252\n"
"v_fma_f32 v238, v238, v251, v252\n"
"v_fma_f32 v239, v239, v251, v252\n"
"v_mfma_f32_32x32x16_bf16 v[144:159], v[216:219], v[4:7], v[144:159]\n"
"ds_read_b64_tr_b16 v[208:209], v244 offset:6144\n"
"ds_read_b64_tr_b16 v[210:211], v244 offset:6656\n"
"ds_read_b64_tr_b16 v[212:213], v245 offset:6144\n"
"ds_read_b64_tr_b16 v[214:215], v245 offset:6656\n"
"v_cvt_pk_bf16_f32 v176, v236, v237\n"
"v_cvt_pk_bf16_f32 v177, v238, v239\n"
"v_lshlrev_b32_e64 v236, 16, v178\n"
"v_and_b32 v237, 0xffff0000, v178\n"
"v_mfma_f32_32x32x16_bf16 v[160:175], v[220:223], v[4:7], v[160:175]\n"
"v_lshlrev_b32_e64 v238, 16, v179\n"
"v_and_b32 v239, 0xffff0000, v179\n"
"v_fma_f32 v236, v236, v251, v252\n"
"v_fma_f32 v237, v237, v251, v252\n"
"s_waitcnt vmcnt(4) lgkmcnt(0)\n"
"s_barrier\n"
"v_mfma_f32_32x32x16_bf16 v[144:159], v[224:227], v[8:11], v[144:159]\n"
"ds_read_b64_tr_b16 v[216:217], v244 offset:16384\n"
"ds_read_b64_tr_b16 v[218:219], v244 offset:16896\n"
"ds_read_b64_tr_b16 v[220:221], v245 offset:16384\n"
"ds_read_b64_tr_b16 v[222:223], v245 offset:16896\n"
"v_fma_f32 v238, v238, v251, v252\n"
"v_fma_f32 v239, v239, v251, v252\n"
"v_cvt_pk_bf16_f32 v178, v236, v237\n"
"v_cvt_pk_bf16_f32 v179, v238, v239\n"
"ds_write_b128 v249, v[176:179] offset:0\n"
"v_mfma_f32_32x32x16_bf16 v[160:175], v[228:231], v[8:11], v[160:175]\n"
"v_lshlrev_b32_e64 v236, 16, v192\n"
"v_and_b32 v237, 0xffff0000, v192\n"
"v_lshlrev_b32_e64 v238, 16, v193\n"
"v_and_b32 v239, 0xffff0000, v193\n"
"v_mfma_f32_32x32x16_bf16 v[144:159], v[208:211], v[12:15], v[144:159]\n"
"ds_read_b64_tr_b16 v[224:225], v244 offset:18432\n"
"ds_read_b64_tr_b16 v[226:227], v244 offset:18944\n"
"ds_read_b64_tr_b16 v[228:229], v245 offset:18432\n"
"ds_read_b64_tr_b16 v[230:231], v245 offset:18944\n"
"s_mov_b32 m0, s43\n"
"s_nop 0\n"
"v_fma_f32 v236, v236, v240, v241\n"
"v_fma_f32 v237, v237, v240, v241\n"
"v_fma_f32 v238, v238, v240, v241\n"
"v_fma_f32 v239, v239, v240, v241\n"
"v_mfma_f32_32x32x16_bf16 v[160:175], v[212:215], v[12:15], v[160:175]\n"
"buffer_load_dwordx4 v254, s[16:19], s36 offen lds\n"
"v_cvt_pk_bf16_f32 v192, v236, v237\n"
"v_cvt_pk_bf16_f32 v193, v238, v239\n"
"v_lshlrev_b32_e64 v236, 16, v194\n"
"v_and_b32 v237, 0xffff0000, v194\n"
"s_waitcnt lgkmcnt(5)\n"
"v_lshlrev_b32 v238, 16, v195\n"
"v_mfma_f32_32x32x16_bf16 v[144:159], v[216:219], v[16:19], v[144:159]\n"
"ds_read_b64_tr_b16 v[208:209], v244 offset:20480\n"
"ds_read_b64_tr_b16 v[210:211], v244 offset:20992\n"
"ds_read_b64_tr_b16 v[212:213], v245 offset:20480\n"
"ds_read_b64_tr_b16 v[214:215], v245 offset:20992\n"
"v_and_b32 v239, 0xffff0000, v195\n"
"v_fma_f32 v236, v236, v240, v241\n"
"v_fma_f32 v237, v237, v240, v241\n"
"v_mfma_f32_32x32x16_bf16 v[160:175], v[220:223], v[16:19], v[160:175]\n"
"v_fma_f32 v238, v238, v240, v241\n"
"v_fma_f32 v239, v239, v240, v241\n"
"v_cvt_pk_bf16_f32 v194, v236, v237\n"
"v_cvt_pk_bf16_f32 v195, v238, v239\n"
"ds_write_b128 v249, v[192:195] offset:128\n"
"s_waitcnt lgkmcnt(5)\n"
"v_lshlrev_b32 v236, 16, v180\n"
"v_mfma_f32_32x32x16_bf16 v[144:159], v[224:227], v[20:23], v[144:159]\n"
"ds_read_b64_tr_b16 v[216:217], v244 offset:22528\n"
"ds_read_b64_tr_b16 v[218:219], v244 offset:23040\n"
"ds_read_b64_tr_b16 v[220:221], v245 offset:22528\n"
"ds_read_b64_tr_b16 v[222:223], v245 offset:23040\n"
"v_and_b32 v237, 0xffff0000, v180\n"
"v_lshlrev_b32_e64 v238, 16, v181\n"
"v_and_b32 v239, 0xffff0000, v181\n"
"v_mfma_f32_32x32x16_bf16 v[160:175], v[228:231], v[20:23], v[160:175]\n"
"buffer_load_dwordx4 v255, s[16:19], s36 offen offset:1024 lds\n"
"s_add_u32 s36, s36, s37\n"
"s_nop 0\n"
"v_fma_f32 v236, v236, v251, v252\n"
"v_fma_f32 v237, v237, v251, v252\n"
"v_fma_f32 v238, v238, v251, v252\n"
"v_fma_f32 v239, v239, v251, v252\n"
"s_waitcnt vmcnt(4) lgkmcnt(0)\n"
"s_barrier\n"
"v_mfma_f32_32x32x16_bf16 v[144:159], v[208:211], v[24:27], v[144:159]\n"
"ds_read_b64_tr_b16 v[224:225], v244 offset:32768\n"
"ds_read_b64_tr_b16 v[226:227], v244 offset:33280\n"
"ds_read_b64_tr_b16 v[228:229], v245 offset:32768\n"
"ds_read_b64_tr_b16 v[230:231], v245 offset:33280\n"
"v_cvt_pk_bf16_f32 v180, v236, v237\n"
"v_cvt_pk_bf16_f32 v181, v238, v239\n"
"v_lshlrev_b32_e64 v236, 16, v182\n"
"v_and_b32 v237, 0xffff0000, v182\n"
"v_mfma_f32_32x32x16_bf16 v[160:175], v[212:215], v[24:27], v[160:175]\n"
"v_lshlrev_b32_e64 v238, 16, v183\n"
"v_and_b32 v239, 0xffff0000, v183\n"
"v_fma_f32 v236, v236, v251, v252\n"
"v_fma_f32 v237, v237, v251, v252\n"
"v_mfma_f32_32x32x16_bf16 v[144:159], v[216:219], v[28:31], v[144:159]\n"
"ds_read_b64_tr_b16 v[208:209], v244 offset:34816\n"
"ds_read_b64_tr_b16 v[210:211], v244 offset:35328\n"
"ds_read_b64_tr_b16 v[212:213], v245 offset:34816\n"
"ds_read_b64_tr_b16 v[214:215], v245 offset:35328\n"
"s_add_u32 m0, s43, 16384\n"
"v_fma_f32 v238, v238, v251, v252\n"
"v_fma_f32 v239, v239, v251, v252\n"
"v_cvt_pk_bf16_f32 v182, v236, v237\n"
"v_cvt_pk_bf16_f32 v183, v238, v239\n"
"ds_write_b128 v249, v[180:183] offset:4096\n"
"v_mfma_f32_32x32x16_bf16 v[160:175], v[220:223], v[28:31], v[160:175]\n"
"buffer_load_dwordx4 v254, s[16:19], s36 offen lds\n"
"v_lshlrev_b32_e64 v236, 16, v196\n"
"v_and_b32 v237, 0xffff0000, v196\n"
"v_lshlrev_b32_e64 v238, 16, v197\n"
"v_and_b32 v239, 0xffff0000, v197\n"
"s_waitcnt lgkmcnt(5)\n"
"s_nop 0\n"
"v_mfma_f32_32x32x16_bf16 v[144:159], v[224:227], v[32:35], v[144:159]\n"
"ds_read_b64_tr_b16 v[216:217], v244 offset:36864\n"
"ds_read_b64_tr_b16 v[218:219], v244 offset:37376\n"
"ds_read_b64_tr_b16 v[220:221], v245 offset:36864\n"
"ds_read_b64_tr_b16 v[222:223], v245 offset:37376\n"
"v_fma_f32 v236, v236, v240, v241\n"
"v_fma_f32 v237, v237, v240, v241\n"
"v_fma_f32 v238, v238, v240, v241\n"
"v_fma_f32 v239, v239, v240, v241\n"
"v_mfma_f32_32x32x16_bf16 v[160:175], v[228:231], v[32:35], v[160:175]\n"
"v_cvt_pk_bf16_f32 v196, v236, v237\n"
"v_cvt_pk_bf16_f32 v197, v238, v239\n"
"v_lshlrev_b32_e64 v236, 16, v198\n"
"v_and_b32 v237, 0xffff0000, v198\n"
"s_waitcnt lgkmcnt(5)\n"
"v_lshlrev_b32 v238, 16, v199\n"
"v_mfma_f32_32x32x16_bf16 v[144:159], v[208:211], v[36:39], v[144:159]\n"
"ds_read_b64_tr_b16 v[224:225], v244 offset:38912\n"
"ds_read_b64_tr_b16 v[226:227], v244 offset:39424\n"
"ds_read_b64_tr_b16 v[228:229], v245 offset:38912\n"
"ds_read_b64_tr_b16 v[230:231], v245 offset:39424\n"
"v_and_b32 v239, 0xffff0000, v199\n"
"v_fma_f32 v236, v236, v240, v241\n"
"v_fma_f32 v237, v237, v240, v241\n"
"v_mfma_f32_32x32x16_bf16 v[160:175], v[212:215], v[36:39], v[160:175]\n"
"buffer_load_dwordx4 v255, s[16:19], s36 offen offset:1024 lds\n"
"s_add_u32 s36, s36, s37\n"
"s_nop 0\n"
"v_fma_f32 v238, v238, v240, v241\n"
"v_fma_f32 v239, v239, v240, v241\n"
"v_cvt_pk_bf16_f32 v198, v236, v237\n"
"v_cvt_pk_bf16_f32 v199, v238, v239\n"
"ds_write_b128 v249, v[196:199] offset:4224\n"
"s_waitcnt vmcnt(2) lgkmcnt(0)\n"
"s_barrier\n"
"v_mfma_f32_32x32x16_bf16 v[144:159], v[216:219], v[40:43], v[144:159]\n"
"ds_read_b64_tr_b16 v[208:209], v244 offset:0\n"
"ds_read_b64_tr_b16 v[210:211], v244 offset:512\n"
"ds_read_b64_tr_b16 v[212:213], v245 offset:0\n"
"ds_read_b64_tr_b16 v[214:215], v245 offset:512\n"
"v_lshlrev_b32_e64 v236, 16, v184\n"
"v_and_b32 v237, 0xffff0000, v184\n"
"v_lshlrev_b32_e64 v238, 16, v185\n"
"v_and_b32 v239, 0xffff0000, v185\n"
"v_mfma_f32_32x32x16_bf16 v[160:175], v[220:223], v[40:43], v[160:175]\n"
"v_fma_f32 v236, v236, v251, v252\n"
"v_fma_f32 v237, v237, v251, v252\n"
"v_fma_f32 v238, v238, v251, v252\n"
"v_fma_f32 v239, v239, v251, v252\n"
"v_mfma_f32_32x32x16_bf16 v[144:159], v[224:227], v[44:47], v[144:159]\n"
"ds_read_b64_tr_b16 v[216:217], v244 offset:2048\n"
"ds_read_b64_tr_b16 v[218:219], v244 offset:2560\n"
"ds_read_b64_tr_b16 v[220:221], v245 offset:2048\n"
"ds_read_b64_tr_b16 v[222:223], v245 offset:2560\n"
"s_add_u32 m0, s43, 32768\n"
"v_cvt_pk_bf16_f32 v184, v236, v237\n"
"v_cvt_pk_bf16_f32 v185, v238, v239\n"
"v_lshlrev_b32_e64 v236, 16, v186\n"
"v_and_b32 v237, 0xffff0000, v186\n"
"v_mfma_f32_32x32x16_bf16 v[160:175], v[228:231], v[44:47], v[160:175]\n"
"buffer_load_dwordx4 v254, s[16:19], s36 offen lds\n"
"v_lshlrev_b32_e64 v238, 16, v187\n"
"v_and_b32 v239, 0xffff0000, v187\n"
"v_fma_f32 v236, v236, v251, v252\n"
"v_fma_f32 v237, v237, v251, v252\n"
"s_waitcnt lgkmcnt(4)\n"
"s_nop 0\n"
"v_mfma_f32_32x32x16_bf16 v[144:159], v[208:211], v[48:51], v[144:159]\n"
"ds_read_b64_tr_b16 v[224:225], v244 offset:4096\n"
"ds_read_b64_tr_b16 v[226:227], v244 offset:4608\n"
"ds_read_b64_tr_b16 v[228:229], v245 offset:4096\n"
"ds_read_b64_tr_b16 v[230:231], v245 offset:4608\n"
"v_fma_f32 v238, v238, v251, v252\n"
"v_fma_f32 v239, v239, v251, v252\n"
"v_cvt_pk_bf16_f32 v186, v236, v237\n"
"v_cvt_pk_bf16_f32 v187, v238, v239\n"
"ds_write_b128 v249, v[184:187] offset:8192\n"
"v_mfma_f32_32x32x16_bf16 v[160:175], v[212:215], v[48:51], v[160:175]\n"
"v_lshlrev_b32_e64 v236, 16, v200\n"
"v_and_b32 v237, 0xffff0000, v200\n"
"v_lshlrev_b32_e64 v238, 16, v201\n"
"v_and_b32 v239, 0xffff0000, v201\n"
"s_waitcnt lgkmcnt(5)\n"
"s_nop 0\n"
"v_mfma_f32_32x32x16_bf16 v[144:159], v[216:219], v[52:55], v[144:159]\n"
"ds_read_b64_tr_b16 v[208:209], v244 offset:6144\n"
"ds_read_b64_tr_b16 v[210:211], v244 offset:6656\n"
"ds_read_b64_tr_b16 v[212:213], v245 offset:6144\n"
"ds_read_b64_tr_b16 v[214:215], v245 offset:6656\n"
"v_fma_f32 v236, v236, v240, v241\n"
"v_fma_f32 v237, v237, v240, v241\n"
"v_fma_f32 v238, v238, v240, v241\n"
"v_fma_f32 v239, v239, v240, v241\n"
"v_mfma_f32_32x32x16_bf16 v[160:175], v[220:223], v[52:55], v[160:175]\n"
"buffer_load_dwordx4 v255, s[16:19], s36 offen offset:1024 lds\n"
"s_add_u32 s36, s36, s37\n"
"s_nop 0\n"
"v_cvt_pk_bf16_f32 v200, v236, v237\n"
"v_cvt_pk_bf16_f32 v201, v238, v239\n"
"v_lshlrev_b32_e64 v236, 16, v202\n"
"v_and_b32 v237, 0xffff0000, v202\n"
"s_waitcnt vmcnt(2) lgkmcnt(0)\n"
"s_barrier\n"
"v_mfma_f32_32x32x16_bf16 v[144:159], v[224:227], v[56:59], v[144:159]\n"
"ds_read_b64_tr_b16 v[216:217], v244 offset:16384\n"
"ds_read_b64_tr_b16 v[218:219], v244 offset:16896\n"
"ds_read_b64_tr_b16 v[220:221], v245 offset:16384\n"
"ds_read_b64_tr_b16 v[222:223], v245 offset:16896\n"
"v_lshlrev_b32_e64 v238, 16, v203\n"
"v_and_b32 v239, 0xffff0000, v203\n"
"v_fma_f32 v236, v236, v240, v241\n"
"v_fma_f32 v237, v237, v240, v241\n"
"v_mfma_f32_32x32x16_bf16 v[160:175], v[228:231], v[56:59], v[160:175]\n"
"v_fma_f32 v238, v238, v240, v241\n"
"v_fma_f32 v239, v239, v240, v241\n"
"v_cvt_pk_bf16_f32 v202, v236, v237\n"
"v_cvt_pk_bf16_f32 v203, v238, v239\n"
"ds_write_b128 v249, v[200:203] offset:8320\n"
"v_mfma_f32_32x32x16_bf16 v[144:159], v[208:211], v[60:63], v[144:159]\n"
"ds_read_b64_tr_b16 v[224:225], v244 offset:18432\n"
"ds_read_b64_tr_b16 v[226:227], v244 offset:18944\n"
"ds_read_b64_tr_b16 v[228:229], v245 offset:18432\n"
"ds_read_b64_tr_b16 v[230:231], v245 offset:18944\n"
"s_mov_b32 m0, s43\n"
"v_lshlrev_b32 v236, 16, v188\n"
"v_and_b32 v237, 0xffff0000, v188\n"
"v_lshlrev_b32_e64 v238, 16, v189\n"
"v_and_b32 v239, 0xffff0000, v189\n"
"v_mfma_f32_32x32x16_bf16 v[160:175], v[212:215], v[60:63], v[160:175]\n"
"buffer_load_dwordx4 v254, s[16:19], s36 offen lds\n"
"v_fma_f32 v236, v236, v251, v252\n"
"v_fma_f32 v237, v237, v251, v252\n"
"v_fma_f32 v238, v238, v251, v252\n"
"v_fma_f32 v239, v239, v251, v252\n"
"s_waitcnt lgkmcnt(5)\n"
"s_nop 0\n"
"v_mfma_f32_32x32x16_bf16 v[144:159], v[216:219], v[64:67], v[144:159]\n"
"ds_read_b64_tr_b16 v[208:209], v244 offset:20480\n"
"ds_read_b64_tr_b16 v[210:211], v244 offset:20992\n"
"ds_read_b64_tr_b16 v[212:213], v245 offset:20480\n"
"ds_read_b64_tr_b16 v[214:215], v245 offset:20992\n"
"ds_read_b128 v[80:83], v232 offset:0\n"
"ds_read_b128 v[84:87], v233 offset:1024\n"
"v_cvt_pk_bf16_f32 v188, v236, v237\n"
"v_cvt_pk_bf16_f32 v189, v238, v239\n"
"v_lshlrev_b32_e64 v236, 16, v190\n"
"v_and_b32 v237, 0xffff0000, v190\n"
"v_mfma_f32_32x32x16_bf16 v[160:175], v[220:223], v[64:67], v[160:175]\n"
"v_lshlrev_b32_e64 v238, 16, v191\n"
"v_and_b32 v239, 0xffff0000, v191\n"
"v_fma_f32 v236, v236, v251, v252\n"
"v_fma_f32 v237, v237, v251, v252\n"
"s_waitcnt lgkmcnt(6)\n"
"s_nop 0\n"
"v_mfma_f32_32x32x16_bf16 v[144:159], v[224:227], v[68:71], v[144:159]\n"
"ds_read_b64_tr_b16 v[216:217], v244 offset:22528\n"
"ds_read_b64_tr_b16 v[218:219], v244 offset:23040\n"
"ds_read_b64_tr_b16 v[220:221], v245 offset:22528\n"
"ds_read_b64_tr_b16 v[222:223], v245 offset:23040\n"
"ds_read_b128 v[88:91], v234 offset:2048\n"
"ds_read_b128 v[92:95], v235 offset:3072\n"
"v_fma_f32 v238, v238, v251, v252\n"
"v_fma_f32 v239, v239, v251, v252\n"
"v_cvt_pk_bf16_f32 v190, v236, v237\n"
"v_cvt_pk_bf16_f32 v191, v238, v239\n"
"ds_write_b128 v249, v[188:191] offset:12288\n"
"v_mfma_f32_32x32x16_bf16 v[160:175], v[228:231], v[68:71], v[160:175]\n"
"buffer_load_dwordx4 v255, s[16:19], s36 offen offset:1024 lds\n"
"s_add_u32 s36, s36, s37\n"
"v_lshlrev_b32 v236, 16, v204\n"
"v_and_b32 v237, 0xffff0000, v204\n"
"v_lshlrev_b32_e64 v238, 16, v205\n"
"v_and_b32 v239, 0xffff0000, v205\n"
"s_waitcnt vmcnt(2) lgkmcnt(0)\n"
"s_barrier\n"
"v_mfma_f32_32x32x16_bf16 v[144:159], v[208:211], v[72:75], v[144:159]\n"
"ds_read_b64_tr_b16 v[224:225], v244 offset:32768\n"
"ds_read_b64_tr_b16 v[226:227], v244 offset:33280\n"
"ds_read_b64_tr_b16 v[228:229], v245 offset:32768\n"
"ds_read_b64_tr_b16 v[230:231], v245 offset:33280\n"
"ds_read_b128 v[96:99], v232 offset:4096\n"
"ds_read_b128 v[100:103], v233 offset:5120\n"
"v_fma_f32 v236, v236, v240, v241\n"
"v_fma_f32 v237, v237, v240, v241\n"
"v_fma_f32 v238, v238, v240, v241\n"
"v_fma_f32 v239, v239, v240, v241\n"
"v_mfma_f32_32x32x16_bf16 v[160:175], v[212:215], v[72:75], v[160:175]\n"
"v_cvt_pk_bf16_f32 v204, v236, v237\n"
"v_cvt_pk_bf16_f32 v205, v238, v239\n"
"v_lshlrev_b32_e64 v236, 16, v206\n"
"v_and_b32 v237, 0xffff0000, v206\n"
"v_mfma_f32_32x32x16_bf16 v[144:159], v[216:219], v[76:79], v[144:159]\n"
"ds_read_b64_tr_b16 v[208:209], v244 offset:34816\n"
"ds_read_b64_tr_b16 v[210:211], v244 offset:35328\n"
"ds_read_b64_tr_b16 v[212:213], v245 offset:34816\n"
"ds_read_b64_tr_b16 v[214:215], v245 offset:35328\n"
"ds_read_b128 v[104:107], v234 offset:6144\n"
"ds_read_b128 v[108:111], v235 offset:7168\n"
"s_add_u32 m0, s43, 16384\n"
"v_lshlrev_b32_e64 v238, 16, v207\n"
"v_and_b32 v239, 0xffff0000, v207\n"
"v_fma_f32 v236, v236, v240, v241\n"
"v_fma_f32 v237, v237, v240, v241\n"
"v_mfma_f32_32x32x16_bf16 v[160:175], v[220:223], v[76:79], v[160:175]\n"
"buffer_load_dwordx4 v254, s[16:19], s36 offen lds\n"
"v_fma_f32 v238, v238, v240, v241\n"
"v_fma_f32 v239, v239, v240, v241\n"
"v_cvt_pk_bf16_f32 v206, v236, v237\n"
"v_cvt_pk_bf16_f32 v207, v238, v239\n"
"ds_write_b128 v249, v[204:207] offset:12416\n"
"s_waitcnt lgkmcnt(9)\n"
"s_nop 0\n"
"v_mfma_f32_32x32x16_bf16 v[144:159], v[224:227], v[80:83], v[144:159]\n"
"ds_read_b64_tr_b16 v[216:217], v244 offset:36864\n"
"ds_read_b64_tr_b16 v[218:219], v244 offset:37376\n"
"ds_read_b64_tr_b16 v[220:221], v245 offset:36864\n"
"ds_read_b64_tr_b16 v[222:223], v245 offset:37376\n"
"v_mfma_f32_32x32x16_bf16 v[160:175], v[228:231], v[80:83], v[160:175]\n"
"s_waitcnt lgkmcnt(7)\n"
"s_nop 0\n"
"v_mfma_f32_32x32x16_bf16 v[144:159], v[208:211], v[84:87], v[144:159]\n"
"ds_read_b64_tr_b16 v[224:225], v244 offset:38912\n"
"ds_read_b64_tr_b16 v[226:227], v244 offset:39424\n"
"ds_read_b64_tr_b16 v[228:229], v245 offset:38912\n"
"ds_read_b64_tr_b16 v[230:231], v245 offset:39424\n"
"v_mfma_f32_32x32x16_bf16 v[160:175], v[212:215], v[84:87], v[160:175]\n"
"buffer_load_dwordx4 v255, s[16:19], s36 offen offset:1024 lds\n"
"s_add_u32 s36, s36, s37\n"
"s_waitcnt vmcnt(2) lgkmcnt(0)\n"
"s_barrier\n"
"s_nop 0\n"
"v_mfma_f32_32x32x16_bf16 v[144:159], v[216:219], v[88:91], v[144:159]\n"
"ds_read_b64_tr_b16 v[208:209], v244 offset:0\n"
"ds_read_b64_tr_b16 v[210:211], v244 offset:512\n"
"ds_read_b64_tr_b16 v[212:213], v245 offset:0\n"
"ds_read_b64_tr_b16 v[214:215], v245 offset:512\n"
"v_mfma_f32_32x32x16_bf16 v[160:175], v[220:223], v[88:91], v[160:175]\n"
"v_mfma_f32_32x32x16_bf16 v[144:159], v[224:227], v[92:95], v[144:159]\n"
"ds_read_b64_tr_b16 v[216:217], v244 offset:2048\n"
"ds_read_b64_tr_b16 v[218:219], v244 offset:2560\n"
"ds_read_b64_tr_b16 v[220:221], v245 offset:2048\n"
"ds_read_b64_tr_b16 v[222:223], v245 offset:2560\n"
"s_add_u32 m0, s43, 32768\n"
"v_mfma_f32_32x32x16_bf16 v[160:175], v[228:231], v[92:95], v[160:175]\n"
"buffer_load_dwordx4 v254, s[16:19], s36 offen lds\n"
"s_waitcnt lgkmcnt(4)\n"
"s_nop 0\n"
"v_mfma_f32_32x32x16_bf16 v[144:159], v[208:211], v[96:99], v[144:159]\n"
"ds_read_b64_tr_b16 v[224:225], v244 offset:4096\n"
"ds_read_b64_tr_b16 v[226:227], v244 offset:4608\n"
"ds_read_b64_tr_b16 v[228:229], v245 offset:4096\n"
"ds_read_b64_tr_b16 v[230:231], v245 offset:4608\n"
"ds_read_b128 v[112:115], v232 offset:8192\n"
"ds_read_b128 v[116:119], v233 offset:9216\n"
"v_mfma_f32_32x32x16_bf16 v[160:175], v[212:215], v[96:99], v[160:175]\n"
"s_waitcnt lgkmcnt(6)\n"
"s_nop 0\n"
"v_mfma_f32_32x32x16_bf16 v[144:159], v[216:219], v[100:103], v[144:159]\n"
"ds_read_b64_tr_b16 v[208:209], v244 offset:6144\n"
"ds_read_b64_tr_b16 v[210:211], v244 offset:6656\n"
"ds_read_b64_tr_b16 v[212:213], v245 offset:6144\n"
"ds_read_b64_tr_b16 v[214:215], v245 offset:6656\n"
"ds_read_b128 v[120:123], v234 offset:10240\n"
"ds_read_b128 v[124:127], v235 offset:11264\n"
"v_mfma_f32_32x32x16_bf16 v[160:175], v[220:223], v[100:103], v[160:175]\n"
"buffer_load_dwordx4 v255, s[16:19], s36 offen offset:1024 lds\n"
"s_add_u32 s36, s36, s37\n"
"s_waitcnt vmcnt(2) lgkmcnt(0)\n"
"s_barrier\n"
"s_nop 0\n"
"v_mfma_f32_32x32x16_bf16 v[144:159], v[224:227], v[104:107], v[144:159]\n"
"ds_read_b64_tr_b16 v[216:217], v244 offset:16384\n"
"ds_read_b64_tr_b16 v[218:219], v244 offset:16896\n"
"ds_read_b64_tr_b16 v[220:221], v245 offset:16384\n"
"ds_read_b64_tr_b16 v[222:223], v245 offset:16896\n"
"ds_read_b128 v[128:131], v232 offset:12288\n"
"ds_read_b128 v[132:135], v233 offset:13312\n"
"v_mfma_f32_32x32x16_bf16 v[160:175], v[228:231], v[104:107], v[160:175]\n"
"v_mfma_f32_32x32x16_bf16 v[144:159], v[208:211], v[108:111], v[144:159]\n"
"ds_read_b64_tr_b16 v[224:225], v244 offset:18432\n"
"ds_read_b64_tr_b16 v[226:227], v244 offset:18944\n"
"ds_read_b64_tr_b16 v[228:229], v245 offset:18432\n"
"ds_read_b64_tr_b16 v[230:231], v245 offset:18944\n"
"ds_read_b128 v[136:139], v234 offset:14336\n"
"ds_read_b128 v[140:143], v235 offset:15360\n"
"s_mov_b32 s36, s38\n"
"s_nop 0\n"
"s_add_u32 s38, s38, 128\n"
"s_mov_b32 m0, s43\n"
"s_nop 0\n"
"v_mfma_f32_32x32x16_bf16 v[160:175], v[212:215], v[108:111], v[160:175]\n"
"buffer_load_dwordx4 v254, s[16:19], s36 offen lds\n"
"s_waitcnt lgkmcnt(8)\n"
"s_nop 0\n"
"v_mfma_f32_32x32x16_bf16 v[144:159], v[216:219], v[112:115], v[144:159]\n"
"ds_read_b64_tr_b16 v[208:209], v244 offset:20480\n"
"ds_read_b64_tr_b16 v[210:211], v244 offset:20992\n"
"ds_read_b64_tr_b16 v[212:213], v245 offset:20480\n"
"ds_read_b64_tr_b16 v[214:215], v245 offset:20992\n"
"v_mfma_f32_32x32x16_bf16 v[160:175], v[220:223], v[112:115], v[160:175]\n"
"s_waitcnt lgkmcnt(6)\n"
"s_nop 0\n"
"v_mfma_f32_32x32x16_bf16 v[144:159], v[224:227], v[116:119], v[144:159]\n"
"ds_read_b64_tr_b16 v[216:217], v244 offset:22528\n"
"ds_read_b64_tr_b16 v[218:219], v244 offset:23040\n"
"ds_read_b64_tr_b16 v[220:221], v245 offset:22528\n"
"ds_read_b64_tr_b16 v[222:223], v245 offset:23040\n"
"v_mfma_f32_32x32x16_bf16 v[160:175], v[228:231], v[116:119], v[160:175]\n"
"buffer_load_dwordx4 v255, s[16:19], s36 offen offset:1024 lds\n"
"s_add_u32 s36, s36, s37\n"
"s_waitcnt vmcnt(2) lgkmcnt(0)\n"
"s_barrier\n"
"s_nop 0\n"
"v_mfma_f32_32x32x16_bf16 v[144:159], v[208:211], v[120:123], v[144:159]\n"
"ds_read_b64_tr_b16 v[224:225], v244 offset:32768\n"
"ds_read_b64_tr_b16 v[226:227], v244 offset:33280\n"
"ds_read_b64_tr_b16 v[228:229], v245 offset:32768\n"
"ds_read_b64_tr_b16 v[230:231], v245 offset:33280\n"
"v_mfma_f32_32x32x16_bf16 v[160:175], v[212:215], v[120:123], v[160:175]\n"
"v_mfma_f32_32x32x16_bf16 v[144:159], v[216:219], v[124:127], v[144:159]\n"
"ds_read_b64_tr_b16 v[208:209], v244 offset:34816\n"
"ds_read_b64_tr_b16 v[210:211], v244 offset:35328\n"
"ds_read_b64_tr_b16 v[212:213], v245 offset:34816\n"
"ds_read_b64_tr_b16 v[214:215], v245 offset:35328\n"
"s_add_u32 m0, s43, 16384\n"
"v_mfma_f32_32x32x16_bf16 v[160:175], v[220:223], v[124:127], v[160:175]\n"
"buffer_load_dwordx4 v254, s[16:19], s36 offen lds\n"
"s_waitcnt lgkmcnt(4)\n"
"s_nop 0\n"
"v_mfma_f32_32x32x16_bf16 v[144:159], v[224:227], v[128:131], v[144:159]\n"
"ds_read_b64_tr_b16 v[216:217], v244 offset:36864\n"
"ds_read_b64_tr_b16 v[218:219], v244 offset:37376\n"
"ds_read_b64_tr_b16 v[220:221], v245 offset:36864\n"
"ds_read_b64_tr_b16 v[222:223], v245 offset:37376\n"
"v_mfma_f32_32x32x16_bf16 v[160:175], v[228:231], v[128:131], v[160:175]\n"
"s_waitcnt lgkmcnt(4)\n"
"s_nop 0\n"
"v_mfma_f32_32x32x16_bf16 v[144:159], v[208:211], v[132:135], v[144:159]\n"
"ds_read_b64_tr_b16 v[224:225], v244 offset:38912\n"
"ds_read_b64_tr_b16 v[226:227], v244 offset:39424\n"
"ds_read_b64_tr_b16 v[228:229], v245 offset:38912\n"
"ds_read_b64_tr_b16 v[230:231], v245 offset:39424\n"
"v_mfma_f32_32x32x16_bf16 v[160:175], v[212:215], v[132:135], v[160:175]\n"
"buffer_load_dwordx4 v255, s[16:19], s36 offen offset:1024 lds\n"
"s_add_u32 s36, s36, s37\n"
"s_waitcnt vmcnt(2) lgkmcnt(0)\n"
"s_barrier\n"
"s_nop 0\n"
"v_mfma_f32_32x32x16_bf16 v[144:159], v[216:219], v[136:139], v[144:159]\n"
"ds_read_b64_tr_b16 v[208:209], v244 offset:0\n"
"ds_read_b64_tr_b16 v[210:211], v244 offset:512\n"
"ds_read_b64_tr_b16 v[212:213], v245 offset:0\n"
"ds_read_b64_tr_b16 v[214:215], v245 offset:512\n"
"v_mfma_f32_32x32x16_bf16 v[160:175], v[220:223], v[136:139], v[160:175]\n"
"v_mfma_f32_32x32x16_bf16 v[176:191], v[224:227], v[140:143], v[144:159]\n"
"ds_read_b64_tr_b16 v[216:217], v244 offset:2048\n"
"ds_read_b64_tr_b16 v[218:219], v244 offset:2560\n"
"ds_read_b64_tr_b16 v[220:221], v245 offset:2048\n"
"ds_read_b64_tr_b16 v[222:223], v245 offset:2560\n"
"s_add_u32 m0, s43, 32768\n"
"s_nop 4\n"
"s_nop 0\n"
"ds_read_b128 v[144:147], v247 offset:0\n"
"ds_read_b128 v[148:151], v247 offset:32\n"
"ds_read_b128 v[152:155], v247 offset:64\n"
"ds_read_b128 v[156:159], v247 offset:96\n"
"v_mfma_f32_32x32x16_bf16 v[192:207], v[228:231], v[140:143], v[160:175]\n"
"buffer_load_dwordx4 v254, s[16:19], s36 offen lds\n"
"s_nop 0\n"
"s_nop 0\n"
"ds_read_b128 v[160:163], v247 offset:128\n"
"ds_read_b128 v[164:167], v247 offset:160\n"
"ds_read_b128 v[168:171], v247 offset:192\n"
"ds_read_b128 v[172:175], v247 offset:224\n"
"buffer_load_dwordx4 v255, s[16:19], s36 offen offset:1024 lds\n"
"s_add_u32 s36, s36, s37\n"
"s_nop 0\n"
"v_add_u32 v247, 256, v247\n"
"s_mov_b32 s39, 2\n"
".p2align 3\n"
"P1LOOP:\n"
"s_waitcnt lgkmcnt(0)\n"
"s_nop 0\n"
"v_mfma_f32_32x32x16_bf16 v[144:159], v[208:211], v[0:3], v[144:159]\n"
"ds_read_b64_tr_b16 v[224:225], v244 offset:4096\n"
"ds_read_b64_tr_b16 v[226:227], v244 offset:4608\n"
"ds_read_b64_tr_b16 v[228:229], v245 offset:4096\n"
"ds_read_b64_tr_b16 v[230:231], v245 offset:4608\n"
"v_fmaak_f32 v236, v176, v176, 0x409d30fc\n"
"v_mul_f32_e64 v236, v236, v176\n"
"v_fmaak_f32 v237, v177, v177, 0x409d30fc\n"
"v_mul_f32_e64 v237, v237, v177\n"
"v_mfma_f32_32x32x16_bf16 v[160:175], v[212:215], v[0:3], v[160:175]\n"
"v_exp_f32_e64 v236, -v236\n"
"v_fmaak_f32 v238, v178, v178, 0x409d30fc\n"
"v_mul_f32_e64 v238, v238, v178\n"
"v_mfma_f32_32x32x16_bf16 v[144:159], v[216:219], v[4:7], v[144:159]\n"
"ds_read_b64_tr_b16 v[208:209], v244 offset:6144\n"
"ds_read_b64_tr_b16 v[210:211], v244 offset:6656\n"
"ds_read_b64_tr_b16 v[212:213], v245 offset:6144\n"
"ds_read_b64_tr_b16 v[214:215], v245 offset:6656\n"
"v_add_f32_e64 v236, 1.0, v236\n"
"v_exp_f32_e64 v237, -v237\n"
"v_fmaak_f32 v239, v179, v179, 0x409d30fc\n"
"v_mfma_f32_32x32x16_bf16 v[160:175], v[220:223], v[4:7], v[160:175]\n"
"v_mul_f32 v239, v239, v179\n"
"v_rcp_f32 v236, v236\n"
"v_add_f32 v237, 1.0, v237\n"
"s_waitcnt vmcnt(2) lgkmcnt(0)\n"
"s_barrier\n"
"s_nop 0\n"
"v_mfma_f32_32x32x16_bf16 v[144:159], v[224:227], v[8:11], v[144:159]\n"
"ds_read_b64_tr_b16 v[216:217], v244 offset:16384\n"
"ds_read_b64_tr_b16 v[218:219], v244 offset:16896\n"
"ds_read_b64_tr_b16 v[220:221], v245 offset:16384\n"
"ds_read_b64_tr_b16 v[222:223], v245 offset:16896\n"
"v_exp_f32_e64 v238, -v238\n"
"v_fmaak_f32 v240, v180, v180, 0x409d30fc\n"
"v_mul_f32 v236, v236, v176\n"
"v_mul_f32 v240, v240, v180\n"
"v_mfma_f32_32x32x16_bf16 v[160:175], v[228:231], v[8:11], v[160:175]\n"
"v_rcp_f32 v237, v237\n"
"v_add_f32 v238, 1.0, v238\n"
"v_exp_f32_e64 v239, -v239\n"
"v_mfma_f32_32x32x16_bf16 v[144:159], v[208:211], v[12:15], v[144:159]\n"
"ds_read_b64_tr_b16 v[224:225], v244 offset:18432\n"
"ds_read_b64_tr_b16 v[226:227], v244 offset:18944\n"
"ds_read_b64_tr_b16 v[228:229], v245 offset:18432\n"
"ds_read_b64_tr_b16 v[230:231], v245 offset:18944\n"
"s_mov_b32 m0, s43\n"
"s_nop 0\n"
"v_fmaak_f32 v241, v181, v181, 0x409d30fc\n"
"v_mul_f32 v237, v237, v177\n"
"v_mul_f32 v241, v241, v181\n"
"v_mfma_f32_32x32x16_bf16 v[160:175], v[212:215], v[12:15], v[160:175]\n"
"buffer_load_dwordx4 v254, s[16:19], s36 offen lds\n"
"v_rcp_f32 v238, v238\n"
"v_add_f32 v239, 1.0, v239\n"
"v_exp_f32_e64 v240, -v240\n"
"s_waitcnt lgkmcnt(4)\n"
"s_nop 0\n"
"v_mfma_f32_32x32x16_bf16 v[144:159], v[216:219], v[16:19], v[144:159]\n"
"ds_read_b64_tr_b16 v[208:209], v244 offset:20480\n"
"ds_read_b64_tr_b16 v[210:211], v244 offset:20992\n"
"ds_read_b64_tr_b16 v[212:213], v245 offset:20480\n"
"ds_read_b64_tr_b16 v[214:215], v245 offset:20992\n"
"v_fmaak_f32 v242, v182, v182, 0x409d30fc\n"
"v_mul_f32 v238, v238, v178\n"
"v_mul_f32 v242, v242, v182\n"
"v_mfma_f32_32x32x16_bf16 v[160:175], v[220:223], v[16:19], v[160:175]\n"
"v_rcp_f32 v239, v239\n"
"v_add_f32 v240, 1.0, v240\n"
"v_exp_f32_e64 v241, -v241\n"
"v_fmaak_f32 v243, v183, v183, 0x409d30fc\n"
"s_waitcnt lgkmcnt(4)\n"
"s_nop 0\n"
"v_mfma_f32_32x32x16_bf16 v[144:159], v[224:227], v[20:23], v[144:159]\n"
"ds_read_b64_tr_b16 v[216:217], v244 offset:22528\n"
"ds_read_b64_tr_b16 v[218:219], v244 offset:23040\n"
"ds_read_b64_tr_b16 v[220:221], v245 offset:22528\n"
"ds_read_b64_tr_b16 v[222:223], v245 offset:23040\n"
"v_cvt_pk_bf16_f32 v232, v236, v237\n"
"v_mul_f32 v239, v239, v179\n"
"v_mul_f32 v243, v243, v183\n"
"v_mfma_f32_32x32x16_bf16 v[160:175], v[228:231], v[20:23], v[160:175]\n"
"buffer_load_dwordx4 v255, s[16:19], s36 offen offset:1024 lds\n"
"s_add_u32 s36, s36, s37\n"
"v_rcp_f32 v240, v240\n"
"v_add_f32_e64 v241, 1.0, v241\n"
"v_exp_f32_e64 v242, -v242\n"
"s_waitcnt vmcnt(2) lgkmcnt(0)\n"
"s_barrier\n"
"v_mfma_f32_32x32x16_bf16 v[144:159], v[208:211], v[24:27], v[144:159]\n"
"ds_read_b64_tr_b16 v[224:225], v244 offset:32768\n"
"ds_read_b64_tr_b16 v[226:227], v244 offset:33280\n"
"ds_read_b64_tr_b16 v[228:229], v245 offset:32768\n"
"ds_read_b64_tr_b16 v[230:231], v245 offset:33280\n"
"v_fmaak_f32 v236, v184, v184, 0x409d30fc\n"
"v_mul_f32 v240, v240, v180\n"
"v_mul_f32 v236, v236, v184\n"
"v_mfma_f32_32x32x16_bf16 v[160:175], v[212:215], v[24:27], v[160:175]\n"
"v_rcp_f32 v241, v241\n"
"v_add_f32 v242, 1.0, v242\n"
"v_exp_f32_e64 v243, -v243\n"
"v_mfma_f32_32x32x16_bf16 v[144:159], v[216:219], v[28:31], v[144:159]\n"
"ds_read_b64_tr_b16 v[208:209], v244 offset:34816\n"
"ds_read_b64_tr_b16 v[210:211], v244 offset:35328\n"
"ds_read_b64_tr_b16 v[212:213], v245 offset:34816\n"
"ds_read_b64_tr_b16 v[214:215], v245 offset:35328\n"
"s_add_u32 m0, s43, 16384\n"
"v_fmaak_f32 v237, v185, v185, 0x409d30fc\n"
"v_cvt_pk_bf16_f32 v233, v238, v239\n"
"v_mul_f32 v241, v241, v181\n"
"v_mul_f32 v237, v237, v185\n"
"v_mfma_f32_32x32x16_bf16 v[160:175], v[220:223], v[28:31], v[160:175]\n"
"buffer_load_dwordx4 v254, s[16:19], s36 offen lds\n"
"v_rcp_f32 v242, v242\n"
"v_add_f32 v243, 1.0, v243\n"
"v_exp_f32_e64 v236, -v236\n"
"s_waitcnt lgkmcnt(4)\n"
"s_nop 0\n"
"v_mfma_f32_32x32x16_bf16 v[144:159], v[224:227], v[32:35], v[144:159]\n"
"ds_read_b64_tr_b16 v[216:217], v244 offset:36864\n"
"ds_read_b64_tr_b16 v[218:219], v244 offset:37376\n"
"ds_read_b64_tr_b16 v[220:221], v245 offset:36864\n"
"ds_read_b64_tr_b16 v[222:223], v245 offset:37376\n"
"v_fmaak_f32 v238, v186, v186, 0x409d30fc\n"
"v_mul_f32 v242, v242, v182\n"
"v_mul_f32 v238, v238, v186\n"
"v_mfma_f32_32x32x16_bf16 v[160:175], v[228:231], v[32:35], v[160:175]\n"
"v_rcp_f32 v243, v243\n"
"v_add_f32 v236, 1.0, v236\n"
"v_exp_f32_e64 v237, -v237\n"
"s_waitcnt lgkmcnt(4)\n"
"s_nop 0\n"
"v_mfma_f32_32x32x16_bf16 v[144:159], v[208:211], v[36:39], v[144:159]\n"
"ds_read_b64_tr_b16 v[224:225], v244 offset:38912\n"
"ds_read_b64_tr_b16 v[226:227], v244 offset:39424\n"
"ds_read_b64_tr_b16 v[228:229], v245 offset:38912\n"
"ds_read_b64_tr_b16 v[230:231], v245 offset:39424\n"
"v_fmaak_f32 v239, v187, v187, 0x409d30fc\n"
"v_cvt_pk_bf16_f32 v234, v240, v241\n"
"v_mul_f32 v243, v243, v183\n"
"v_mul_f32 v239, v239, v187\n"
"v_mfma_f32_32x32x16_bf16 v[160:175], v[212:215], v[36:39], v[160:175]\n"
"buffer_load_dwordx4 v255, s[16:19], s36 offen offset:1024 lds\n"
"s_add_u32 s36, s36, s37\n"
"v_rcp_f32 v236, v236\n"
"v_add_f32_e64 v237, 1.0, v237\n"
"v_exp_f32_e64 v238, -v238\n"
"s_waitcnt vmcnt(2) lgkmcnt(0)\n"
"s_barrier\n"
"v_mfma_f32_32x32x16_bf16 v[144:159], v[216:219], v[40:43], v[144:159]\n"
"ds_read_b64_tr_b16 v[208:209], v244 offset:0\n"
"ds_read_b64_tr_b16 v[210:211], v244 offset:512\n"
"ds_read_b64_tr_b16 v[212:213], v245 offset:0\n"
"ds_read_b64_tr_b16 v[214:215], v245 offset:512\n"
"v_fmaak_f32 v240, v188, v188, 0x409d30fc\n"
"v_mul_f32 v236, v236, v184\n"
"v_mul_f32 v240, v240, v188\n"
"v_mfma_f32_32x32x16_bf16 v[160:175], v[220:223], v[40:43], v[160:175]\n"
"v_rcp_f32 v237, v237\n"
"v_add_f32 v238, 1.0, v238\n"
"v_exp_f32_e64 v239, -v239\n"
"v_mfma_f32_32x32x16_bf16 v[144:159], v[224:227], v[44:47], v[144:159]\n"
"ds_read_b64_tr_b16 v[216:217], v244 offset:2048\n"
"ds_read_b64_tr_b16 v[218:219], v244 offset:2560\n"
"ds_read_b64_tr_b16 v[220:221], v245 offset:2048\n"
"ds_read_b64_tr_b16 v[222:223], v245 offset:2560\n"
"s_add_u32 m0, s43, 32768\n"
"v_fmaak_f32 v241, v189, v189, 0x409d30fc\n"
"v_cvt_pk_bf16_f32 v235, v242, v243\n"
"ds_write_b128 v246, v[232:235] offset:0\n"
"v_mfma_f32_32x32x16_bf16 v[160:175], v[228:231], v[44:47], v[160:175]\n"
"buffer_load_dwordx4 v254, s[16:19], s36 offen lds\n"
"v_mul_f32 v237, v237, v185\n"
"v_mul_f32 v241, v241, v189\n"
"v_rcp_f32 v238, v238\n"
"v_add_f32 v239, 1.0, v239\n"
"s_waitcnt lgkmcnt(5)\n"
"s_nop 0\n"
"v_mfma_f32_32x32x16_bf16 v[144:159], v[208:211], v[48:51], v[144:159]\n"
"ds_read_b64_tr_b16 v[224:225], v244 offset:4096\n"
"ds_read_b64_tr_b16 v[226:227], v244 offset:4608\n"
"ds_read_b64_tr_b16 v[228:229], v245 offset:4096\n"
"ds_read_b64_tr_b16 v[230:231], v245 offset:4608\n"
"v_exp_f32_e64 v240, -v240\n"
"v_fmaak_f32 v242, v190, v190, 0x409d30fc\n"
"v_mul_f32_e64 v238, v238, v186\n"
"v_mfma_f32_32x32x16_bf16 v[160:175], v[212:215], v[48:51], v[160:175]\n"
"v_mul_f32 v242, v242, v190\n"
"v_rcp_f32 v239, v239\n"
"v_add_f32 v240, 1.0, v240\n"
"s_waitcnt lgkmcnt(5)\n"
"v_mfma_f32_32x32x16_bf16 v[144:159], v[216:219], v[52:55], v[144:159]\n"
"ds_read_b64_tr_b16 v[208:209], v244 offset:6144\n"
"ds_read_b64_tr_b16 v[210:211], v244 offset:6656\n"
"ds_read_b64_tr_b16 v[212:213], v245 offset:6144\n"
"ds_read_b64_tr_b16 v[214:215], v245 offset:6656\n"
"v_exp_f32_e64 v241, -v241\n"
"v_fmaak_f32 v243, v191, v191, 0x409d30fc\n"
"v_cvt_pk_bf16_f32 v232, v236, v237\n"
"v_mfma_f32_32x32x16_bf16 v[160:175], v[220:223], v[52:55], v[160:175]\n"
"buffer_load_dwordx4 v255, s[16:19], s36 offen offset:1024 lds\n"
"s_add_u32 s36, s36, s37\n"
"v_mul_f32 v239, v239, v187\n"
"v_mul_f32 v243, v243, v191\n"
"v_rcp_f32 v240, v240\n"
"s_waitcnt vmcnt(2) lgkmcnt(0)\n"
"s_barrier\n"
"v_mfma_f32_32x32x16_bf16 v[144:159], v[224:227], v[56:59], v[144:159]\n"
"ds_read_b64_tr_b16 v[216:217], v244 offset:16384\n"
"ds_read_b64_tr_b16 v[218:219], v244 offset:16896\n"
"ds_read_b64_tr_b16 v[220:221], v245 offset:16384\n"
"ds_read_b64_tr_b16 v[222:223], v245 offset:16896\n"
"v_add_f32_e64 v241, 1.0, v241\n"
"v_exp_f32_e64 v242, -v242\n"
"v_fmaak_f32 v236, v192, v192, 0x409d30fc\n"
"v_mul_f32_e64 v240, v240, v188\n"
"v_mfma_f32_32x32x16_bf16 v[160:175], v[228:231], v[56:59], v[160:175]\n"
"v_mul_f32 v236, v236, v192\n"
"v_rcp_f32 v241, v241\n"
"v_add_f32_e64 v242, 1.0, v242\n"
"v_mfma_f32_32x32x16_bf16 v[144:159], v[208:211], v[60:63], v[144:159]\n"
"ds_read_b64_tr_b16 v[224:225], v244 offset:18432\n"
"ds_read_b64_tr_b16 v[226:227], v244 offset:18944\n"
"ds_read_b64_tr_b16 v[228:229], v245 offset:18432\n"
"ds_read_b64_tr_b16 v[230:231], v245 offset:18944\n"
"s_mov_b32 m0, s43\n"
"s_nop 0\n"
"v_exp_f32_e64 v243, -v243\n"
"v_fmaak_f32 v237, v193, v193, 0x409d30fc\n"
"v_cvt_pk_bf16_f32 v233, v238, v239\n"
"v_mfma_f32_32x32x16_bf16 v[160:175], v[212:215], v[60:63], v[160:175]\n"
"buffer_load_dwordx4 v254, s[16:19], s36 offen lds\n"
"v_mul_f32 v241, v241, v189\n"
"v_mul_f32 v237, v237, v193\n"
"v_rcp_f32 v242, v242\n"
"s_waitcnt lgkmcnt(4)\n"
"v_add_f32_e64 v243, 1.0, v243\n"
"v_mfma_f32_32x32x16_bf16 v[144:159], v[216:219], v[64:67], v[144:159]\n"
"ds_read_b64_tr_b16 v[208:209], v244 offset:20480\n"
"ds_read_b64_tr_b16 v[210:211], v244 offset:20992\n"
"ds_read_b64_tr_b16 v[212:213], v245 offset:20480\n"
"ds_read_b64_tr_b16 v[214:215], v245 offset:20992\n"
"v_exp_f32_e64 v236, -v236\n"
"v_fmaak_f32 v238, v194, v194, 0x409d30fc\n"
"v_mfma_f32_32x32x16_bf16 v[160:175], v[220:223], v[64:67], v[160:175]\n"
"v_mul_f32 v242, v242, v190\n"
"v_mul_f32 v238, v238, v194\n"
"v_rcp_f32 v243, v243\n"
"v_add_f32 v236, 1.0, v236\n"
"s_waitcnt lgkmcnt(4)\n"
"s_nop 0\n"
"v_mfma_f32_32x32x16_bf16 v[144:159], v[224:227], v[68:71], v[144:159]\n"
"ds_read_b64_tr_b16 v[216:217], v244 offset:22528\n"
"ds_read_b64_tr_b16 v[218:219], v244 offset:23040\n"
"ds_read_b64_tr_b16 v[220:221], v245 offset:22528\n"
"ds_read_b64_tr_b16 v[222:223], v245 offset:23040\n"
"v_exp_f32_e64 v237, -v237\n"
"v_fmaak_f32 v239, v195, v195, 0x409d30fc\n"
"v_cvt_pk_bf16_f32 v234, v240, v241\n"
"v_mfma_f32_32x32x16_bf16 v[160:175], v[228:231], v[68:71], v[160:175]\n"
"buffer_load_dwordx4 v255, s[16:19], s36 offen offset:1024 lds\n"
"s_add_u32 s36, s36, s37\n"
"v_mul_f32 v243, v243, v191\n"
"v_mul_f32 v239, v239, v195\n"
"v_rcp_f32 v236, v236\n"
"s_waitcnt vmcnt(2) lgkmcnt(0)\n"
"s_barrier\n"
"v_mfma_f32_32x32x16_bf16 v[144:159], v[208:211], v[72:75], v[144:159]\n"
"ds_read_b64_tr_b16 v[224:225], v244 offset:32768\n"
"ds_read_b64_tr_b16 v[226:227], v244 offset:33280\n"
"ds_read_b64_tr_b16 v[228:229], v245 offset:32768\n"
"ds_read_b64_tr_b16 v[230:231], v245 offset:33280\n"
"v_add_f32_e64 v237, 1.0, v237\n"
"v_exp_f32_e64 v238, -v238\n"
"v_fmaak_f32 v240, v196, v196, 0x409d30fc\n"
"v_mfma_f32_32x32x16_bf16 v[160:175], v[212:215], v[72:75], v[160:175]\n"
"v_mul_f32 v236, v236, v192\n"
"v_mul_f32 v240, v240, v196\n"
"v_rcp_f32 v237, v237\n"
"v_add_f32 v238, 1.0, v238\n"
"v_mfma_f32_32x32x16_bf16 v[144:159], v[216:219], v[76:79], v[144:159]\n"
"ds_read_b64_tr_b16 v[208:209], v244 offset:34816\n"
"ds_read_b64_tr_b16 v[210:211], v244 offset:35328\n"
"ds_read_b64_tr_b16 v[212:213], v245 offset:34816\n"
"ds_read_b64_tr_b16 v[214:215], v245 offset:35328\n"
"s_add_u32 m0, s43, 16384\n"
"v_exp_f32_e64 v239, -v239\n"
"v_fmaak_f32 v241, v197, v197, 0x409d30fc\n"
"v_cvt_pk_bf16_f32 v235, v242, v243\n"
"v_mfma_f32_32x32x16_bf16 v[160:175], v[220:223], v[76:79], v[160:175]\n"
"buffer_load_dwordx4 v254, s[16:19], s36 offen lds\n"
"ds_write_b128 v246, v[232:235] offset:512\n"
"v_mul_f32 v237, v237, v193\n"
"v_mul_f32 v241, v241, v197\n"
"s_waitcnt lgkmcnt(5)\n"
"v_rcp_f32 v238, v238\n"
"v_mfma_f32_32x32x16_bf16 v[144:159], v[224:227], v[80:83], v[144:159]\n"
"ds_read_b64_tr_b16 v[216:217], v244 offset:36864\n"
"ds_read_b64_tr_b16 v[218:219], v244 offset:37376\n"
"ds_read_b64_tr_b16 v[220:221], v245 offset:36864\n"
"ds_read_b64_tr_b16 v[222:223], v245 offset:37376\n"
"v_add_f32_e64 v239, 1.0, v239\n"
"v_exp_f32_e64 v240, -v240\n"
"v_mfma_f32_32x32x16_bf16 v[160:175], v[228:231], v[80:83], v[160:175]\n"
"v_fmaak_f32 v242, v198, v198, 0x409d30fc\n"
"v_mul_f32 v238, v238, v194\n"
"v_mul_f32 v242, v242, v198\n"
"s_waitcnt lgkmcnt(5)\n"
"v_rcp_f32 v239, v239\n"
"v_mfma_f32_32x32x16_bf16 v[144:159], v[208:211], v[84:87], v[144:159]\n"
"ds_read_b64_tr_b16 v[224:225], v244 offset:38912\n"
"ds_read_b64_tr_b16 v[226:227], v244 offset:39424\n"
"ds_read_b64_tr_b16 v[228:229], v245 offset:38912\n"
"ds_read_b64_tr_b16 v[230:231], v245 offset:39424\n"
"v_add_f32_e64 v240, 1.0, v240\n"
"v_exp_f32_e64 v241, -v241\n"
"v_fmaak_f32 v243, v199, v199, 0x409d30fc\n"
"v_mfma_f32_32x32x16_bf16 v[160:175], v[212:215], v[84:87], v[160:175]\n"
"buffer_load_dwordx4 v255, s[16:19], s36 offen offset:1024 lds\n"
"s_add_u32 s36, s36, s37\n"
"s_nop 0\n"
"v_cvt_pk_bf16_f32 v232, v236, v237\n"
"v_mul_f32 v239, v239, v195\n"
"v_mul_f32 v243, v243, v199\n"
"s_waitcnt vmcnt(2) lgkmcnt(0)\n"
"s_barrier\n"
"v_mfma_f32_32x32x16_bf16 v[144:159], v[216:219], v[88:91], v[144:159]\n"
"ds_read_b64_tr_b16 v[208:209], v244 offset:0\n"
"ds_read_b64_tr_b16 v[210:211], v244 offset:512\n"
"ds_read_b64_tr_b16 v[212:213], v245 offset:0\n"
"ds_read_b64_tr_b16 v[214:215], v245 offset:512\n"
"v_rcp_f32 v240, v240\n"
"v_add_f32 v241, 1.0, v241\n"
"v_exp_f32_e64 v242, -v242\n"
"v_mfma_f32_32x32x16_bf16 v[160:175], v[220:223], v[88:91], v[160:175]\n"
"v_fmaak_f32 v236, v200, v200, 0x409d30fc\n"
"v_mul_f32 v240, v240, v196\n"
"v_mul_f32 v236, v236, v200\n"
"v_mfma_f32_32x32x16_bf16 v[144:159], v[224:227], v[92:95], v[144:159]\n"
"ds_read_b64_tr_b16 v[216:217], v244 offset:2048\n"
"ds_read_b64_tr_b16 v[218:219], v244 offset:2560\n"
"ds_read_b64_tr_b16 v[220:221], v245 offset:2048\n"
"ds_read_b64_tr_b16 v[222:223], v245 offset:2560\n"
"s_add_u32 m0, s43, 32768\n"
"v_rcp_f32 v241, v241\n"
"v_add_f32 v242, 1.0, v242\n"
"v_exp_f32_e64 v243, -v243\n"
"v_mfma_f32_32x32x16_bf16 v[160:175], v[228:231], v[92:95], v[160:175]\n"
"buffer_load_dwordx4 v254, s[16:19], s36 offen lds\n"
"v_fmaak_f32 v237, v201, v201, 0x409d30fc\n"
"v_cvt_pk_bf16_f32 v233, v238, v239\n"
"v_mul_f32 v241, v241, v197\n"
"v_mul_f32 v237, v237, v201\n"
"s_waitcnt lgkmcnt(4)\n"
"v_rcp_f32 v242, v242\n"
"v_mfma_f32_32x32x16_bf16 v[144:159], v[208:211], v[96:99], v[144:159]\n"
"ds_read_b64_tr_b16 v[224:225], v244 offset:4096\n"
"ds_read_b64_tr_b16 v[226:227], v244 offset:4608\n"
"ds_read_b64_tr_b16 v[228:229], v245 offset:4096\n"
"ds_read_b64_tr_b16 v[230:231], v245 offset:4608\n"
"v_add_f32_e64 v243, 1.0, v243\n"
"v_exp_f32_e64 v236, -v236\n"
"v_mfma_f32_32x32x16_bf16 v[160:175], v[212:215], v[96:99], v[160:175]\n"
"v_fmaak_f32 v238, v202, v202, 0x409d30fc\n"
"v_mul_f32 v242, v242, v198\n"
"v_mul_f32 v238, v238, v202\n"
"s_waitcnt lgkmcnt(4)\n"
"v_rcp_f32 v243, v243\n"
"v_mfma_f32_32x32x16_bf16 v[144:159], v[216:219], v[100:103], v[144:159]\n"
"ds_read_b64_tr_b16 v[208:209], v244 offset:6144\n"
"ds_read_b64_tr_b16 v[210:211], v244 offset:6656\n"
"ds_read_b64_tr_b16 v[212:213], v245 offset:6144\n"
"ds_read_b64_tr_b16 v[214:215], v245 offset:6656\n"
"v_add_f32_e64 v236, 1.0, v236\n"
"v_exp_f32_e64 v237, -v237\n"
"v_mfma_f32_32x32x16_bf16 v[160:175], v[220:223], v[100:103], v[160:175]\n"
"buffer_load_dwordx4 v255, s[16:19], s36 offen offset:1024 lds\n"
"s_add_u32 s36, s36, s37\n"
"s_nop 0\n"
"v_fmaak_f32 v239, v203, v203, 0x409d30fc\n"
"v_cvt_pk_bf16_f32 v234, v240, v241\n"
"v_mul_f32 v243, v243, v199\n"
"v_mul_f32 v239, v239, v203\n"
"s_waitcnt vmcnt(2) lgkmcnt(0)\n"
"s_barrier\n"
"v_mfma_f32_32x32x16_bf16 v[144:159], v[224:227], v[104:107], v[144:159]\n"
"ds_read_b64_tr_b16 v[216:217], v244 offset:16384\n"
"ds_read_b64_tr_b16 v[218:219], v244 offset:16896\n"
"ds_read_b64_tr_b16 v[220:221], v245 offset:16384\n"
"ds_read_b64_tr_b16 v[222:223], v245 offset:16896\n"
"v_rcp_f32 v236, v236\n"
"v_add_f32 v237, 1.0, v237\n"
"v_exp_f32_e64 v238, -v238\n"
"v_mfma_f32_32x32x16_bf16 v[160:175], v[228:231], v[104:107], v[160:175]\n"
"v_fmaak_f32 v240, v204, v204, 0x409d30fc\n"
"v_mul_f32 v236, v236, v200\n"
"v_mul_f32 v240, v240, v204\n"
"v_mfma_f32_32x32x16_bf16 v[144:159], v[208:211], v[108:111], v[144:159]\n"
"ds_read_b64_tr_b16 v[224:225], v244 offset:18432\n"
"ds_read_b64_tr_b16 v[226:227], v244 offset:18944\n"
"ds_read_b64_tr_b16 v[228:229], v245 offset:18432\n"
"ds_read_b64_tr_b16 v[230:231], v245 offset:18944\n"
"s_mov_b32 s36, s38\n"
"s_nop 0\n"
"s_add_u32 s38, s38, 128\n"
"s_cmp_eq_u32 s39, 1\n"
"s_cbranch_scc0 SW_SKIP_0\n"
"s_mov_b32 s16, s24\n"
"s_mov_b32 s17, s25\n"
"s_mov_b32 s18, s26\n"
"s_mov_b32 s19, s27\n"
"s_mul_i32 s36, s82, 1152\n"
"s_mov_b32 s37, 147456\n"
"v_lshrrev_b32_e64 v249, 3, v248\n"
"v_bfe_u32 v250, v248, 1, 2\n"
"v_bfe_u32 v251, v248, 4, 2\n"
"v_xor_b32_e64 v250, v250, v251\n"
"v_mul_u32_u24 v249, 1152, v249\n"
"v_lshl_add_u32 v249, v250, 5, v249\n"
"v_and_b32_e64 v250, 1, v248\n"
"v_lshl_add_u32 v249, v250, 4, v249\n"
"s_mul_i32 s76, s11, 18432\n"
"v_add_u32_e64 v254, s76, v249\n"
"v_add_u32 v255, 8192, v254\n"
"SW_SKIP_0:\n"
"s_mov_b32 m0, s43\n"
"v_rcp_f32 v237, v237\n"
"v_add_f32_e64 v238, 1.0, v238\n"
"v_exp_f32_e64 v239, -v239\n"
"v_mfma_f32_32x32x16_bf16 v[160:175], v[212:215], v[108:111], v[160:175]\n"
"buffer_load_dwordx4 v254, s[16:19], s36 offen lds\n"
"v_fmaak_f32 v241, v205, v205, 0x409d30fc\n"
"v_cvt_pk_bf16_f32 v235, v242, v243\n"
"ds_write_b128 v246, v[232:235] offset:8192\n"
"s_waitcnt lgkmcnt(5)\n"
"v_mul_f32 v237, v237, v201\n"
"v_mfma_f32_32x32x16_bf16 v[144:159], v[216:219], v[112:115], v[144:159]\n"
"ds_read_b64_tr_b16 v[208:209], v244 offset:20480\n"
"ds_read_b64_tr_b16 v[210:211], v244 offset:20992\n"
"ds_read_b64_tr_b16 v[212:213], v245 offset:20480\n"
"ds_read_b64_tr_b16 v[214:215], v245 offset:20992\n"
"v_mul_f32 v241, v241, v205\n"
"v_rcp_f32 v238, v238\n"
"v_add_f32_e64 v239, 1.0, v239\n"
"v_mfma_f32_32x32x16_bf16 v[160:175], v[220:223], v[112:115], v[160:175]\n"
"v_exp_f32_e64 v240, -v240\n"
"v_fmaak_f32 v242, v206, v206, 0x409d30fc\n"
"v_mul_f32 v238, v238, v202\n"
"s_waitcnt lgkmcnt(5)\n"
"v_mul_f32_e64 v242, v242, v206\n"
"v_mfma_f32_32x32x16_bf16 v[144:159], v[224:227], v[116:119], v[144:159]\n"
"ds_read_b64_tr_b16 v[216:217], v244 offset:22528\n"
"ds_read_b64_tr_b16 v[218:219], v244 offset:23040\n"
"ds_read_b64_tr_b16 v[220:221], v245 offset:22528\n"
"ds_read_b64_tr_b16 v[222:223], v245 offset:23040\n"
"v_rcp_f32 v239, v239\n"
"v_add_f32 v240, 1.0, v240\n"
"v_mfma_f32_32x32x16_bf16 v[160:175], v[228:231], v[116:119], v[160:175]\n"
"buffer_load_dwordx4 v255, s[16:19], s36 offen offset:1024 lds\n"
"s_add_u32 s36, s36, s37\n"
"s_nop 0\n"
"v_exp_f32_e64 v241, -v241\n"
"v_fmaak_f32 v243, v207, v207, 0x409d30fc\n"
"v_cvt_pk_bf16_f32 v232, v236, v237\n"
"s_waitcnt vmcnt(2) lgkmcnt(0)\n"
"s_barrier\n"
"v_mfma_f32_32x32x16_bf16 v[144:159], v[208:211], v[120:123], v[144:159]\n"
"ds_read_b64_tr_b16 v[224:225], v244 offset:32768\n"
"ds_read_b64_tr_b16 v[226:227], v244 offset:33280\n"
"ds_read_b64_tr_b16 v[228:229], v245 offset:32768\n"
"ds_read_b64_tr_b16 v[230:231], v245 offset:33280\n"
"v_mul_f32 v239, v239, v203\n"
"v_mul_f32 v243, v243, v207\n"
"v_rcp_f32_e64 v240, v240\n"
"v_mfma_f32_32x32x16_bf16 v[160:175], v[212:215], v[120:123], v[160:175]\n"
"v_add_f32_e64 v241, 1.0, v241\n"
"v_exp_f32_e64 v242, -v242\n"
"v_mul_f32 v240, v240, v204\n"
"v_rcp_f32 v241, v241\n"
"v_mfma_f32_32x32x16_bf16 v[144:159], v[216:219], v[124:127], v[144:159]\n"
"ds_read_b64_tr_b16 v[208:209], v244 offset:34816\n"
"ds_read_b64_tr_b16 v[210:211], v244 offset:35328\n"
"ds_read_b64_tr_b16 v[212:213], v245 offset:34816\n"
"ds_read_b64_tr_b16 v[214:215], v245 offset:35328\n"
"s_add_u32 m0, s43, 16384\n"
"v_add_f32_e64 v242, 1.0, v242\n"
"v_exp_f32_e64 v243, -v243\n"
"v_cvt_pk_bf16_f32 v233, v238, v239\n"
"v_mfma_f32_32x32x16_bf16 v[160:175], v[220:223], v[124:127], v[160:175]\n"
"buffer_load_dwordx4 v254, s[16:19], s36 offen lds\n"
"v_mul_f32 v241, v241, v205\n"
"v_rcp_f32 v242, v242\n"
"v_add_f32 v243, 1.0, v243\n"
"s_waitcnt lgkmcnt(4)\n"
"v_mul_f32_e64 v242, v242, v206\n"
"v_mfma_f32_32x32x16_bf16 v[144:159], v[224:227], v[128:131], v[144:159]\n"
"ds_read_b64_tr_b16 v[216:217], v244 offset:36864\n"
"ds_read_b64_tr_b16 v[218:219], v244 offset:37376\n"
"ds_read_b64_tr_b16 v[220:221], v245 offset:36864\n"
"ds_read_b64_tr_b16 v[222:223], v245 offset:37376\n"
"v_rcp_f32_e64 v243, v243\n"
"v_cvt_pk_bf16_f32 v234, v240, v241\n"
"v_mfma_f32_32x32x16_bf16 v[160:175], v[228:231], v[128:131], v[160:175]\n"
"v_mul_f32_e64 v243, v243, v207\n"
"v_cvt_pk_bf16_f32 v235, v242, v243\n"
"ds_write_b128 v246, v[232:235] offset:8704\n"
"s_waitcnt lgkmcnt(5)\n"
"s_nop 0\n"
"v_mfma_f32_32x32x16_bf16 v[144:159], v[208:211], v[132:135], v[144:159]\n"
"ds_read_b64_tr_b16 v[224:225], v244 offset:38912\n"
"ds_read_b64_tr_b16 v[226:227], v244 offset:39424\n"
"ds_read_b64_tr_b16 v[228:229], v245 offset:38912\n"
"ds_read_b64_tr_b16 v[230:231], v245 offset:39424\n"
"v_mfma_f32_32x32x16_bf16 v[160:175], v[212:215], v[132:135], v[160:175]\n"
"buffer_load_dwordx4 v255, s[16:19], s36 offen offset:1024 lds\n"
"s_add_u32 s36, s36, s37\n"
"s_waitcnt vmcnt(2) lgkmcnt(0)\n"
"s_barrier\n"
"s_nop 0\n"
"v_mfma_f32_32x32x16_bf16 v[144:159], v[216:219], v[136:139], v[144:159]\n"
"ds_read_b64_tr_b16 v[208:209], v244 offset:0\n"
"ds_read_b64_tr_b16 v[210:211], v244 offset:512\n"
"ds_read_b64_tr_b16 v[212:213], v245 offset:0\n"
"ds_read_b64_tr_b16 v[214:215], v245 offset:512\n"
"v_mfma_f32_32x32x16_bf16 v[160:175], v[220:223], v[136:139], v[160:175]\n"
"v_mfma_f32_32x32x16_bf16 v[176:191], v[224:227], v[140:143], v[144:159]\n"
"ds_read_b64_tr_b16 v[216:217], v244 offset:2048\n"
"ds_read_b64_tr_b16 v[218:219], v244 offset:2560\n"
"ds_read_b64_tr_b16 v[220:221], v245 offset:2048\n"
"ds_read_b64_tr_b16 v[222:223], v245 offset:2560\n"
"s_add_u32 m0, s43, 32768\n"
"s_nop 4\n"
"s_nop 0\n"
"ds_read_b128 v[144:147], v247 offset:0\n"
"ds_read_b128 v[148:151], v247 offset:32\n"
"ds_read_b128 v[152:155], v247 offset:64\n"
"ds_read_b128 v[156:159], v247 offset:96\n"
"v_mfma_f32_32x32x16_bf16 v[192:207], v[228:231], v[140:143], v[160:175]\n"
"buffer_load_dwordx4 v254, s[16:19], s36 offen lds\n"
"s_nop 0\n"
"s_nop 0\n"
"ds_read_b128 v[160:163], v247 offset:128\n"
"ds_read_b128 v[164:167], v247 offset:160\n"
"ds_read_b128 v[168:171], v247 offset:192\n"
"ds_read_b128 v[172:175], v247 offset:224\n"
"buffer_load_dwordx4 v255, s[16:19], s36 offen offset:1024 lds\n"
"s_add_u32 s36, s36, s37\n"
"s_nop 0\n"
"v_add_u32 v247, 256, v247\n"
"v_add_u32 v246, 32768, v246\n"
"s_sub_u32 s39, s39, 1\n"
"s_cmp_lg_u32 s39, 0\n"
"s_cbranch_scc1 P1LOOP\n"
".p2align 3\n"
"v_bfe_u32 v249, v248, 5, 1\n"
"v_bfe_u32 v250, v248, 2, 3\n"
"v_and_b32_e64 v251, 3, v248\n"
"v_bfe_u32 v252, v248, 3, 2\n"
"s_and_b32 s76, s11, 3\n"
"v_xor_b32 v252, s76, v252\n"
"v_lshl_add_u32 v250, v249, 4, v250\n"
"v_lshlrev_b32_e64 v250, 7, v250\n"
"v_lshl_add_u32 v250, v252, 5, v250\n"
"v_lshl_add_u32 v244, v251, 3, v250\n"
"s_lshr_b32 s77, s11, 2\n"
"s_lshl_b32 s78, s34, 1\n"
"s_add_u32 s78, s78, s77\n"
"s_mul_i32 s78, s78, 36\n"
"s_add_u32 s78, s78, s76\n"
"s_lshl_b32 s78, s78, 5\n"
"s_add_u32 s78, s78, s35\n"
"s_lshl_b32 s81, s78, 10\n"
"v_lshlrev_b32 v249, 4, v248\n"
"s_lshl_b32 s77, s77, 12\n"
"s_add_u32 s77, s77, 65536\n"
"v_add_u32_e64 v245, s77, v249\n"
"v_add_u32 v247, 65536, v245\n"
"s_mul_i32 s38, s82, 1152\n"
"s_add_u32 s38, s38, 128\n"
"s_mov_b32 s74, s43\n"
"s_nop 0\n"
"s_add_u32 s75, s43, 65536\n"
"s_add_u32 s80, s43, 114688\n"
"s_waitcnt vmcnt(0) lgkmcnt(0)\n"
"s_barrier\n"
"ds_read_b64_tr_b16 v[224:225], v244 offset:0\n"
"ds_read_b64_tr_b16 v[226:227], v244 offset:1024\n"
"ds_read_b64_tr_b16 v[228:229], v244 offset:4096\n"
"ds_read_b64_tr_b16 v[230:231], v244 offset:5120\n"
"ds_read_b128 v[0:3], v245 offset:0\n"
"ds_read_b128 v[48:51], v245 offset:1024\n"
"ds_read_b128 v[96:99], v245 offset:2048\n"
"ds_read_b128 v[144:147], v245 offset:3072\n"
"ds_read_b128 v[4:7], v245 offset:8192\n"
"ds_read_b128 v[52:55], v245 offset:9216\n"
"ds_read_b128 v[100:103], v245 offset:10240\n"
"ds_read_b128 v[148:151], v245 offset:11264\n"
"s_waitcnt lgkmcnt(4)\n"
"s_nop 0\n"
"v_mfma_f32_16x16x32_bf16 v[208:211], v[224:227], v[0:3], 0\n"
"ds_read_b64_tr_b16 v[250:251], v244 offset:8192\n"
"ds_read_b64_tr_b16 v[252:253], v244 offset:9216\n"
"ds_read_b128 v[8:11], v245 offset:32768\n"
"ds_read_b128 v[56:59], v245 offset:33792\n"
"ds_read_b128 v[104:107], v245 offset:34816\n"
"ds_read_b128 v[152:155], v245 offset:35840\n"
"v_fmaak_f32 v236, v176, v176, 0x409d30fc\n"
"v_mul_f32_e64 v236, v236, v176\n"
"v_fmaak_f32 v237, v177, v177, 0x409d30fc\n"
"v_mul_f32_e64 v237, v237, v177\n"
"v_exp_f32_e64 v236, -v236\n"
"v_fmaak_f32 v238, v178, v178, 0x409d30fc\n"
"v_mul_f32 v238, v238, v178\n"
"v_add_f32 v236, 1.0, v236\n"
"v_mfma_f32_16x16x32_bf16 v[212:215], v[224:227], v[48:51], 0\n"
"v_exp_f32_e64 v237, -v237\n"
"v_fmaak_f32 v239, v179, v179, 0x409d30fc\n"
"v_mul_f32 v239, v239, v179\n"
"v_rcp_f32 v236, v236\n"
"v_add_f32_e64 v237, 1.0, v237\n"
"v_exp_f32_e64 v238, -v238\n"
"v_fmaak_f32 v240, v180, v180, 0x409d30fc\n"
"v_mfma_f32_16x16x32_bf16 v[216:219], v[224:227], v[96:99], 0\n"
"v_mul_f32 v236, v236, v176\n"
"v_mul_f32 v240, v240, v180\n"
"v_rcp_f32 v237, v237\n"
"v_add_f32 v238, 1.0, v238\n"
"v_exp_f32_e64 v239, -v239\n"
"v_fmaak_f32 v241, v181, v181, 0x409d30fc\n"
"v_mul_f32_e64 v237, v237, v177\n"
"v_mfma_f32_16x16x32_bf16 v[220:223], v[224:227], v[144:147], 0\n"
"v_mul_f32 v241, v241, v181\n"
"v_rcp_f32 v238, v238\n"
"v_add_f32_e64 v239, 1.0, v239\n"
"v_exp_f32_e64 v240, -v240\n"
"v_fmaak_f32 v242, v182, v182, 0x409d30fc\n"
"v_mul_f32 v238, v238, v178\n"
"v_mul_f32 v242, v242, v182\n"
"s_waitcnt lgkmcnt(6)\n"
"s_nop 0\n"
"v_mfma_f32_16x16x32_bf16 v[208:211], v[228:231], v[4:7], v[208:211]\n"
"ds_read_b64_tr_b16 v[224:225], v244 offset:12288\n"
"ds_read_b64_tr_b16 v[226:227], v244 offset:13312\n"
"ds_read_b128 v[12:15], v245 offset:40960\n"
"ds_read_b128 v[60:63], v245 offset:41984\n"
"ds_read_b128 v[108:111], v245 offset:43008\n"
"ds_read_b128 v[156:159], v245 offset:44032\n"
"v_rcp_f32 v239, v239\n"
"v_add_f32 v240, 1.0, v240\n"
"v_exp_f32_e64 v241, -v241\n"
"v_fmaak_f32 v243, v183, v183, 0x409d30fc\n"
"v_cvt_pk_bf16_f32 v232, v236, v237\n"
"v_mul_f32 v239, v239, v179\n"
"v_mul_f32 v243, v243, v183\n"
"v_mfma_f32_16x16x32_bf16 v[212:215], v[228:231], v[52:55], v[212:215]\n"
"v_rcp_f32 v240, v240\n"
"v_add_f32 v241, 1.0, v241\n"
"v_exp_f32_e64 v242, -v242\n"
"v_fmaak_f32 v236, v184, v184, 0x409d30fc\n"
"v_mul_f32 v240, v240, v180\n"
"v_mul_f32 v236, v236, v184\n"
"v_rcp_f32_e64 v241, v241\n"
"v_mfma_f32_16x16x32_bf16 v[216:219], v[228:231], v[100:103], v[216:219]\n"
"v_add_f32_e64 v242, 1.0, v242\n"
"v_exp_f32_e64 v243, -v243\n"
"v_fmaak_f32 v237, v185, v185, 0x409d30fc\n"
"v_cvt_pk_bf16_f32 v233, v238, v239\n"
"v_mul_f32 v241, v241, v181\n"
"v_mul_f32 v237, v237, v185\n"
"v_rcp_f32_e64 v242, v242\n"
"v_mfma_f32_16x16x32_bf16 v[220:223], v[228:231], v[148:151], v[220:223]\n"
"v_add_f32_e64 v243, 1.0, v243\n"
"v_exp_f32_e64 v236, -v236\n"
"v_fmaak_f32 v238, v186, v186, 0x409d30fc\n"
"v_mul_f32 v242, v242, v182\n"
"v_mul_f32 v238, v238, v186\n"
"v_rcp_f32 v243, v243\n"
"v_add_f32 v236, 1.0, v236\n"
"s_waitcnt lgkmcnt(6)\n"
"s_nop 0\n"
"v_mfma_f32_16x16x32_bf16 v[208:211], v[250:253], v[8:11], v[208:211]\n"
"ds_read_b64_tr_b16 v[228:229], v244 offset:24576\n"
"ds_read_b64_tr_b16 v[230:231], v244 offset:25600\n"
"ds_read_b128 v[24:27], v245 offset:16384\n"
"ds_read_b128 v[72:75], v245 offset:17408\n"
"ds_read_b128 v[120:123], v245 offset:18432\n"
"ds_read_b128 v[168:171], v245 offset:19456\n"
"v_exp_f32_e64 v237, -v237\n"
"v_fmaak_f32 v239, v187, v187, 0x409d30fc\n"
"v_cvt_pk_bf16_f32 v234, v240, v241\n"
"v_mul_f32 v243, v243, v183\n"
"v_mul_f32 v239, v239, v187\n"
"v_rcp_f32 v236, v236\n"
"v_add_f32 v237, 1.0, v237\n"
"v_mfma_f32_16x16x32_bf16 v[212:215], v[250:253], v[56:59], v[212:215]\n"
"v_exp_f32_e64 v238, -v238\n"
"v_fmaak_f32 v240, v188, v188, 0x409d30fc\n"
"v_mul_f32 v236, v236, v184\n"
"v_mul_f32 v240, v240, v188\n"
"v_rcp_f32 v237, v237\n"
"v_add_f32 v238, 1.0, v238\n"
"v_exp_f32_e64 v239, -v239\n"
"v_mfma_f32_16x16x32_bf16 v[216:219], v[250:253], v[104:107], v[216:219]\n"
"v_fmaak_f32 v241, v189, v189, 0x409d30fc\n"
"v_cvt_pk_bf16_f32 v235, v242, v243\n"
"ds_write_b128 v246, v[232:235] offset:0\n"
"v_mul_f32 v237, v237, v185\n"
"v_mul_f32 v241, v241, v189\n"
"v_rcp_f32 v238, v238\n"
"v_add_f32 v239, 1.0, v239\n"
"v_mfma_f32_16x16x32_bf16 v[220:223], v[250:253], v[152:155], v[220:223]\n"
"v_exp_f32_e64 v240, -v240\n"
"v_fmaak_f32 v242, v190, v190, 0x409d30fc\n"
"v_mul_f32 v238, v238, v186\n"
"v_mul_f32 v242, v242, v190\n"
"v_rcp_f32 v239, v239\n"
"v_add_f32 v240, 1.0, v240\n"
"v_exp_f32_e64 v241, -v241\n"
"s_waitcnt lgkmcnt(7)\n"
"s_nop 0\n"
"v_mfma_f32_16x16x32_bf16 v[208:211], v[224:227], v[12:15], v[208:211]\n"
"ds_read_b64_tr_b16 v[250:251], v244 offset:28672\n"
"ds_read_b64_tr_b16 v[252:253], v244 offset:29696\n"
"ds_read_b128 v[28:31], v245 offset:24576\n"
"ds_read_b128 v[76:79], v245 offset:25600\n"
"ds_read_b128 v[124:127], v245 offset:26624\n"
"ds_read_b128 v[172:175], v245 offset:27648\n"
"v_fmaak_f32 v243, v191, v191, 0x409d30fc\n"
"v_cvt_pk_bf16_f32 v232, v236, v237\n"
"v_mul_f32 v239, v239, v187\n"
"v_mul_f32 v243, v243, v191\n"
"v_rcp_f32 v240, v240\n"
"v_add_f32 v241, 1.0, v241\n"
"v_exp_f32_e64 v242, -v242\n"
"v_mfma_f32_16x16x32_bf16 v[212:215], v[224:227], v[60:63], v[212:215]\n"
"v_fmaak_f32 v236, v192, v192, 0x409d30fc\n"
"v_mul_f32 v240, v240, v188\n"
"v_mul_f32 v236, v236, v192\n"
"v_rcp_f32 v241, v241\n"
"v_add_f32 v242, 1.0, v242\n"
"v_exp_f32_e64 v243, -v243\n"
"v_fmaak_f32 v237, v193, v193, 0x409d30fc\n"
"v_mfma_f32_16x16x32_bf16 v[216:219], v[224:227], v[108:111], v[216:219]\n"
"v_cvt_pk_bf16_f32 v233, v238, v239\n"
"v_mul_f32 v241, v241, v189\n"
"v_mul_f32 v237, v237, v193\n"
"v_rcp_f32 v242, v242\n"
"v_add_f32 v243, 1.0, v243\n"
"v_exp_f32_e64 v236, -v236\n"
"v_fmaak_f32 v238, v194, v194, 0x409d30fc\n"
"v_mfma_f32_16x16x32_bf16 v[220:223], v[224:227], v[156:159], v[220:223]\n"
"v_mul_f32 v242, v242, v190\n"
"v_mul_f32 v238, v238, v194\n"
"v_rcp_f32 v243, v243\n"
"v_add_f32 v236, 1.0, v236\n"
"v_exp_f32_e64 v237, -v237\n"
"v_fmaak_f32 v239, v195, v195, 0x409d30fc\n"
"v_cvt_pk_bf16_f32 v234, v240, v241\n"
"v_mul_f32 v243, v243, v191\n"
"s_waitcnt lgkmcnt(7)\n"
"v_mfma_f32_16x16x32_bf16 v[208:211], v[228:231], v[24:27], v[208:211]\n"
"ds_read_b64_tr_b16 v[224:225], v244 offset:32768\n"
"ds_read_b64_tr_b16 v[226:227], v244 offset:33792\n"
"ds_read_b128 v[32:35], v245 offset:49152\n"
"ds_read_b128 v[80:83], v245 offset:50176\n"
"ds_read_b128 v[128:131], v245 offset:51200\n"
"ds_read_b128 v[176:179], v245 offset:52224\n"
"v_mul_f32 v239, v239, v195\n"
"v_rcp_f32 v236, v236\n"
"v_add_f32_e64 v237, 1.0, v237\n"
"v_exp_f32_e64 v238, -v238\n"
"v_fmaak_f32 v240, v196, v196, 0x409d30fc\n"
"v_mul_f32 v236, v236, v192\n"
"v_mul_f32 v240, v240, v196\n"
"v_mfma_f32_16x16x32_bf16 v[212:215], v[228:231], v[72:75], v[212:215]\n"
"v_rcp_f32 v237, v237\n"
"v_add_f32 v238, 1.0, v238\n"
"v_exp_f32_e64 v239, -v239\n"
"v_fmaak_f32 v241, v197, v197, 0x409d30fc\n"
"v_cvt_pk_bf16_f32 v235, v242, v243\n"
"ds_write_b128 v246, v[232:235] offset:512\n"
"v_mul_f32_e64 v237, v237, v193\n"
"v_mfma_f32_16x16x32_bf16 v[216:219], v[228:231], v[120:123], v[216:219]\n"
"v_mul_f32 v241, v241, v197\n"
"v_rcp_f32 v238, v238\n"
"v_add_f32_e64 v239, 1.0, v239\n"
"v_exp_f32_e64 v240, -v240\n"
"v_fmaak_f32 v242, v198, v198, 0x409d30fc\n"
"v_mul_f32 v238, v238, v194\n"
"v_mul_f32 v242, v242, v198\n"
"v_mfma_f32_16x16x32_bf16 v[220:223], v[228:231], v[168:171], v[220:223]\n"
"v_rcp_f32 v239, v239\n"
"v_add_f32 v240, 1.0, v240\n"
"v_exp_f32_e64 v241, -v241\n"
"v_fmaak_f32 v243, v199, v199, 0x409d30fc\n"
"v_cvt_pk_bf16_f32 v232, v236, v237\n"
"v_mul_f32 v239, v239, v195\n"
"v_mul_f32 v243, v243, v199\n"
"s_waitcnt lgkmcnt(7)\n"
"s_nop 0\n"
"v_mfma_f32_16x16x32_bf16 v[208:211], v[250:253], v[28:31], v[208:211]\n"
"ds_read_b64_tr_b16 v[228:229], v244 offset:36864\n"
"ds_read_b64_tr_b16 v[230:231], v244 offset:37888\n"
"ds_read_b128 v[36:39], v245 offset:57344\n"
"ds_read_b128 v[84:87], v245 offset:58368\n"
"ds_read_b128 v[132:135], v245 offset:59392\n"
"ds_read_b128 v[180:183], v245 offset:60416\n"
"v_rcp_f32 v240, v240\n"
"v_add_f32 v241, 1.0, v241\n"
"v_exp_f32_e64 v242, -v242\n"
"v_fmaak_f32 v236, v200, v200, 0x409d30fc\n"
"v_mul_f32 v240, v240, v196\n"
"v_mul_f32 v236, v236, v200\n"
"v_rcp_f32_e64 v241, v241\n"
"v_mfma_f32_16x16x32_bf16 v[212:215], v[250:253], v[76:79], v[212:215]\n"
"v_add_f32_e64 v242, 1.0, v242\n"
"v_exp_f32_e64 v243, -v243\n"
"v_fmaak_f32 v237, v201, v201, 0x409d30fc\n"
"v_cvt_pk_bf16_f32 v233, v238, v239\n"
"v_mul_f32 v241, v241, v197\n"
"v_mul_f32 v237, v237, v201\n"
"v_rcp_f32_e64 v242, v242\n"
"v_mfma_f32_16x16x32_bf16 v[216:219], v[250:253], v[124:127], v[216:219]\n"
"v_add_f32_e64 v243, 1.0, v243\n"
"v_exp_f32_e64 v236, -v236\n"
"v_fmaak_f32 v238, v202, v202, 0x409d30fc\n"
"v_mul_f32 v242, v242, v198\n"
"v_mul_f32 v238, v238, v202\n"
"v_rcp_f32 v243, v243\n"
"v_add_f32 v236, 1.0, v236\n"
"v_mfma_f32_16x16x32_bf16 v[220:223], v[250:253], v[172:175], v[220:223]\n"
"v_exp_f32_e64 v237, -v237\n"
"v_fmaak_f32 v239, v203, v203, 0x409d30fc\n"
"v_cvt_pk_bf16_f32 v234, v240, v241\n"
"v_mul_f32 v243, v243, v199\n"
"v_mul_f32 v239, v239, v203\n"
"v_rcp_f32 v236, v236\n"
"v_add_f32 v237, 1.0, v237\n"
"s_waitcnt lgkmcnt(7)\n"
"s_nop 0\n"
"v_mfma_f32_16x16x32_bf16 v[208:211], v[224:227], v[32:35], v[208:211]\n"
"ds_read_b64_tr_b16 v[250:251], v244 offset:16384\n"
"ds_read_b64_tr_b16 v[252:253], v244 offset:17408\n"
"v_exp_f32_e64 v238, -v238\n"
"v_fmaak_f32 v240, v204, v204, 0x409d30fc\n"
"v_mul_f32 v236, v236, v200\n"
"v_mul_f32 v240, v240, v204\n"
"v_rcp_f32 v237, v237\n"
"v_add_f32 v238, 1.0, v238\n"
"v_exp_f32_e64 v239, -v239\n"
"v_mfma_f32_16x16x32_bf16 v[212:215], v[224:227], v[80:83], v[212:215]\n"
"v_fmaak_f32 v241, v205, v205, 0x409d30fc\n"
"v_cvt_pk_bf16_f32 v235, v242, v243\n"
"ds_write_b128 v246, v[232:235] offset:8192\n"
"v_mul_f32 v237, v237, v201\n"
"v_mul_f32 v241, v241, v205\n"
"v_rcp_f32 v238, v238\n"
"v_add_f32 v239, 1.0, v239\n"
"v_mfma_f32_16x16x32_bf16 v[216:219], v[224:227], v[128:131], v[216:219]\n"
"v_exp_f32_e64 v240, -v240\n"
"v_fmaak_f32 v242, v206, v206, 0x409d30fc\n"
"v_mul_f32 v238, v238, v202\n"
"v_mul_f32 v242, v242, v206\n"
"v_rcp_f32 v239, v239\n"
"v_add_f32 v240, 1.0, v240\n"
"v_exp_f32_e64 v241, -v241\n"
"v_mfma_f32_16x16x32_bf16 v[220:223], v[224:227], v[176:179], v[220:223]\n"
"v_fmaak_f32 v243, v207, v207, 0x409d30fc\n"
"v_cvt_pk_bf16_f32 v232, v236, v237\n"
"v_mul_f32 v239, v239, v203\n"
"v_mul_f32 v243, v243, v207\n"
"v_rcp_f32 v240, v240\n"
"v_add_f32 v241, 1.0, v241\n"
"v_exp_f32_e64 v242, -v242\n"
"s_waitcnt lgkmcnt(0)\n"
"s_barrier\n"
"v_mfma_f32_16x16x32_bf16 v[208:211], v[228:231], v[36:39], v[208:211]\n"
"ds_read_b64_tr_b16 v[224:225], v244 offset:20480\n"
"ds_read_b64_tr_b16 v[226:227], v244 offset:21504\n"
"s_mov_b32 s36, s38\n"
"s_nop 0\n"
"s_add_u32 s38, s38, 128\n"
"s_mov_b32 m0, s75\n"
"s_nop 0\n"
"buffer_load_dwordx4 v254, s[16:19], s36 offen lds\n"
"v_mul_f32 v240, v240, v204\n"
"v_rcp_f32 v241, v241\n"
"v_add_f32_e64 v242, 1.0, v242\n"
"v_exp_f32_e64 v243, -v243\n"
"v_cvt_pk_bf16_f32 v233, v238, v239\n"
"v_mul_f32 v241, v241, v205\n"
"v_rcp_f32 v242, v242\n"
"v_mfma_f32_16x16x32_bf16 v[212:215], v[228:231], v[84:87], v[212:215]\n"
"buffer_load_dwordx4 v255, s[16:19], s36 offen offset:1024 lds\n"
"s_add_u32 s36, s36, s37\n"
"v_add_f32 v243, 1.0, v243\n"
"v_mul_f32 v242, v242, v206\n"
"v_rcp_f32 v243, v243\n"
"v_cvt_pk_bf16_f32 v234, v240, v241\n"
"v_mul_f32_e64 v243, v243, v207\n"
"v_cvt_pk_bf16_f32 v235, v242, v243\n"
"ds_write_b128 v246, v[232:235] offset:8704\n"
"v_mfma_f32_16x16x32_bf16 v[216:219], v[228:231], v[132:135], v[216:219]\n"
"s_add_u32 m0, s75, 16384\n"
"s_nop 0\n"
"s_nop 0\n"
"buffer_load_dwordx4 v254, s[16:19], s36 offen lds\n"
"v_mfma_f32_16x16x32_bf16 v[220:223], v[228:231], v[180:183], v[220:223]\n"
"buffer_load_dwordx4 v255, s[16:19], s36 offen offset:1024 lds\n"
"s_add_u32 s36, s36, s37\n"
"s_waitcnt lgkmcnt(0)\n"
"s_barrier\n"
"s_nop 0\n"
"ds_read_b128 v[16:19], v247 offset:0\n"
"ds_read_b128 v[64:67], v247 offset:1024\n"
"ds_read_b128 v[112:115], v247 offset:2048\n"
"ds_read_b128 v[160:163], v247 offset:3072\n"
"ds_read_b128 v[20:23], v247 offset:8192\n"
"ds_read_b128 v[68:71], v247 offset:9216\n"
"ds_read_b128 v[116:119], v247 offset:10240\n"
"ds_read_b128 v[164:167], v247 offset:11264\n"
"ds_read_b128 v[40:43], v247 offset:16384\n"
"ds_read_b128 v[88:91], v247 offset:17408\n"
"ds_read_b128 v[136:139], v247 offset:18432\n"
"ds_read_b128 v[184:187], v247 offset:19456\n"
"ds_read_b128 v[44:47], v247 offset:24576\n"
"ds_read_b128 v[92:95], v247 offset:25600\n"
"ds_read_b128 v[140:143], v247 offset:26624\n"
"ds_read_b128 v[188:191], v247 offset:27648\n"
"v_add_u32 v247, 16384, v249\n"
"s_waitcnt lgkmcnt(12)\n"
"s_nop 0\n"
"v_mfma_f32_16x16x32_bf16 v[208:211], v[250:253], v[16:19], v[208:211]\n"
"ds_read_b64_tr_b16 v[228:229], v244 offset:40960\n"
"ds_read_b64_tr_b16 v[230:231], v244 offset:41984\n"
"s_add_u32 m0, s75, 32768\n"
"s_nop 0\n"
"s_nop 0\n"
"buffer_load_dwordx4 v254, s[16:19], s36 offen lds\n"
"v_mfma_f32_16x16x32_bf16 v[212:215], v[250:253], v[64:67], v[212:215]\n"
"buffer_load_dwordx4 v255, s[16:19], s36 offen offset:1024 lds\n"
"s_add_u32 s36, s36, s37\n"
"s_nop 0\n"
"v_mfma_f32_16x16x32_bf16 v[216:219], v[250:253], v[112:115], v[216:219]\n"
"v_mfma_f32_16x16x32_bf16 v[220:223], v[250:253], v[160:163], v[220:223]\n"
"s_waitcnt lgkmcnt(10)\n"
"s_nop 0\n"
"v_mfma_f32_16x16x32_bf16 v[208:211], v[224:227], v[20:23], v[208:211]\n"
"ds_read_b64_tr_b16 v[250:251], v244 offset:45056\n"
"ds_read_b64_tr_b16 v[252:253], v244 offset:46080\n"
"v_mfma_f32_16x16x32_bf16 v[212:215], v[224:227], v[68:71], v[212:215]\n"
"v_mfma_f32_16x16x32_bf16 v[216:219], v[224:227], v[116:119], v[216:219]\n"
"v_mfma_f32_16x16x32_bf16 v[220:223], v[224:227], v[164:167], v[220:223]\n"
"s_waitcnt vmcnt(0) lgkmcnt(0)\n"
"s_barrier\n"
"v_add_u32 v244, 65536, v244\n"
"v_mfma_f32_16x16x32_bf16 v[208:211], v[228:231], v[40:43], v[208:211]\n"
"ds_read_b64_tr_b16 v[224:225], v244 offset:0\n"
"ds_read_b64_tr_b16 v[226:227], v244 offset:1024\n"
"v_mfma_f32_16x16x32_bf16 v[212:215], v[228:231], v[88:91], v[212:215]\n"
"v_mfma_f32_16x16x32_bf16 v[216:219], v[228:231], v[136:139], v[216:219]\n"
"v_mfma_f32_16x16x32_bf16 v[220:223], v[228:231], v[184:187], v[220:223]\n"
"v_mfma_f32_16x16x32_bf16 v[192:195], v[250:253], v[44:47], v[208:211]\n"
"ds_read_b64_tr_b16 v[228:229], v244 offset:4096\n"
"ds_read_b64_tr_b16 v[230:231], v244 offset:5120\n"
"v_mfma_f32_16x16x32_bf16 v[196:199], v[250:253], v[92:95], v[212:215]\n"
"v_mfma_f32_16x16x32_bf16 v[200:203], v[250:253], v[140:143], v[216:219]\n"
"v_mfma_f32_16x16x32_bf16 v[204:207], v[250:253], v[188:191], v[220:223]\n"
"s_waitcnt lgkmcnt(2)\n"
"s_nop 0\n"
"v_mfma_f32_16x16x32_bf16 v[208:211], v[224:227], v[0:3], 0\n"
"ds_read_b64_tr_b16 v[250:251], v244 offset:8192\n"
"ds_read_b64_tr_b16 v[252:253], v244 offset:9216\n"
"v_mfma_f32_16x16x32_bf16 v[212:215], v[224:227], v[48:51], 0\n"
"s_mov_b32 s36, s38\n"
"s_nop 0\n"
"s_add_u32 s38, s38, 128\n"
"s_mov_b32 m0, s80\n"
"s_nop 0\n"
"buffer_load_dwordx4 v254, s[16:19], s36 offen lds\n"
"v_mfma_f32_16x16x32_bf16 v[216:219], v[224:227], v[96:99], 0\n"
"v_mfma_f32_16x16x32_bf16 v[220:223], v[224:227], v[144:147], 0\n"
"s_waitcnt lgkmcnt(2)\n"
"s_nop 0\n"
"v_mfma_f32_16x16x32_bf16 v[208:211], v[228:231], v[4:7], v[208:211]\n"
"ds_read_b64_tr_b16 v[224:225], v244 offset:12288\n"
"ds_read_b64_tr_b16 v[226:227], v244 offset:13312\n"
"v_cvt_pk_bf16_f32 v236, v192, v193\n"
"v_mfma_f32_16x16x32_bf16 v[212:215], v[228:231], v[52:55], v[212:215]\n"
"buffer_load_dwordx4 v255, s[16:19], s36 offen offset:1024 lds\n"
"s_add_u32 s36, s36, s37\n"
"s_nop 0\n"
"v_cvt_pk_bf16_f32 v237, v194, v195\n"
"v_mfma_f32_16x16x32_bf16 v[216:219], v[228:231], v[100:103], v[216:219]\n"
"v_cvt_pk_bf16_f32 v238, v196, v197\n"
"v_mfma_f32_16x16x32_bf16 v[220:223], v[228:231], v[148:151], v[220:223]\n"
"v_cvt_pk_bf16_f32 v239, v198, v199\n"
"s_waitcnt lgkmcnt(2)\n"
"s_nop 0\n"
"v_mfma_f32_16x16x32_bf16 v[208:211], v[250:253], v[8:11], v[208:211]\n"
"ds_read_b64_tr_b16 v[228:229], v244 offset:16384\n"
"ds_read_b64_tr_b16 v[230:231], v244 offset:17408\n"
"v_cvt_pk_bf16_f32 v240, v200, v201\n"
"v_mfma_f32_16x16x32_bf16 v[212:215], v[250:253], v[56:59], v[212:215]\n"
"s_add_u32 m0, s80, 16384\n"
"s_nop 0\n"
"s_nop 0\n"
"buffer_load_dwordx4 v254, s[16:19], s36 offen lds\n"
"v_cvt_pk_bf16_f32 v241, v202, v203\n"
"v_mfma_f32_16x16x32_bf16 v[216:219], v[250:253], v[104:107], v[216:219]\n"
"v_cvt_pk_bf16_f32 v242, v204, v205\n"
"v_mfma_f32_16x16x32_bf16 v[220:223], v[250:253], v[152:155], v[220:223]\n"
"v_cvt_pk_bf16_f32 v243, v206, v207\n"
"s_waitcnt lgkmcnt(2)\n"
"s_nop 0\n"
"v_mfma_f32_16x16x32_bf16 v[208:211], v[224:227], v[12:15], v[208:211]\n"
"ds_read_b64_tr_b16 v[250:251], v244 offset:20480\n"
"ds_read_b64_tr_b16 v[252:253], v244 offset:21504\n"
"v_mfma_f32_16x16x32_bf16 v[212:215], v[224:227], v[60:63], v[212:215]\n"
"buffer_load_dwordx4 v255, s[16:19], s36 offen offset:1024 lds\n"
"s_add_u32 s36, s36, s37\n"
"s_nop 0\n"
"buffer_store_dwordx4 v[236:239], v249, s[20:23], s81 offen sc1\n"
"v_mfma_f32_16x16x32_bf16 v[216:219], v[224:227], v[108:111], v[216:219]\n"
"v_mfma_f32_16x16x32_bf16 v[220:223], v[224:227], v[156:159], v[220:223]\n"
"s_waitcnt lgkmcnt(2)\n"
"s_nop 0\n"
"v_mfma_f32_16x16x32_bf16 v[208:211], v[228:231], v[16:19], v[208:211]\n"
"ds_read_b64_tr_b16 v[224:225], v244 offset:24576\n"
"ds_read_b64_tr_b16 v[226:227], v244 offset:25600\n"
"v_mfma_f32_16x16x32_bf16 v[212:215], v[228:231], v[64:67], v[212:215]\n"
"s_add_u32 m0, s80, 32768\n"
"s_nop 0\n"
"s_nop 0\n"
"buffer_load_dwordx4 v254, s[16:19], s36 offen lds\n"
"v_mfma_f32_16x16x32_bf16 v[216:219], v[228:231], v[112:115], v[216:219]\n"
"v_mfma_f32_16x16x32_bf16 v[220:223], v[228:231], v[160:163], v[220:223]\n"
"s_waitcnt lgkmcnt(2)\n"
"s_nop 0\n"
"v_mfma_f32_16x16x32_bf16 v[208:211], v[250:253], v[20:23], v[208:211]\n"
"ds_read_b64_tr_b16 v[228:229], v244 offset:28672\n"
"ds_read_b64_tr_b16 v[230:231], v244 offset:29696\n"
"buffer_store_dwordx4 v[240:243], v247, s[20:23], s81 offen sc1\n"
"s_add_u32 s81, s81, 131072\n"
"v_mfma_f32_16x16x32_bf16 v[212:215], v[250:253], v[68:71], v[212:215]\n"
"buffer_load_dwordx4 v255, s[16:19], s36 offen offset:1024 lds\n"
"s_add_u32 s36, s36, s37\n"
"s_nop 0\n"
"v_mfma_f32_16x16x32_bf16 v[216:219], v[250:253], v[116:119], v[216:219]\n"
"v_mfma_f32_16x16x32_bf16 v[220:223], v[250:253], v[164:167], v[220:223]\n"
"s_waitcnt lgkmcnt(2)\n"
"s_nop 0\n"
"v_mfma_f32_16x16x32_bf16 v[208:211], v[224:227], v[24:27], v[208:211]\n"
"ds_read_b64_tr_b16 v[250:251], v244 offset:32768\n"
"ds_read_b64_tr_b16 v[252:253], v244 offset:33792\n"
"v_mfma_f32_16x16x32_bf16 v[212:215], v[224:227], v[72:75], v[212:215]\n"
"s_mov_b32 s36, s38\n"
"s_nop 0\n"
"s_add_u32 s38, s38, 128\n"
"s_mov_b32 m0, s74\n"
"s_nop 0\n"
"buffer_load_dwordx4 v254, s[16:19], s36 offen lds\n"
"v_mfma_f32_16x16x32_bf16 v[216:219], v[224:227], v[120:123], v[216:219]\n"
"v_mfma_f32_16x16x32_bf16 v[220:223], v[224:227], v[168:171], v[220:223]\n"
"s_waitcnt lgkmcnt(2)\n"
"s_nop 0\n"
"v_mfma_f32_16x16x32_bf16 v[208:211], v[228:231], v[28:31], v[208:211]\n"
"ds_read_b64_tr_b16 v[224:225], v244 offset:36864\n"
"ds_read_b64_tr_b16 v[226:227], v244 offset:37888\n"
"v_mfma_f32_16x16x32_bf16 v[212:215], v[228:231], v[76:79], v[212:215]\n"
"buffer_load_dwordx4 v255, s[16:19], s36 offen offset:1024 lds\n"
"s_add_u32 s36, s36, s37\n"
"s_nop 0\n"
"v_mfma_f32_16x16x32_bf16 v[216:219], v[228:231], v[124:127], v[216:219]\n"
"v_mfma_f32_16x16x32_bf16 v[220:223], v[228:231], v[172:175], v[220:223]\n"
"s_waitcnt lgkmcnt(2)\n"
"s_nop 0\n"
"v_mfma_f32_16x16x32_bf16 v[208:211], v[250:253], v[32:35], v[208:211]\n"
"ds_read_b64_tr_b16 v[228:229], v244 offset:40960\n"
"ds_read_b64_tr_b16 v[230:231], v244 offset:41984\n"
"v_mfma_f32_16x16x32_bf16 v[212:215], v[250:253], v[80:83], v[212:215]\n"
"s_add_u32 m0, s74, 16384\n"
"s_nop 0\n"
"s_nop 0\n"
"buffer_load_dwordx4 v254, s[16:19], s36 offen lds\n"
"v_mfma_f32_16x16x32_bf16 v[216:219], v[250:253], v[128:131], v[216:219]\n"
"v_mfma_f32_16x16x32_bf16 v[220:223], v[250:253], v[176:179], v[220:223]\n"
"s_waitcnt lgkmcnt(2)\n"
"s_nop 0\n"
"v_mfma_f32_16x16x32_bf16 v[208:211], v[224:227], v[36:39], v[208:211]\n"
"ds_read_b64_tr_b16 v[250:251], v244 offset:45056\n"
"ds_read_b64_tr_b16 v[252:253], v244 offset:46080\n"
"v_mfma_f32_16x16x32_bf16 v[212:215], v[224:227], v[84:87], v[212:215]\n"
"buffer_load_dwordx4 v255, s[16:19], s36 offen offset:1024 lds\n"
"s_add_u32 s36, s36, s37\n"
"s_nop 0\n"
"v_mfma_f32_16x16x32_bf16 v[216:219], v[224:227], v[132:135], v[216:219]\n"
"v_mfma_f32_16x16x32_bf16 v[220:223], v[224:227], v[180:183], v[220:223]\n"
"s_waitcnt vmcnt(4) lgkmcnt(0)\n"
"s_barrier\n"
"v_add_u32 v244, 49152, v244\n"
"v_mfma_f32_16x16x32_bf16 v[208:211], v[228:231], v[40:43], v[208:211]\n"
"ds_read_b64_tr_b16 v[224:225], v244 offset:0\n"
"ds_read_b64_tr_b16 v[226:227], v244 offset:1024\n"
"v_mfma_f32_16x16x32_bf16 v[212:215], v[228:231], v[88:91], v[212:215]\n"
"s_add_u32 m0, s74, 32768\n"
"s_nop 0\n"
"s_nop 0\n"
"buffer_load_dwordx4 v254, s[16:19], s36 offen lds\n"
"v_mfma_f32_16x16x32_bf16 v[216:219], v[228:231], v[136:139], v[216:219]\n"
"v_mfma_f32_16x16x32_bf16 v[220:223], v[228:231], v[184:187], v[220:223]\n"
"v_mfma_f32_16x16x32_bf16 v[192:195], v[250:253], v[44:47], v[208:211]\n"
"ds_read_b64_tr_b16 v[228:229], v244 offset:4096\n"
"ds_read_b64_tr_b16 v[230:231], v244 offset:5120\n"
"v_mfma_f32_16x16x32_bf16 v[196:199], v[250:253], v[92:95], v[212:215]\n"
"buffer_load_dwordx4 v255, s[16:19], s36 offen offset:1024 lds\n"
"s_add_u32 s36, s36, s37\n"
"s_nop 0\n"
"v_mfma_f32_16x16x32_bf16 v[200:203], v[250:253], v[140:143], v[216:219]\n"
"v_mfma_f32_16x16x32_bf16 v[204:207], v[250:253], v[188:191], v[220:223]\n"
"s_mov_b32 s76, s74\n"
"s_mov_b32 s74, s75\n"
"s_mov_b32 s75, s80\n"
"s_mov_b32 s80, s76\n"
"s_mov_b32 s76, s74\n"
"s_mov_b32 s74, s75\n"
"s_mov_b32 s75, s80\n"
"s_mov_b32 s80, s76\n"
"s_mov_b32 s39, 5\n"
".p2align 3\n"
"P2LOOP:\n"
"s_cmp_eq_u32 s39, 1\n"
"s_cbranch_scc1 MP_OOL\n"
"MP_BACK:\n"
"s_waitcnt lgkmcnt(2)\n"
"s_nop 0\n"
"v_mfma_f32_16x16x32_bf16 v[208:211], v[224:227], v[0:3], 0\n"
"ds_read_b64_tr_b16 v[250:251], v244 offset:8192\n"
"ds_read_b64_tr_b16 v[252:253], v244 offset:9216\n"
"v_mfma_f32_16x16x32_bf16 v[212:215], v[224:227], v[48:51], 0\n"
"v_mfma_f32_16x16x32_bf16 v[216:219], v[224:227], v[96:99], 0\n"
"s_mov_b32 s36, s38\n"
"s_nop 0\n"
"s_add_u32 s38, s38, 128\n"
"s_mov_b32 m0, s80\n"
"s_nop 0\n"
"buffer_load_dwordx4 v254, s[16:19], s36 offen lds\n"
"v_mfma_f32_16x16x32_bf16 v[220:223], v[224:227], v[144:147], 0\n"
"s_waitcnt lgkmcnt(2)\n"
"s_nop 0\n"
"v_mfma_f32_16x16x32_bf16 v[208:211], v[228:231], v[4:7], v[208:211]\n"
"ds_read_b64_tr_b16 v[224:225], v244 offset:12288\n"
"ds_read_b64_tr_b16 v[226:227], v244 offset:13312\n"
"v_cvt_pk_bf16_f32 v236, v192, v193\n"
"v_mfma_f32_16x16x32_bf16 v[212:215], v[228:231], v[52:55], v[212:215]\n"
"v_cvt_pk_bf16_f32 v237, v194, v195\n"
"v_mfma_f32_16x16x32_bf16 v[216:219], v[228:231], v[100:103], v[216:219]\n"
"v_cvt_pk_bf16_f32 v238, v196, v197\n"
"v_mfma_f32_16x16x32_bf16 v[220:223], v[228:231], v[148:151], v[220:223]\n"
"v_cvt_pk_bf16_f32 v239, v198, v199\n"
"s_waitcnt lgkmcnt(2)\n"
"s_nop 0\n"
"v_mfma_f32_16x16x32_bf16 v[208:211], v[250:253], v[8:11], v[208:211]\n"
"ds_read_b64_tr_b16 v[228:229], v244 offset:16384\n"
"ds_read_b64_tr_b16 v[230:231], v244 offset:17408\n"
"v_cvt_pk_bf16_f32 v240, v200, v201\n"
"v_mfma_f32_16x16x32_bf16 v[212:215], v[250:253], v[56:59], v[212:215]\n"
"buffer_load_dwordx4 v255, s[16:19], s36 offen offset:1024 lds\n"
"s_add_u32 s36, s36, s37\n"
"s_nop 0\n"
"v_cvt_pk_bf16_f32 v241, v202, v203\n"
"v_mfma_f32_16x16x32_bf16 v[216:219], v[250:253], v[104:107], v[216:219]\n"
"v_cvt_pk_bf16_f32 v242, v204, v205\n"
"v_mfma_f32_16x16x32_bf16 v[220:223], v[250:253], v[152:155], v[220:223]\n"
"v_cvt_pk_bf16_f32 v243, v206, v207\n"
"s_waitcnt lgkmcnt(2)\n"
"s_nop 0\n"
"v_mfma_f32_16x16x32_bf16 v[208:211], v[224:227], v[12:15], v[208:211]\n"
"ds_read_b64_tr_b16 v[250:251], v244 offset:20480\n"
"ds_read_b64_tr_b16 v[252:253], v244 offset:21504\n"
"v_mfma_f32_16x16x32_bf16 v[212:215], v[224:227], v[60:63], v[212:215]\n"
"buffer_store_dwordx4 v[236:239], v249, s[20:23], s81 offen sc1\n"
"v_mfma_f32_16x16x32_bf16 v[216:219], v[224:227], v[108:111], v[216:219]\n"
"v_mfma_f32_16x16x32_bf16 v[220:223], v[224:227], v[156:159], v[220:223]\n"
"s_waitcnt lgkmcnt(2)\n"
"s_nop 0\n"
"v_mfma_f32_16x16x32_bf16 v[208:211], v[228:231], v[16:19], v[208:211]\n"
"ds_read_b64_tr_b16 v[224:225], v244 offset:24576\n"
"ds_read_b64_tr_b16 v[226:227], v244 offset:25600\n"
"s_add_u32 m0, s80, 16384\n"
"s_nop 0\n"
"s_nop 0\n"
"buffer_load_dwordx4 v254, s[16:19], s36 offen lds\n"
"v_mfma_f32_16x16x32_bf16 v[212:215], v[228:231], v[64:67], v[212:215]\n"
"v_mfma_f32_16x16x32_bf16 v[216:219], v[228:231], v[112:115], v[216:219]\n"
"v_mfma_f32_16x16x32_bf16 v[220:223], v[228:231], v[160:163], v[220:223]\n"
"s_waitcnt lgkmcnt(2)\n"
"s_nop 0\n"
"v_mfma_f32_16x16x32_bf16 v[208:211], v[250:253], v[20:23], v[208:211]\n"
"ds_read_b64_tr_b16 v[228:229], v244 offset:28672\n"
"ds_read_b64_tr_b16 v[230:231], v244 offset:29696\n"
"buffer_store_dwordx4 v[240:243], v247, s[20:23], s81 offen sc1\n"
"s_add_u32 s81, s81, 131072\n"
"v_mfma_f32_16x16x32_bf16 v[212:215], v[250:253], v[68:71], v[212:215]\n"
"v_mfma_f32_16x16x32_bf16 v[216:219], v[250:253], v[116:119], v[216:219]\n"
"v_mfma_f32_16x16x32_bf16 v[220:223], v[250:253], v[164:167], v[220:223]\n"
"buffer_load_dwordx4 v255, s[16:19], s36 offen offset:1024 lds\n"
"s_add_u32 s36, s36, s37\n"
"s_waitcnt lgkmcnt(2)\n"
"v_mfma_f32_16x16x32_bf16 v[208:211], v[224:227], v[24:27], v[208:211]\n"
"ds_read_b64_tr_b16 v[250:251], v244 offset:32768\n"
"ds_read_b64_tr_b16 v[252:253], v244 offset:33792\n"
"v_mfma_f32_16x16x32_bf16 v[212:215], v[224:227], v[72:75], v[212:215]\n"
"v_mfma_f32_16x16x32_bf16 v[216:219], v[224:227], v[120:123], v[216:219]\n"
"v_mfma_f32_16x16x32_bf16 v[220:223], v[224:227], v[168:171], v[220:223]\n"
"s_waitcnt lgkmcnt(2)\n"
"s_nop 0\n"
"v_mfma_f32_16x16x32_bf16 v[208:211], v[228:231], v[28:31], v[208:211]\n"
"ds_read_b64_tr_b16 v[224:225], v244 offset:36864\n"
"ds_read_b64_tr_b16 v[226:227], v244 offset:37888\n"
"v_mfma_f32_16x16x32_bf16 v[212:215], v[228:231], v[76:79], v[212:215]\n"
"v_mfma_f32_16x16x32_bf16 v[216:219], v[228:231], v[124:127], v[216:219]\n"
"s_add_u32 m0, s80, 32768\n"
"s_nop 0\n"
"s_nop 0\n"
"buffer_load_dwordx4 v254, s[16:19], s36 offen lds\n"
"v_mfma_f32_16x16x32_bf16 v[220:223], v[228:231], v[172:175], v[220:223]\n"
"s_waitcnt lgkmcnt(2)\n"
"s_nop 0\n"
"v_mfma_f32_16x16x32_bf16 v[208:211], v[250:253], v[32:35], v[208:211]\n"
"ds_read_b64_tr_b16 v[228:229], v244 offset:40960\n"
"ds_read_b64_tr_b16 v[230:231], v244 offset:41984\n"
"v_mfma_f32_16x16x32_bf16 v[212:215], v[250:253], v[80:83], v[212:215]\n"
"v_mfma_f32_16x16x32_bf16 v[216:219], v[250:253], v[128:131], v[216:219]\n"
"v_mfma_f32_16x16x32_bf16 v[220:223], v[250:253], v[176:179], v[220:223]\n"
"s_waitcnt lgkmcnt(2)\n"
"s_nop 0\n"
"v_mfma_f32_16x16x32_bf16 v[208:211], v[224:227], v[36:39], v[208:211]\n"
"ds_read_b64_tr_b16 v[250:251], v244 offset:45056\n"
"ds_read_b64_tr_b16 v[252:253], v244 offset:46080\n"
"v_mfma_f32_16x16x32_bf16 v[212:215], v[224:227], v[84:87], v[212:215]\n"
"buffer_load_dwordx4 v255, s[16:19], s36 offen offset:1024 lds\n"
"s_add_u32 s36, s36, s37\n"
"s_nop 0\n"
"v_mfma_f32_16x16x32_bf16 v[216:219], v[224:227], v[132:135], v[216:219]\n"
"v_mfma_f32_16x16x32_bf16 v[220:223], v[224:227], v[180:183], v[220:223]\n"
"s_cmp_eq_u32 s39, 2\n"
"s_cbranch_scc1 SW_OOL\n"
"SW_BACK:\n"
"s_waitcnt vmcnt(8) lgkmcnt(0)\n"
"s_barrier\n"
"s_sub_u32 s76, s75, s74\n"
"v_add_u32 v244, s76, v244\n"
"v_mfma_f32_16x16x32_bf16 v[208:211], v[228:231], v[40:43], v[208:211]\n"
"ds_read_b64_tr_b16 v[224:225], v244 offset:0\n"
"ds_read_b64_tr_b16 v[226:227], v244 offset:1024\n"
"v_mfma_f32_16x16x32_bf16 v[212:215], v[228:231], v[88:91], v[212:215]\n"
"v_mfma_f32_16x16x32_bf16 v[216:219], v[228:231], v[136:139], v[216:219]\n"
"v_mfma_f32_16x16x32_bf16 v[220:223], v[228:231], v[184:187], v[220:223]\n"
"v_mfma_f32_16x16x32_bf16 v[192:195], v[250:253], v[44:47], v[208:211]\n"
"ds_read_b64_tr_b16 v[228:229], v244 offset:4096\n"
"ds_read_b64_tr_b16 v[230:231], v244 offset:5120\n"
"v_mfma_f32_16x16x32_bf16 v[196:199], v[250:253], v[92:95], v[212:215]\n"
"v_mfma_f32_16x16x32_bf16 v[200:203], v[250:253], v[140:143], v[216:219]\n"
"v_mfma_f32_16x16x32_bf16 v[204:207], v[250:253], v[188:191], v[220:223]\n"
"s_mov_b32 s76, s74\n"
"s_mov_b32 s74, s75\n"
"s_mov_b32 s75, s80\n"
"s_mov_b32 s80, s76\n"
"s_sub_u32 s39, s39, 1\n"
"s_cmp_lg_u32 s39, 0\n"
"s_cbranch_scc1 P2LOOP\n"
"s_waitcnt lgkmcnt(2)\n"
"v_mfma_f32_16x16x32_bf16 v[208:211], v[224:227], v[0:3], 0\n"
"ds_read_b64_tr_b16 v[250:251], v244 offset:8192\n"
"ds_read_b64_tr_b16 v[252:253], v244 offset:9216\n"
"v_mfma_f32_16x16x32_bf16 v[212:215], v[224:227], v[48:51], 0\n"
"v_mfma_f32_16x16x32_bf16 v[216:219], v[224:227], v[96:99], 0\n"
"v_mfma_f32_16x16x32_bf16 v[220:223], v[224:227], v[144:147], 0\n"
"s_waitcnt lgkmcnt(2)\n"
"s_nop 0\n"
"v_mfma_f32_16x16x32_bf16 v[208:211], v[228:231], v[4:7], v[208:211]\n"
"ds_read_b64_tr_b16 v[224:225], v244 offset:12288\n"
"ds_read_b64_tr_b16 v[226:227], v244 offset:13312\n"
"v_cvt_pk_bf16_f32 v236, v192, v193\n"
"v_mfma_f32_16x16x32_bf16 v[212:215], v[228:231], v[52:55], v[212:215]\n"
"v_cvt_pk_bf16_f32 v237, v194, v195\n"
"v_mfma_f32_16x16x32_bf16 v[216:219], v[228:231], v[100:103], v[216:219]\n"
"v_cvt_pk_bf16_f32 v238, v196, v197\n"
"v_mfma_f32_16x16x32_bf16 v[220:223], v[228:231], v[148:151], v[220:223]\n"
"v_cvt_pk_bf16_f32 v239, v198, v199\n"
"v_and_b32 v234, 15, v248\n"
"v_lshlrev_b32 v234, 3, v234\n"
"s_lshl_b32 s76, s34, 7\n"
"v_add_u32 v234, s76, v234\n"
"global_load_dwordx2 v[232:233], v234, s[100:101] sc1\n"
"s_waitcnt lgkmcnt(2)\n"
"s_nop 0\n"
"v_mfma_f32_16x16x32_bf16 v[208:211], v[250:253], v[8:11], v[208:211]\n"
"ds_read_b64_tr_b16 v[228:229], v244 offset:16384\n"
"ds_read_b64_tr_b16 v[230:231], v244 offset:17408\n"
"v_cvt_pk_bf16_f32 v240, v200, v201\n"
"v_mfma_f32_16x16x32_bf16 v[212:215], v[250:253], v[56:59], v[212:215]\n"
"v_cvt_pk_bf16_f32 v241, v202, v203\n"
"v_mfma_f32_16x16x32_bf16 v[216:219], v[250:253], v[104:107], v[216:219]\n"
"v_cvt_pk_bf16_f32 v242, v204, v205\n"
"v_mfma_f32_16x16x32_bf16 v[220:223], v[250:253], v[152:155], v[220:223]\n"
"v_cvt_pk_bf16_f32 v243, v206, v207\n"
"s_waitcnt lgkmcnt(2)\n"
"s_nop 0\n"
"v_mfma_f32_16x16x32_bf16 v[208:211], v[224:227], v[12:15], v[208:211]\n"
"ds_read_b64_tr_b16 v[250:251], v244 offset:20480\n"
"ds_read_b64_tr_b16 v[252:253], v244 offset:21504\n"
"v_mfma_f32_16x16x32_bf16 v[212:215], v[224:227], v[60:63], v[212:215]\n"
"buffer_store_dwordx4 v[236:239], v249, s[20:23], s81 offen sc1\n"
"v_mfma_f32_16x16x32_bf16 v[216:219], v[224:227], v[108:111], v[216:219]\n"
"v_mfma_f32_16x16x32_bf16 v[220:223], v[224:227], v[156:159], v[220:223]\n"
"s_waitcnt lgkmcnt(2)\n"
"s_nop 0\n"
"v_mfma_f32_16x16x32_bf16 v[208:211], v[228:231], v[16:19], v[208:211]\n"
"ds_read_b64_tr_b16 v[224:225], v244 offset:24576\n"
"ds_read_b64_tr_b16 v[226:227], v244 offset:25600\n"
"v_mfma_f32_16x16x32_bf16 v[212:215], v[228:231], v[64:67], v[212:215]\n"
"v_mfma_f32_16x16x32_bf16 v[216:219], v[228:231], v[112:115], v[216:219]\n"
"v_mfma_f32_16x16x32_bf16 v[220:223], v[228:231], v[160:163], v[220:223]\n"
"s_waitcnt vmcnt(1)\n"
"s_mov_b32 s63, 0\n"
"MC_T:\n"
"v_cmp_ne_u32 vcc, s4, v232\n"
"s_nop 0\n"
"v_cmp_ne_u32 s[76:77], s5, v233\n"
"s_nop 3\n"
"s_or_b64 vcc, vcc, s[76:77]\n"
"s_cmp_eq_u64 vcc, 0\n"
"s_cbranch_scc1 MC_OK\n"
"s_add_u32 s63, s63, 1\n"
"s_nop 0\n"
"s_cmp_gt_u32 s63, 400000\n"
"s_cbranch_scc1 MC_OK\n"
"s_sleep 1\n"
"global_load_dwordx2 v[232:233], v234, s[100:101] sc1\n"
"s_waitcnt vmcnt(0)\n"
"s_branch MC_T\n"
"MC_OK:\n"
"s_waitcnt lgkmcnt(2)\n"
"s_nop 0\n"
"v_mfma_f32_16x16x32_bf16 v[208:211], v[250:253], v[20:23], v[208:211]\n"
"ds_read_b64_tr_b16 v[228:229], v244 offset:28672\n"
"ds_read_b64_tr_b16 v[230:231], v244 offset:29696\n"
"buffer_store_dwordx4 v[240:243], v247, s[20:23], s81 offen sc1\n"
"s_add_u32 s81, s81, 131072\n"
"v_mfma_f32_16x16x32_bf16 v[212:215], v[250:253], v[68:71], v[212:215]\n"
"v_mfma_f32_16x16x32_bf16 v[216:219], v[250:253], v[116:119], v[216:219]\n"
"s_mul_i32 s25, s35, 5\n"
"s_cmp_ge_u32 s25, 40\n"
"s_cselect_b32 s79, 32, 0\n"
"s_add_u32 s25, s25, s79\n"
"s_mul_i32 s26, s34, 144\n"
"s_add_u32 s25, s25, s26\n"
"s_lshl_b32 s25, s25, 14\n"
"s_lshl_b32 s26, s11, 11\n"
"s_add_u32 s25, s25, s26\n"
"s_add_u32 m0, s26, 0\n"
"s_nop 0\n"
"buffer_load_dwordx4 v249, s[20:23], s25 offen sc1 lds\n"
"v_mfma_f32_16x16x32_bf16 v[220:223], v[250:253], v[164:167], v[220:223]\n"
"s_waitcnt lgkmcnt(2)\n"
"s_nop 0\n"
"v_mfma_f32_16x16x32_bf16 v[208:211], v[224:227], v[24:27], v[208:211]\n"
"ds_read_b64_tr_b16 v[250:251], v244 offset:32768\n"
"ds_read_b64_tr_b16 v[252:253], v244 offset:33792\n"
"v_mfma_f32_16x16x32_bf16 v[212:215], v[224:227], v[72:75], v[212:215]\n"
"v_mfma_f32_16x16x32_bf16 v[216:219], v[224:227], v[120:123], v[216:219]\n"
"buffer_load_dwordx4 v249, s[20:23], s25 offen offset:1024 sc1 lds\n"
"v_mfma_f32_16x16x32_bf16 v[220:223], v[224:227], v[168:171], v[220:223]\n"
"s_waitcnt lgkmcnt(2)\n"
"s_nop 0\n"
"v_mfma_f32_16x16x32_bf16 v[208:211], v[228:231], v[28:31], v[208:211]\n"
"ds_read_b64_tr_b16 v[224:225], v244 offset:36864\n"
"ds_read_b64_tr_b16 v[226:227], v244 offset:37888\n"
"v_mfma_f32_16x16x32_bf16 v[212:215], v[228:231], v[76:79], v[212:215]\n"
"v_mfma_f32_16x16x32_bf16 v[216:219], v[228:231], v[124:127], v[216:219]\n"
"s_mul_i32 s25, s35, 5\n"
"s_add_u32 s25, s25, 1\n"
"s_cmp_ge_u32 s25, 40\n"
"s_cselect_b32 s79, 32, 0\n"
"s_add_u32 s25, s25, s79\n"
"s_nop 0\n"
"s_mul_i32 s26, s34, 144\n"
"s_add_u32 s25, s25, s26\n"
"s_lshl_b32 s25, s25, 14\n"
"s_lshl_b32 s26, s11, 11\n"
"s_add_u32 s25, s25, s26\n"
"s_add_u32 m0, s26, 16384\n"
"s_nop 0\n"
"s_nop 0\n"
"buffer_load_dwordx4 v249, s[20:23], s25 offen sc1 lds\n"
"v_mfma_f32_16x16x32_bf16 v[220:223], v[228:231], v[172:175], v[220:223]\n"
"s_waitcnt lgkmcnt(2)\n"
"s_nop 0\n"
"v_mfma_f32_16x16x32_bf16 v[208:211], v[250:253], v[32:35], v[208:211]\n"
"ds_read_b64_tr_b16 v[228:229], v244 offset:40960\n"
"ds_read_b64_tr_b16 v[230:231], v244 offset:41984\n"
"v_mfma_f32_16x16x32_bf16 v[212:215], v[250:253], v[80:83], v[212:215]\n"
"v_mfma_f32_16x16x32_bf16 v[216:219], v[250:253], v[128:131], v[216:219]\n"
"buffer_load_dwordx4 v249, s[20:23], s25 offen offset:1024 sc1 lds\n"
"v_mfma_f32_16x16x32_bf16 v[220:223], v[250:253], v[176:179], v[220:223]\n"
"s_waitcnt lgkmcnt(2)\n"
"s_nop 0\n"
"v_mfma_f32_16x16x32_bf16 v[208:211], v[224:227], v[36:39], v[208:211]\n"
"ds_read_b64_tr_b16 v[250:251], v244 offset:45056\n"
"ds_read_b64_tr_b16 v[252:253], v244 offset:46080\n"
"v_mfma_f32_16x16x32_bf16 v[212:215], v[224:227], v[84:87], v[212:215]\n"
"v_mfma_f32_16x16x32_bf16 v[216:219], v[224:227], v[132:135], v[216:219]\n"
"s_mul_i32 s25, s35, 5\n"
"s_add_u32 s25, s25, 2\n"
"s_cmp_ge_u32 s25, 40\n"
"s_cselect_b32 s79, 32, 0\n"
"s_add_u32 s25, s25, s79\n"
"s_nop 0\n"
"s_mul_i32 s26, s34, 144\n"
"s_add_u32 s25, s25, s26\n"
"s_lshl_b32 s25, s25, 14\n"
"s_lshl_b32 s26, s11, 11\n"
"s_add_u32 s25, s25, s26\n"
"s_add_u32 m0, s26, 32768\n"
"s_nop 0\n"
"s_nop 0\n"
"buffer_load_dwordx4 v249, s[20:23], s25 offen sc1 lds\n"
"v_mfma_f32_16x16x32_bf16 v[220:223], v[224:227], v[180:183], v[220:223]\n"
"s_waitcnt lgkmcnt(0)\n"
"s_barrier\n"
"s_sub_u32 s76, s75, s74\n"
"v_add_u32 v244, s76, v244\n"
"v_mfma_f32_16x16x32_bf16 v[208:211], v[228:231], v[40:43], v[208:211]\n"
"ds_read_b64_tr_b16 v[224:225], v244 offset:0\n"
"ds_read_b64_tr_b16 v[226:227], v244 offset:1024\n"
"v_mfma_f32_16x16x32_bf16 v[212:215], v[228:231], v[88:91], v[212:215]\n"
"v_mfma_f32_16x16x32_bf16 v[216:219], v[228:231], v[136:139], v[216:219]\n"
"buffer_load_dwordx4 v249, s[20:23], s25 offen offset:1024 sc1 lds\n"
"v_mfma_f32_16x16x32_bf16 v[220:223], v[228:231], v[184:187], v[220:223]\n"
"v_mfma_f32_16x16x32_bf16 v[192:195], v[250:253], v[44:47], v[208:211]\n"
"ds_read_b64_tr_b16 v[228:229], v244 offset:4096\n"
"ds_read_b64_tr_b16 v[230:231], v244 offset:5120\n"
"v_mfma_f32_16x16x32_bf16 v[196:199], v[250:253], v[92:95], v[212:215]\n"
"v_mfma_f32_16x16x32_bf16 v[200:203], v[250:253], v[140:143], v[216:219]\n"
"v_mfma_f32_16x16x32_bf16 v[204:207], v[250:253], v[188:191], v[220:223]\n"
"s_mov_b32 s76, s74\n"
"s_mov_b32 s74, s75\n"
"s_mov_b32 s75, s80\n"
"s_mov_b32 s80, s76\n"
"s_waitcnt lgkmcnt(2)\n"
"s_nop 0\n"
"v_mfma_f32_16x16x32_bf16 v[208:211], v[224:227], v[0:3], 0\n"
"ds_read_b64_tr_b16 v[250:251], v244 offset:8192\n"
"ds_read_b64_tr_b16 v[252:253], v244 offset:9216\n"
"v_mfma_f32_16x16x32_bf16 v[212:215], v[224:227], v[48:51], 0\n"
"v_mfma_f32_16x16x32_bf16 v[216:219], v[224:227], v[96:99], 0\n"
"s_mul_i32 s25, s35, 5\n"
"s_add_u32 s25, s25, 3\n"
"s_cmp_ge_u32 s25, 40\n"
"s_cselect_b32 s79, 32, 0\n"
"s_add_u32 s25, s25, s79\n"
"s_nop 0\n"
"s_mul_i32 s26, s34, 144\n"
"s_add_u32 s25, s25, s26\n"
"s_lshl_b32 s25, s25, 14\n"
"s_lshl_b32 s26, s11, 11\n"
"s_add_u32 s25, s25, s26\n"
"s_add_u32 m0, s26, 65536\n"
"s_nop 0\n"
"s_nop 0\n"
"buffer_load_dwordx4 v249, s[20:23], s25 offen sc1 lds\n"
"v_mfma_f32_16x16x32_bf16 v[220:223], v[224:227], v[144:147], 0\n"
"s_waitcnt lgkmcnt(2)\n"
"s_nop 0\n"
"v_mfma_f32_16x16x32_bf16 v[208:211], v[228:231], v[4:7], v[208:211]\n"
"ds_read_b64_tr_b16 v[224:225], v244 offset:12288\n"
"ds_read_b64_tr_b16 v[226:227], v244 offset:13312\n"
"v_cvt_pk_bf16_f32 v236, v192, v193\n"
"v_mfma_f32_16x16x32_bf16 v[212:215], v[228:231], v[52:55], v[212:215]\n"
"v_cvt_pk_bf16_f32 v237, v194, v195\n"
"v_mfma_f32_16x16x32_bf16 v[216:219], v[228:231], v[100:103], v[216:219]\n"
"v_cvt_pk_bf16_f32 v238, v196, v197\n"
"v_mfma_f32_16x16x32_bf16 v[220:223], v[228:231], v[148:151], v[220:223]\n"
"v_cvt_pk_bf16_f32 v239, v198, v199\n"
"s_waitcnt lgkmcnt(2)\n"
"s_nop 0\n"
"v_mfma_f32_16x16x32_bf16 v[208:211], v[250:253], v[8:11], v[208:211]\n"
"ds_read_b64_tr_b16 v[228:229], v244 offset:16384\n"
"ds_read_b64_tr_b16 v[230:231], v244 offset:17408\n"
"buffer_load_dwordx4 v249, s[20:23], s25 offen offset:1024 sc1 lds\n"
"v_cvt_pk_bf16_f32 v240, v200, v201\n"
"v_mfma_f32_16x16x32_bf16 v[212:215], v[250:253], v[56:59], v[212:215]\n"
"v_cvt_pk_bf16_f32 v241, v202, v203\n"
"v_mfma_f32_16x16x32_bf16 v[216:219], v[250:253], v[104:107], v[216:219]\n"
"v_cvt_pk_bf16_f32 v242, v204, v205\n"
"v_mfma_f32_16x16x32_bf16 v[220:223], v[250:253], v[152:155], v[220:223]\n"
"v_cvt_pk_bf16_f32 v243, v206, v207\n"
"s_waitcnt lgkmcnt(2)\n"
"s_nop 0\n"
"v_mfma_f32_16x16x32_bf16 v[208:211], v[224:227], v[12:15], v[208:211]\n"
"ds_read_b64_tr_b16 v[250:251], v244 offset:20480\n"
"ds_read_b64_tr_b16 v[252:253], v244 offset:21504\n"
"v_mfma_f32_16x16x32_bf16 v[212:215], v[224:227], v[60:63], v[212:215]\n"
"buffer_store_dwordx4 v[236:239], v249, s[20:23], s81 offen sc1\n"
"v_mfma_f32_16x16x32_bf16 v[216:219], v[224:227], v[108:111], v[216:219]\n"
"s_mul_i32 s25, s35, 5\n"
"s_add_u32 s25, s25, 4\n"
"s_cmp_ge_u32 s25, 40\n"
"s_cselect_b32 s79, 32, 0\n"
"s_add_u32 s25, s25, s79\n"
"s_nop 0\n"
"s_mul_i32 s26, s34, 144\n"
"s_add_u32 s25, s25, s26\n"
"s_lshl_b32 s25, s25, 14\n"
"s_lshl_b32 s26, s11, 11\n"
"s_add_u32 s25, s25, s26\n"
"s_add_u32 m0, s26, 81920\n"
"s_nop 0\n"
"s_nop 0\n"
"buffer_load_dwordx4 v249, s[20:23], s25 offen sc1 lds\n"
"v_mfma_f32_16x16x32_bf16 v[220:223], v[224:227], v[156:159], v[220:223]\n"
"s_waitcnt lgkmcnt(2)\n"
"s_nop 0\n"
"v_mfma_f32_16x16x32_bf16 v[208:211], v[228:231], v[16:19], v[208:211]\n"
"ds_read_b64_tr_b16 v[224:225], v244 offset:24576\n"
"ds_read_b64_tr_b16 v[226:227], v244 offset:25600\n"
"v_mfma_f32_16x16x32_bf16 v[212:215], v[228:231], v[64:67], v[212:215]\n"
"v_mfma_f32_16x16x32_bf16 v[216:219], v[228:231], v[112:115], v[216:219]\n"
"v_mfma_f32_16x16x32_bf16 v[220:223], v[228:231], v[160:163], v[220:223]\n"
"s_waitcnt lgkmcnt(2)\n"
"s_nop 0\n"
"v_mfma_f32_16x16x32_bf16 v[208:211], v[250:253], v[20:23], v[208:211]\n"
"ds_read_b64_tr_b16 v[228:229], v244 offset:28672\n"
"ds_read_b64_tr_b16 v[230:231], v244 offset:29696\n"
"buffer_load_dwordx4 v249, s[20:23], s25 offen offset:1024 sc1 lds\n"
"buffer_store_dwordx4 v[240:243], v247, s[20:23], s81 offen sc1\n"
"s_add_u32 s81, s81, 131072\n"
"v_mfma_f32_16x16x32_bf16 v[212:215], v[250:253], v[68:71], v[212:215]\n"
"v_mfma_f32_16x16x32_bf16 v[216:219], v[250:253], v[116:119], v[216:219]\n"
"v_mfma_f32_16x16x32_bf16 v[220:223], v[250:253], v[164:167], v[220:223]\n"
"s_waitcnt lgkmcnt(2)\n"
"s_nop 0\n"
"v_mfma_f32_16x16x32_bf16 v[208:211], v[224:227], v[24:27], v[208:211]\n"
"ds_read_b64_tr_b16 v[250:251], v244 offset:32768\n"
"ds_read_b64_tr_b16 v[252:253], v244 offset:33792\n"
"v_mfma_f32_16x16x32_bf16 v[212:215], v[224:227], v[72:75], v[212:215]\n"
"v_mfma_f32_16x16x32_bf16 v[216:219], v[224:227], v[120:123], v[216:219]\n"
"v_mfma_f32_16x16x32_bf16 v[220:223], v[224:227], v[168:171], v[220:223]\n"
"s_waitcnt lgkmcnt(2)\n"
"s_nop 0\n"
"v_mfma_f32_16x16x32_bf16 v[208:211], v[228:231], v[28:31], v[208:211]\n"
"ds_read_b64_tr_b16 v[224:225], v244 offset:36864\n"
"ds_read_b64_tr_b16 v[226:227], v244 offset:37888\n"
"v_mfma_f32_16x16x32_bf16 v[212:215], v[228:231], v[76:79], v[212:215]\n"
"v_mfma_f32_16x16x32_bf16 v[216:219], v[228:231], v[124:127], v[216:219]\n"
"v_mfma_f32_16x16x32_bf16 v[220:223], v[228:231], v[172:175], v[220:223]\n"
"s_waitcnt lgkmcnt(2)\n"
"s_nop 0\n"
"v_mfma_f32_16x16x32_bf16 v[208:211], v[250:253], v[32:35], v[208:211]\n"
"ds_read_b64_tr_b16 v[228:229], v244 offset:40960\n"
"ds_read_b64_tr_b16 v[230:231], v244 offset:41984\n"
"v_mfma_f32_16x16x32_bf16 v[212:215], v[250:253], v[80:83], v[212:215]\n"
"v_mfma_f32_16x16x32_bf16 v[216:219], v[250:253], v[128:131], v[216:219]\n"
"v_mfma_f32_16x16x32_bf16 v[220:223], v[250:253], v[176:179], v[220:223]\n"
"s_waitcnt lgkmcnt(2)\n"
"s_nop 0\n"
"v_mfma_f32_16x16x32_bf16 v[208:211], v[224:227], v[36:39], v[208:211]\n"
"ds_read_b64_tr_b16 v[250:251], v244 offset:45056\n"
"ds_read_b64_tr_b16 v[252:253], v244 offset:46080\n"
"v_mfma_f32_16x16x32_bf16 v[212:215], v[224:227], v[84:87], v[212:215]\n"
"v_mfma_f32_16x16x32_bf16 v[216:219], v[224:227], v[132:135], v[216:219]\n"
"v_mfma_f32_16x16x32_bf16 v[220:223], v[224:227], v[180:183], v[220:223]\n"
"s_waitcnt lgkmcnt(2)\n"
"s_nop 0\n"
"v_mfma_f32_16x16x32_bf16 v[208:211], v[228:231], v[40:43], v[208:211]\n"
"v_mfma_f32_16x16x32_bf16 v[212:215], v[228:231], v[88:91], v[212:215]\n"
"v_mfma_f32_16x16x32_bf16 v[216:219], v[228:231], v[136:139], v[216:219]\n"
"v_mfma_f32_16x16x32_bf16 v[220:223], v[228:231], v[184:187], v[220:223]\n"
"s_waitcnt lgkmcnt(0)\n"
"s_nop 0\n"
"v_mfma_f32_16x16x32_bf16 v[192:195], v[250:253], v[44:47], v[208:211]\n"
"v_mfma_f32_16x16x32_bf16 v[196:199], v[250:253], v[92:95], v[212:215]\n"
"v_mfma_f32_16x16x32_bf16 v[200:203], v[250:253], v[140:143], v[216:219]\n"
"v_mfma_f32_16x16x32_bf16 v[204:207], v[250:253], v[188:191], v[220:223]\n"
"s_nop 7\n"
"s_nop 0\n"
"v_cvt_pk_bf16_f32 v236, v192, v193\n"
"v_cvt_pk_bf16_f32 v237, v194, v195\n"
"v_cvt_pk_bf16_f32 v238, v196, v197\n"
"v_cvt_pk_bf16_f32 v239, v198, v199\n"
"v_cvt_pk_bf16_f32 v240, v200, v201\n"
"v_cvt_pk_bf16_f32 v241, v202, v203\n"
"v_cvt_pk_bf16_f32 v242, v204, v205\n"
"v_cvt_pk_bf16_f32 v243, v206, v207\n"
"buffer_store_dwordx4 v[236:239], v249, s[20:23], s81 offen sc1\n"
"buffer_store_dwordx4 v[240:243], v247, s[20:23], s81 offen sc1\n"
"s_add_u32 s81, s81, 131072\n"
"s_waitcnt vmcnt(2)\n"
"s_barrier\n"
"s_cmp_lg_u32 s11, 6\n"
"s_cbranch_scc1 ML_SKIP\n"
"s_lshl_b32 s76, s34, 4\n"
"s_add_u32 s76, s76, s35\n"
"s_lshl_b32 s76, s76, 3\n"
"v_mov_b32 v232, s4\n"
"v_mov_b32 v233, s5\n"
"v_mov_b32 v234, s76\n"
"s_mov_b64 exec, 1\n"
"s_nop 0\n"
"global_store_dwordx2 v234, v[232:233], s[100:101] offset:2048 sc1\n"
"s_mov_b64 exec, -1\n"
"ML_SKIP:\n"
"s_mov_b32 s12, 0\n"
"s_mov_b32 s13, 16384\n"
"s_mov_b32 s14, 32768\n"
"s_mov_b32 s15, 65536\n"
"s_mov_b32 s24, 81920\n"
"s_mov_b32 s6, 51200\n"
"s_branch TAIL\n"
"SW_OOL:\n"
"s_waitcnt vmcnt(3)\n"
"s_branch SW_BACK\n"
"MP_OOL:\n"
"s_cmp_lg_u32 s11, 0\n"
"s_cbranch_scc1 MP_SKIP\n"
"s_lshl_b32 s76, s34, 4\n"
"s_add_u32 s76, s76, s35\n"
"s_lshl_b32 s76, s76, 3\n"
"v_mov_b32 v232, s4\n"
"v_mov_b32 v233, s5\n"
"v_mov_b32 v234, s76\n"
"s_mov_b64 exec, 1\n"
"global_store_dwordx2 v234, v[232:233], s[100:101] sc1\n"
"s_mov_b64 exec, -1\n"
"MP_SKIP:\n"
"s_branch MP_BACK\n"
".p2align 6\n"
"SSM_PATH:\n"
"s_mov_b32 s82, s76\n"
"s_add_u32 s35, s82, 12\n"
"v_min_u32 v0, 143, v231\n"
"s_lshl_b32 s76, s82, 7\n"
"s_lshl_b32 s77, s82, 4\n"
"s_add_u32 s77, s77, 384\n"
"v_add_u32 v1, s76, v0\n"
"v_add_u32 v2, s77, v0\n"
"v_cmp_gt_u32 vcc, 128, v0\n"
"s_nop 1\n"
"v_cndmask_b32 v1, v2, v1, vcc\n"
"v_lshlrev_b32_e64 v1, 1, v1\n"
"global_load_ushort v4, v1, s[46:47]\n"
"global_load_ushort v5, v1, s[48:49]\n"
"global_load_ushort v6, v1, s[58:59]\n"
"s_mov_b32 s12, s44\n"
"s_nop 0\n"
"s_and_b32 s13, s45, 0xffff\n"
"s_mov_b32 s14, 2359296\n"
"s_mov_b32 s15, 0x20000\n"
"v_lshrrev_b32 v232, 3, v248\n"
"v_and_b32 v233, 7, v248\n"
"v_mul_u32_u24 v234, 1152, v232\n"
"v_lshl_add_u32 v234, v233, 4, v234\n"
"s_lshl_b32 s76, s34, 7\n"
"s_lshl_b32 s77, s11, 4\n"
"s_add_u32 s76, s76, s77\n"
"s_nop 0\n"
"s_mul_i32 s2, s76, 1152\n"
"s_add_u32 s3, s2, 9216\n"
"buffer_load_dwordx4 v[144:147], v234, s[12:15], s2 offen offset:0\n"
"buffer_load_dwordx4 v[148:151], v234, s[12:15], s2 offen offset:128\n"
"buffer_load_dwordx4 v[152:155], v234, s[12:15], s2 offen offset:256\n"
"buffer_load_dwordx4 v[156:159], v234, s[12:15], s2 offen offset:384\n"
"buffer_load_dwordx4 v[160:163], v234, s[12:15], s2 offen offset:512\n"
"buffer_load_dwordx4 v[184:187], v234, s[12:15], s2 offen offset:640\n"
"buffer_load_dwordx4 v[188:191], v234, s[12:15], s2 offen offset:768\n"
"buffer_load_dwordx4 v[192:195], v234, s[12:15], s2 offen offset:896\n"
"buffer_load_dwordx4 v[196:199], v234, s[12:15], s2 offen offset:1024\n"
"s_mov_b32 s41, 0xffff0000\n"
"s_mov_b32 s42, 0x3f803f80\n"
"s_and_b32 s76, s11, 1\n"
"s_cmp_eq_u32 s76, 1\n"
"s_cselect_b64 s[50:51], s[52:53], s[50:51]\n"
"s_cmp_ge_u32 s11, 4\n"
"s_cselect_b64 s[24:25], s[56:57], s[54:55]\n"
"s_nop 0\n"
"s_and_b32 s25, s25, 0xffff\n"
"s_mov_b32 s26, 663552\n"
"s_mov_b32 s27, 0x20000\n"
"s_mov_b32 s16, s50\n"
"s_nop 0\n"
"s_and_b32 s17, s51, 0xffff\n"
"s_mov_b32 s18, 663552\n"
"s_mov_b32 s19, 0x20000\n"
"s_mov_b32 s20, s72\n"
"s_nop 0\n"
"s_and_b32 s21, s73, 0xffff\n"
"s_mov_b32 s22, 37748736\n"
"s_mov_b32 s23, 0x20000\n"
"s_lshr_b32 s76, s11, 2\n"
"s_nop 0\n"
"s_bfe_u32 s77, s11, 0x10001\n"
"s_mul_i32 s78, s76, 5\n"
"s_add_u32 s78, s78, s77\n"
"s_lshl_b32 s79, s78, 4\n"
"s_lshl_b32 s64, s82, 7\n"
"s_add_u32 s79, s79, s64\n"
"s_lshl_b32 s64, s82, 4\n"
"s_add_u32 s64, s64, 512\n"
"s_cmp_eq_u32 s78, 8\n"
"s_cselect_b32 s79, s64, s79\n"
"s_mul_i32 s64, s79, 1152\n"
"s_add_u32 s78, s78, 2\n"
"s_lshl_b32 s79, s78, 4\n"
"s_lshl_b32 s65, s82, 7\n"
"s_add_u32 s79, s79, s65\n"
"s_lshl_b32 s65, s82, 4\n"
"s_nop 0\n"
"s_add_u32 s65, s65, 512\n"
"s_cmp_eq_u32 s78, 8\n"
"s_cselect_b32 s79, s65, s79\n"
"s_mul_i32 s65, s79, 1152\n"
"s_add_u32 s78, s78, 2\n"
"s_lshl_b32 s79, s82, 7\n"
"s_add_u32 s79, s79, 64\n"
"s_nop 0\n"
"s_mul_i32 s79, s79, 1152\n"
"s_lshr_b32 s78, s11, 1\n"
"s_lshl_b32 s78, s78, 7\n"
"s_add_u32 s79, s79, s78\n"
"s_cmp_lt_u32 s11, 6\n"
"s_cselect_b32 s66, s79, 0x40000000\n"
"v_lshrrev_b32 v236, 1, v232\n"
"v_xor_b32_e64 v236, v236, v233\n"
"v_mul_u32_u24 v235, 1152, v232\n"
"v_lshl_add_u32 v254, v236, 4, v235\n"
"v_xor_b32_e64 v236, 4, v236\n"
"v_lshl_add_u32 v255, v236, 4, v235\n"
"v_add_u32 v255, 8192, v255\n"
"s_lshl_b32 s43, s11, 11\n"
"v_lshrrev_b32 v235, 2, v233\n"
"v_lshlrev_b32 v246, 10, v235\n"
"v_and_b32 v235, 3, v233\n"
"v_lshl_add_u32 v246, v235, 8, v246\n"
"s_and_b32 s79, s11, 1\n"
"s_lshl_b32 s79, s79, 3\n"
"v_and_b32 v235, 3, v232\n"
"v_or_b32 v235, s79, v235\n"
"v_xor_b32_e64 v235, v235, v233\n"
"v_lshl_add_u32 v246, v235, 4, v246\n"
"v_lshrrev_b32_e64 v235, 2, v232\n"
"v_mul_u32_u24 v235, 10240, v235\n"
"v_add_u32 v246, v235, v246\n"
"s_lshr_b32 s78, s11, 1\n"
"s_mul_i32 s78, s78, 20480\n"
"s_add_u32 s78, s78, 65536\n"
"v_add_u32 v246, s78, v246\n"
"v_xor_b32 v247, 64, v246\n"
"v_lshlrev_b32 v230, 3, v232\n"
"s_lshl_b32 s78, s11, 7\n"
"v_add_u32 v230, s78, v230\n"
"v_lshrrev_b32 v237, 4, v248\n"
"v_and_b32 v238, 15, v248\n"
"s_and_b32 s78, s11, 3\n"
"s_mul_i32 s79, s78, 20480\n"
"s_add_u32 s79, s79, 65536\n"
"v_xor_b32 v244, v238, v237\n"
"v_xor_b32 v245, 4, v244\n"
"v_lshlrev_b32_e64 v244, 4, v244\n"
"v_lshl_add_u32 v244, v237, 8, v244\n"
"v_add_u32 v244, s79, v244\n"
"v_lshlrev_b32 v245, 4, v245\n"
"v_lshl_add_u32 v245, v237, 8, v245\n"
"v_add_u32 v245, s79, v245\n"
"v_lshrrev_b32 v235, 1, v238\n"
"v_xor_b32 v235, v235, v237\n"
"v_lshlrev_b32 v235, 4, v235\n"
"v_lshl_add_u32 v235, v238, 7, v235\n"
"s_lshr_b32 s79, s11, 2\n"
"s_lshl_b32 s76, s79, 13\n"
"v_add_u32 v249, s76, v235\n"
"v_xor_b32 v250, 64, v249\n"
"buffer_load_dwordx4 v[164:167], v234, s[12:15], s3 offen offset:0\n"
"buffer_load_dwordx4 v[168:171], v234, s[12:15], s3 offen offset:128\n"
"buffer_load_dwordx4 v[172:175], v234, s[12:15], s3 offen offset:256\n"
"buffer_load_dwordx4 v[176:179], v234, s[12:15], s3 offen offset:384\n"
"buffer_load_dwordx4 v[180:183], v234, s[12:15], s3 offen offset:512\n"
"buffer_load_dwordx4 v[200:203], v234, s[12:15], s3 offen offset:640\n"
"buffer_load_dwordx4 v[204:207], v234, s[12:15], s3 offen offset:768\n"
"buffer_load_dwordx4 v[208:211], v234, s[12:15], s3 offen offset:896\n"
"buffer_load_dwordx4 v[212:215], v234, s[12:15], s3 offen offset:1024\n"
"s_waitcnt vmcnt(18)\n"
"v_lshlrev_b32 v4, 16, v4\n"
"v_lshlrev_b32 v5, 16, v5\n"
"v_lshlrev_b32 v6, 16, v6\n"
"v_mul_f32 v7, 0x3fb8aa3b, v6\n"
"v_exp_f32 v7, v7\n"
"s_nop 0\n"
"v_cvt_pk_bf16_f32 v7, v7, v7\n"
"v_lshlrev_b32 v7, 16, v7\n"
"v_mul_f32 v8, v7, v4\n"
"v_mul_f32_e64 v9, v7, v5\n"
"v_mul_f32 v8, 0x3fb8aa3b, v8\n"
"v_exp_f32 v10, v8\n"
"v_mul_f32 v9, 0x3e22f983, v9\n"
"v_rndne_f32 v8, v9\n"
"v_sub_f32 v9, v9, v8\n"
"v_sin_f32 v11, v9\n"
"v_cos_f32 v12, v9\n"
"s_nop 0\n"
"v_mul_f32 v17, v10, v11\n"
"v_mul_f32 v16, v10, v12\n"
"v_add_f32 v13, -1.0, v16\n"
"v_mul_f32 v14, v4, v4\n"
"v_fmac_f32 v14, v5, v5\n"
"v_rcp_f32 v14, v14\n"
"v_mul_f32 v18, v13, v4\n"
"v_fmac_f32 v18, v17, v5\n"
"v_mul_f32 v19, v17, v4\n"
"v_fma_f32 v19, -v13, v5, v19\n"
"v_add_f32 v14, v14, v14\n"
"v_mul_f32 v18, v18, v14\n"
"v_mul_f32 v19, v19, v14\n"
"v_lshlrev_b32 v15, 4, v0\n"
"ds_write_b128 v15, v[16:19] offset:49152\n"
"v_mul_f32_e64 v24, v16, v16\n"
"v_fma_f32 v24, -v17, v17, v24\n"
"v_mul_f32_e64 v25, v16, v17\n"
"v_fma_f32 v21, v17, v16, v25\n"
"v_mov_b32 v20, v24\n"
"v_mul_f32 v24, v20, v20\n"
"v_fma_f32 v24, -v21, v21, v24\n"
"v_mul_f32_e64 v25, v20, v21\n"
"v_fma_f32 v21, v21, v20, v25\n"
"v_mov_b32 v20, v24\n"
"v_mul_f32 v24, v20, v20\n"
"v_fma_f32 v24, -v21, v21, v24\n"
"v_mul_f32_e64 v25, v20, v21\n"
"v_fma_f32 v21, v21, v20, v25\n"
"v_mov_b32 v20, v24\n"
"v_mul_f32 v24, v20, v20\n"
"v_fma_f32 v24, -v21, v21, v24\n"
"v_mul_f32_e64 v25, v20, v21\n"
"v_fma_f32 v23, v21, v20, v25\n"
"v_mov_b32 v22, v24\n"
"v_mul_f32 v24, v22, v22\n"
"v_fma_f32 v24, -v23, v23, v24\n"
"v_mul_f32_e64 v25, v22, v23\n"
"v_fma_f32 v23, v23, v22, v25\n"
"v_mov_b32 v22, v24\n"
"v_mul_f32 v24, v22, v22\n"
"v_fma_f32 v24, -v23, v23, v24\n"
"v_mul_f32_e64 v25, v22, v23\n"
"v_fma_f32 v23, v23, v22, v25\n"
"v_mov_b32 v22, v24\n"
"v_mul_f32 v24, v22, v22\n"
"v_fma_f32 v24, -v23, v23, v24\n"
"v_mul_f32_e64 v25, v22, v23\n"
"v_fma_f32 v23, v23, v22, v25\n"
"v_mov_b32_e64 v22, v24\n"
"ds_write_b128 v15, v[20:23] offset:51456\n"
"s_waitcnt vmcnt(9)\n"
"v_mov_b32 v224, 0\n"
"v_mov_b32 v225, 0\n"
"v_mov_b32 v226, 0\n"
"v_mov_b32 v227, 0\n"
"v_dot2c_f32_bf16 v224, s42, v144\n"
"v_dot2c_f32_bf16 v226, v144, v144\n"
"v_dot2c_f32_bf16 v225, s42, v145\n"
"v_dot2c_f32_bf16 v227, v145, v145\n"
"v_dot2c_f32_bf16 v224, s42, v146\n"
"v_dot2c_f32_bf16 v226, v146, v146\n"
"v_dot2c_f32_bf16 v225, s42, v147\n"
"v_dot2c_f32_bf16 v227, v147, v147\n"
"v_dot2c_f32_bf16 v224, s42, v148\n"
"v_dot2c_f32_bf16 v226, v148, v148\n"
"v_dot2c_f32_bf16 v225, s42, v149\n"
"v_dot2c_f32_bf16 v227, v149, v149\n"
"v_dot2c_f32_bf16 v224, s42, v150\n"
"v_dot2c_f32_bf16 v226, v150, v150\n"
"v_dot2c_f32_bf16 v225, s42, v151\n"
"v_dot2c_f32_bf16 v227, v151, v151\n"
"v_dot2c_f32_bf16 v224, s42, v152\n"
"v_dot2c_f32_bf16 v226, v152, v152\n"
"v_dot2c_f32_bf16 v225, s42, v153\n"
"v_dot2c_f32_bf16 v227, v153, v153\n"
"v_dot2c_f32_bf16 v224, s42, v154\n"
"v_dot2c_f32_bf16 v226, v154, v154\n"
"v_dot2c_f32_bf16 v225, s42, v155\n"
"v_dot2c_f32_bf16 v227, v155, v155\n"
"v_dot2c_f32_bf16 v224, s42, v156\n"
"v_dot2c_f32_bf16 v226, v156, v156\n"
"v_dot2c_f32_bf16 v225, s42, v157\n"
"v_dot2c_f32_bf16 v227, v157, v157\n"
"v_dot2c_f32_bf16 v224, s42, v158\n"
"v_dot2c_f32_bf16 v226, v158, v158\n"
"v_dot2c_f32_bf16 v225, s42, v159\n"
"v_dot2c_f32_bf16 v227, v159, v159\n"
"v_dot2c_f32_bf16 v224, s42, v160\n"
"v_dot2c_f32_bf16 v226, v160, v160\n"
"v_dot2c_f32_bf16 v225, s42, v161\n"
"v_dot2c_f32_bf16 v227, v161, v161\n"
"v_dot2c_f32_bf16 v224, s42, v162\n"
"v_dot2c_f32_bf16 v226, v162, v162\n"
"v_dot2c_f32_bf16 v225, s42, v163\n"
"v_dot2c_f32_bf16 v227, v163, v163\n"
"v_dot2c_f32_bf16 v224, s42, v184\n"
"v_dot2c_f32_bf16 v226, v184, v184\n"
"v_dot2c_f32_bf16 v225, s42, v185\n"
"v_dot2c_f32_bf16 v227, v185, v185\n"
"v_dot2c_f32_bf16 v224, s42, v186\n"
"v_dot2c_f32_bf16 v226, v186, v186\n"
"v_dot2c_f32_bf16 v225, s42, v187\n"
"v_dot2c_f32_bf16 v227, v187, v187\n"
"v_dot2c_f32_bf16 v224, s42, v188\n"
"v_dot2c_f32_bf16 v226, v188, v188\n"
"v_dot2c_f32_bf16 v225, s42, v189\n"
"v_dot2c_f32_bf16 v227, v189, v189\n"
"v_dot2c_f32_bf16 v224, s42, v190\n"
"v_dot2c_f32_bf16 v226, v190, v190\n"
"v_dot2c_f32_bf16 v225, s42, v191\n"
"v_dot2c_f32_bf16 v227, v191, v191\n"
"v_dot2c_f32_bf16 v224, s42, v192\n"
"v_dot2c_f32_bf16 v226, v192, v192\n"
"v_dot2c_f32_bf16 v225, s42, v193\n"
"v_dot2c_f32_bf16 v227, v193, v193\n"
"v_dot2c_f32_bf16 v224, s42, v194\n"
"v_dot2c_f32_bf16 v226, v194, v194\n"
"v_dot2c_f32_bf16 v225, s42, v195\n"
"v_dot2c_f32_bf16 v227, v195, v195\n"
"v_dot2c_f32_bf16 v224, s42, v196\n"
"v_dot2c_f32_bf16 v226, v196, v196\n"
"v_dot2c_f32_bf16 v225, s42, v197\n"
"v_dot2c_f32_bf16 v227, v197, v197\n"
"v_dot2c_f32_bf16 v224, s42, v198\n"
"v_dot2c_f32_bf16 v226, v198, v198\n"
"v_dot2c_f32_bf16 v225, s42, v199\n"
"v_dot2c_f32_bf16 v227, v199, v199\n"
"s_nop 2\n"
"v_add_f32 v224, v224, v225\n"
"v_add_f32 v226, v226, v227\n"
"s_nop 1\n"
"s_nop 0\n"
"v_add_f32_dpp v224, v224, v224 quad_perm:[1,0,3,2] row_mask:0xf bank_mask:0xf\n"
"v_add_f32_dpp v226, v226, v226 quad_perm:[1,0,3,2] row_mask:0xf bank_mask:0xf\n"
"s_nop 1\n"
"s_nop 0\n"
"v_add_f32_dpp v224, v224, v224 quad_perm:[2,3,0,1] row_mask:0xf bank_mask:0xf\n"
"v_add_f32_dpp v226, v226, v226 quad_perm:[2,3,0,1] row_mask:0xf bank_mask:0xf\n"
"s_nop 1\n"
"s_nop 0\n"
"v_add_f32_dpp v224, v224, v224 row_half_mirror row_mask:0xf bank_mask:0xf\n"
"v_add_f32_dpp v226, v226, v226 row_half_mirror row_mask:0xf bank_mask:0xf\n"
"v_mul_f32 v224, 0x3ae38e39, v224\n"
"v_mul_f32 v226, 0x3ae38e39, v226\n"
"v_fma_f32 v226, -v224, v224, v226\n"
"v_max_f32_e64 v226, 0, v226\n"
"v_add_f32 v227, 0x3727c5ac, v226\n"
"v_rsq_f32 v227, v227\n"
"v_mov_b32 v228, v224\n"
"v_mov_b32_e64 v229, v227\n"
"ds_write_b64 v230, v[228:229] offset:53760\n"
"v_cvt_pk_bf16_f32 v217, v227, v227\n"
"v_and_b32 v216, 0xffff, v217\n"
"v_and_b32 v217, s41, v217\n"
"v_mul_f32 v218, v224, v217\n"
"v_sub_f32_e64 v218, 0, v218\n"
"v_dot2_f32_bf16 v240, v144, v216, v218\n"
"v_dot2_f32_bf16 v241, v144, v217, v218\n"
"v_dot2_f32_bf16 v242, v145, v216, v218\n"
"v_dot2_f32_bf16 v243, v145, v217, v218\n"
"s_nop 1\n"
"s_nop 0\n"
"v_cvt_pk_bf16_f32 v144, v240, v241\n"
"v_cvt_pk_bf16_f32 v145, v242, v243\n"
"v_dot2_f32_bf16 v240, v146, v216, v218\n"
"v_dot2_f32_bf16 v241, v146, v217, v218\n"
"v_dot2_f32_bf16 v242, v147, v216, v218\n"
"v_dot2_f32_bf16 v243, v147, v217, v218\n"
"s_nop 1\n"
"s_nop 0\n"
"v_cvt_pk_bf16_f32 v146, v240, v241\n"
"v_cvt_pk_bf16_f32 v147, v242, v243\n"
"ds_write_b128 v246, v[144:147] offset:0\n"
"v_dot2_f32_bf16 v240, v148, v216, v218\n"
"v_dot2_f32_bf16 v241, v148, v217, v218\n"
"v_dot2_f32_bf16 v242, v149, v216, v218\n"
"v_dot2_f32_bf16 v243, v149, v217, v218\n"
"s_nop 1\n"
"s_nop 0\n"
"v_cvt_pk_bf16_f32 v148, v240, v241\n"
"v_cvt_pk_bf16_f32 v149, v242, v243\n"
"v_dot2_f32_bf16 v240, v150, v216, v218\n"
"v_dot2_f32_bf16 v241, v150, v217, v218\n"
"v_dot2_f32_bf16 v242, v151, v216, v218\n"
"v_dot2_f32_bf16 v243, v151, v217, v218\n"
"s_nop 1\n"
"s_nop 0\n"
"v_cvt_pk_bf16_f32 v150, v240, v241\n"
"v_cvt_pk_bf16_f32 v151, v242, v243\n"
"ds_write_b128 v246, v[148:151] offset:2048\n"
"v_dot2_f32_bf16 v240, v152, v216, v218\n"
"v_dot2_f32_bf16 v241, v152, v217, v218\n"
"v_dot2_f32_bf16 v242, v153, v216, v218\n"
"v_dot2_f32_bf16 v243, v153, v217, v218\n"
"s_nop 1\n"
"s_nop 0\n"
"v_cvt_pk_bf16_f32 v152, v240, v241\n"
"v_cvt_pk_bf16_f32 v153, v242, v243\n"
"v_dot2_f32_bf16 v240, v154, v216, v218\n"
"v_dot2_f32_bf16 v241, v154, v217, v218\n"
"v_dot2_f32_bf16 v242, v155, v216, v218\n"
"v_dot2_f32_bf16 v243, v155, v217, v218\n"
"s_nop 1\n"
"s_nop 0\n"
"v_cvt_pk_bf16_f32 v154, v240, v241\n"
"v_cvt_pk_bf16_f32 v155, v242, v243\n"
"ds_write_b128 v246, v[152:155] offset:4096\n"
"v_dot2_f32_bf16 v240, v156, v216, v218\n"
"v_dot2_f32_bf16 v241, v156, v217, v218\n"
"v_dot2_f32_bf16 v242, v157, v216, v218\n"
"v_dot2_f32_bf16 v243, v157, v217, v218\n"
"s_nop 1\n"
"s_nop 0\n"
"v_cvt_pk_bf16_f32 v156, v240, v241\n"
"v_cvt_pk_bf16_f32 v157, v242, v243\n"
"v_dot2_f32_bf16 v240, v158, v216, v218\n"
"v_dot2_f32_bf16 v241, v158, v217, v218\n"
"v_dot2_f32_bf16 v242, v159, v216, v218\n"
"v_dot2_f32_bf16 v243, v159, v217, v218\n"
"s_nop 1\n"
"s_nop 0\n"
"v_cvt_pk_bf16_f32 v158, v240, v241\n"
"v_cvt_pk_bf16_f32 v159, v242, v243\n"
"ds_write_b128 v246, v[156:159] offset:6144\n"
"v_dot2_f32_bf16 v240, v160, v216, v218\n"
"v_dot2_f32_bf16 v241, v160, v217, v218\n"
"v_dot2_f32_bf16 v242, v161, v216, v218\n"
"v_dot2_f32_bf16 v243, v161, v217, v218\n"
"s_nop 1\n"
"s_nop 0\n"
"v_cvt_pk_bf16_f32 v160, v240, v241\n"
"v_cvt_pk_bf16_f32 v161, v242, v243\n"
"v_dot2_f32_bf16 v240, v162, v216, v218\n"
"v_dot2_f32_bf16 v241, v162, v217, v218\n"
"v_dot2_f32_bf16 v242, v163, v216, v218\n"
"v_dot2_f32_bf16 v243, v163, v217, v218\n"
"s_nop 1\n"
"s_nop 0\n"
"v_cvt_pk_bf16_f32 v162, v240, v241\n"
"v_cvt_pk_bf16_f32 v163, v242, v243\n"
"ds_write_b128 v246, v[160:163] offset:8192\n"
"s_waitcnt vmcnt(0)\n"
"v_mov_b32 v224, 0\n"
"v_mov_b32 v225, 0\n"
"v_mov_b32 v226, 0\n"
"v_mov_b32 v227, 0\n"
"v_dot2c_f32_bf16 v224, s42, v164\n"
"v_dot2c_f32_bf16 v226, v164, v164\n"
"v_dot2c_f32_bf16 v225, s42, v165\n"
"v_dot2c_f32_bf16 v227, v165, v165\n"
"v_dot2c_f32_bf16 v224, s42, v166\n"
"v_dot2c_f32_bf16 v226, v166, v166\n"
"v_dot2c_f32_bf16 v225, s42, v167\n"
"v_dot2c_f32_bf16 v227, v167, v167\n"
"s_mov_b32 s36, s64\n"
"s_mov_b32 m0, s43\n"
"s_nop 0\n"
"buffer_load_dwordx4 v254, s[16:19], s36 offen lds\n"
"v_dot2c_f32_bf16 v224, s42, v168\n"
"v_dot2c_f32_bf16 v226, v168, v168\n"
"v_dot2c_f32_bf16 v225, s42, v169\n"
"v_dot2c_f32_bf16 v227, v169, v169\n"
"v_dot2c_f32_bf16 v224, s42, v170\n"
"v_dot2c_f32_bf16 v226, v170, v170\n"
"v_dot2c_f32_bf16 v225, s42, v171\n"
"v_dot2c_f32_bf16 v227, v171, v171\n"
"buffer_load_dwordx4 v255, s[16:19], s36 offen offset:1024 lds\n"
"v_dot2c_f32_bf16 v224, s42, v172\n"
"v_dot2c_f32_bf16 v226, v172, v172\n"
"v_dot2c_f32_bf16 v225, s42, v173\n"
"v_dot2c_f32_bf16 v227, v173, v173\n"
"v_dot2c_f32_bf16 v224, s42, v174\n"
"v_dot2c_f32_bf16 v226, v174, v174\n"
"v_dot2c_f32_bf16 v225, s42, v175\n"
"v_dot2c_f32_bf16 v227, v175, v175\n"
"s_add_u32 s36, s36, 128\n"
"s_add_u32 m0, s43, 16384\n"
"s_nop 0\n"
"s_nop 0\n"
"buffer_load_dwordx4 v254, s[16:19], s36 offen lds\n"
"v_dot2c_f32_bf16 v224, s42, v176\n"
"v_dot2c_f32_bf16 v226, v176, v176\n"
"v_dot2c_f32_bf16 v225, s42, v177\n"
"v_dot2c_f32_bf16 v227, v177, v177\n"
"v_dot2c_f32_bf16 v224, s42, v178\n"
"v_dot2c_f32_bf16 v226, v178, v178\n"
"v_dot2c_f32_bf16 v225, s42, v179\n"
"v_dot2c_f32_bf16 v227, v179, v179\n"
"buffer_load_dwordx4 v255, s[16:19], s36 offen offset:1024 lds\n"
"v_dot2c_f32_bf16 v224, s42, v180\n"
"v_dot2c_f32_bf16 v226, v180, v180\n"
"v_dot2c_f32_bf16 v225, s42, v181\n"
"v_dot2c_f32_bf16 v227, v181, v181\n"
"v_dot2c_f32_bf16 v224, s42, v182\n"
"v_dot2c_f32_bf16 v226, v182, v182\n"
"v_dot2c_f32_bf16 v225, s42, v183\n"
"v_dot2c_f32_bf16 v227, v183, v183\n"
"s_add_u32 s36, s36, 128\n"
"s_add_u32 m0, s43, 32768\n"
"s_nop 0\n"
"s_nop 0\n"
"buffer_load_dwordx4 v254, s[16:19], s36 offen lds\n"
"v_dot2c_f32_bf16 v224, s42, v200\n"
"v_dot2c_f32_bf16 v226, v200, v200\n"
"v_dot2c_f32_bf16 v225, s42, v201\n"
"v_dot2c_f32_bf16 v227, v201, v201\n"
"v_dot2c_f32_bf16 v224, s42, v202\n"
"v_dot2c_f32_bf16 v226, v202, v202\n"
"v_dot2c_f32_bf16 v225, s42, v203\n"
"v_dot2c_f32_bf16 v227, v203, v203\n"
"buffer_load_dwordx4 v255, s[16:19], s36 offen offset:1024 lds\n"
"v_dot2c_f32_bf16 v224, s42, v204\n"
"v_dot2c_f32_bf16 v226, v204, v204\n"
"v_dot2c_f32_bf16 v225, s42, v205\n"
"v_dot2c_f32_bf16 v227, v205, v205\n"
"v_dot2c_f32_bf16 v224, s42, v206\n"
"v_dot2c_f32_bf16 v226, v206, v206\n"
"v_dot2c_f32_bf16 v225, s42, v207\n"
"v_dot2c_f32_bf16 v227, v207, v207\n"
"v_dot2c_f32_bf16 v224, s42, v208\n"
"v_dot2c_f32_bf16 v226, v208, v208\n"
"v_dot2c_f32_bf16 v225, s42, v209\n"
"v_dot2c_f32_bf16 v227, v209, v209\n"
"v_dot2c_f32_bf16 v224, s42, v210\n"
"v_dot2c_f32_bf16 v226, v210, v210\n"
"v_dot2c_f32_bf16 v225, s42, v211\n"
"v_dot2c_f32_bf16 v227, v211, v211\n"
"v_dot2c_f32_bf16 v224, s42, v212\n"
"v_dot2c_f32_bf16 v226, v212, v212\n"
"v_dot2c_f32_bf16 v225, s42, v213\n"
"v_dot2c_f32_bf16 v227, v213, v213\n"
"v_dot2c_f32_bf16 v224, s42, v214\n"
"v_dot2c_f32_bf16 v226, v214, v214\n"
"v_dot2c_f32_bf16 v225, s42, v215\n"
"v_dot2c_f32_bf16 v227, v215, v215\n"
"s_nop 2\n"
"v_add_f32 v224, v224, v225\n"
"v_add_f32 v226, v226, v227\n"
"s_nop 1\n"
"v_add_f32_dpp v224, v224, v224 quad_perm:[1,0,3,2] row_mask:0xf bank_mask:0xf\n"
"v_add_f32_dpp v226, v226, v226 quad_perm:[1,0,3,2] row_mask:0xf bank_mask:0xf\n"
"s_nop 1\n"
"s_nop 0\n"
"v_add_f32_dpp v224, v224, v224 quad_perm:[2,3,0,1] row_mask:0xf bank_mask:0xf\n"
"v_add_f32_dpp v226, v226, v226 quad_perm:[2,3,0,1] row_mask:0xf bank_mask:0xf\n"
"s_nop 1\n"
"s_nop 0\n"
"v_add_f32_dpp v224, v224, v224 row_half_mirror row_mask:0xf bank_mask:0xf\n"
"v_add_f32_dpp v226, v226, v226 row_half_mirror row_mask:0xf bank_mask:0xf\n"
"v_mul_f32 v224, 0x3ae38e39, v224\n"
"v_mul_f32 v226, 0x3ae38e39, v226\n"
"v_fma_f32 v226, -v224, v224, v226\n"
"v_max_f32_e64 v226, 0, v226\n"
"v_add_f32 v227, 0x3727c5ac, v226\n"
"v_rsq_f32 v227, v227\n"
"v_mov_b32 v228, v224\n"
"v_mov_b32_e64 v229, v227\n"
"ds_write_b64 v230, v[228:229] offset:53824\n"
"v_cvt_pk_bf16_f32 v220, v227, v227\n"
"v_and_b32 v219, 0xffff, v220\n"
"v_and_b32 v220, s41, v220\n"
"v_mul_f32 v221, v224, v220\n"
"v_sub_f32_e64 v221, 0, v221\n"
"v_dot2_f32_bf16 v240, v164, v219, v221\n"
"v_dot2_f32_bf16 v241, v164, v220, v221\n"
"v_dot2_f32_bf16 v242, v165, v219, v221\n"
"v_dot2_f32_bf16 v243, v165, v220, v221\n"
"s_nop 1\n"
"s_nop 0\n"
"v_cvt_pk_bf16_f32 v164, v240, v241\n"
"v_cvt_pk_bf16_f32 v165, v242, v243\n"
"v_dot2_f32_bf16 v240, v166, v219, v221\n"
"v_dot2_f32_bf16 v241, v166, v220, v221\n"
"v_dot2_f32_bf16 v242, v167, v219, v221\n"
"v_dot2_f32_bf16 v243, v167, v220, v221\n"
"s_nop 1\n"
"s_nop 0\n"
"v_cvt_pk_bf16_f32 v166, v240, v241\n"
"v_cvt_pk_bf16_f32 v167, v242, v243\n"
"ds_write_b128 v247, v[164:167] offset:0\n"
"v_dot2_f32_bf16 v240, v168, v219, v221\n"
"v_dot2_f32_bf16 v241, v168, v220, v221\n"
"v_dot2_f32_bf16 v242, v169, v219, v221\n"
"v_dot2_f32_bf16 v243, v169, v220, v221\n"
"s_nop 1\n"
"s_nop 0\n"
"v_cvt_pk_bf16_f32 v168, v240, v241\n"
"v_cvt_pk_bf16_f32 v169, v242, v243\n"
"v_dot2_f32_bf16 v240, v170, v219, v221\n"
"v_dot2_f32_bf16 v241, v170, v220, v221\n"
"v_dot2_f32_bf16 v242, v171, v219, v221\n"
"v_dot2_f32_bf16 v243, v171, v220, v221\n"
"s_nop 1\n"
"s_nop 0\n"
"v_cvt_pk_bf16_f32 v170, v240, v241\n"
"v_cvt_pk_bf16_f32 v171, v242, v243\n"
"ds_write_b128 v247, v[168:171] offset:2048\n"
"v_dot2_f32_bf16 v240, v172, v219, v221\n"
"v_dot2_f32_bf16 v241, v172, v220, v221\n"
"v_dot2_f32_bf16 v242, v173, v219, v221\n"
"v_dot2_f32_bf16 v243, v173, v220, v221\n"
"s_nop 1\n"
"s_nop 0\n"
"v_cvt_pk_bf16_f32 v172, v240, v241\n"
"v_cvt_pk_bf16_f32 v173, v242, v243\n"
"v_dot2_f32_bf16 v240, v174, v219, v221\n"
"v_dot2_f32_bf16 v241, v174, v220, v221\n"
"v_dot2_f32_bf16 v242, v175, v219, v221\n"
"v_dot2_f32_bf16 v243, v175, v220, v221\n"
"s_nop 1\n"
"s_nop 0\n"
"v_cvt_pk_bf16_f32 v174, v240, v241\n"
"v_cvt_pk_bf16_f32 v175, v242, v243\n"
"ds_write_b128 v247, v[172:175] offset:4096\n"
"v_dot2_f32_bf16 v240, v176, v219, v221\n"
"v_dot2_f32_bf16 v241, v176, v220, v221\n"
"v_dot2_f32_bf16 v242, v177, v219, v221\n"
"v_dot2_f32_bf16 v243, v177, v220, v221\n"
"s_nop 1\n"
"s_nop 0\n"
"v_cvt_pk_bf16_f32 v176, v240, v241\n"
"v_cvt_pk_bf16_f32 v177, v242, v243\n"
"v_dot2_f32_bf16 v240, v178, v219, v221\n"
"v_dot2_f32_bf16 v241, v178, v220, v221\n"
"v_dot2_f32_bf16 v242, v179, v219, v221\n"
"v_dot2_f32_bf16 v243, v179, v220, v221\n"
"s_nop 1\n"
"s_nop 0\n"
"v_cvt_pk_bf16_f32 v178, v240, v241\n"
"v_cvt_pk_bf16_f32 v179, v242, v243\n"
"ds_write_b128 v247, v[176:179] offset:6144\n"
"v_dot2_f32_bf16 v240, v180, v219, v221\n"
"v_dot2_f32_bf16 v241, v180, v220, v221\n"
"v_dot2_f32_bf16 v242, v181, v219, v221\n"
"v_dot2_f32_bf16 v243, v181, v220, v221\n"
"s_nop 1\n"
"s_nop 0\n"
"v_cvt_pk_bf16_f32 v180, v240, v241\n"
"v_cvt_pk_bf16_f32 v181, v242, v243\n"
"v_dot2_f32_bf16 v240, v182, v219, v221\n"
"v_dot2_f32_bf16 v241, v182, v220, v221\n"
"v_dot2_f32_bf16 v242, v183, v219, v221\n"
"v_dot2_f32_bf16 v243, v183, v220, v221\n"
"s_nop 1\n"
"s_nop 0\n"
"v_cvt_pk_bf16_f32 v182, v240, v241\n"
"v_cvt_pk_bf16_f32 v183, v242, v243\n"
"ds_write_b128 v247, v[180:183] offset:8192\n"
"s_waitcnt vmcnt(4) lgkmcnt(0)\n"
"s_barrier\n"
"s_add_u32 s4, s7, 1\n"
"s_nop 0\n"
"s_xor_b32 s5, s4, 0xa5a5a5a5\n"
"ds_read_b128 v[224:227], v249 offset:0\n"
"ds_read_b128 v[228:231], v249 offset:2048\n"
"ds_read_b128 v[0:3], v244 offset:0\n"
"ds_read_b128 v[72:75], v244 offset:10240\n"
"ds_read_b128 v[4:7], v245 offset:1024\n"
"ds_read_b128 v[76:79], v245 offset:11264\n"
"ds_read_b128 v[8:11], v244 offset:2048\n"
"ds_read_b128 v[80:83], v244 offset:12288\n"
"ds_read_b128 v[12:15], v245 offset:3072\n"
"ds_read_b128 v[84:87], v245 offset:13312\n"
"ds_read_b128 v[16:19], v244 offset:4096\n"
"ds_read_b128 v[88:91], v244 offset:14336\n"
"ds_read_b128 v[20:23], v245 offset:5120\n"
"ds_read_b128 v[92:95], v245 offset:15360\n"
"ds_read_b128 v[24:27], v244 offset:6144\n"
"ds_read_b128 v[96:99], v244 offset:16384\n"
"ds_read_b128 v[28:31], v245 offset:7168\n"
"ds_read_b128 v[100:103], v245 offset:17408\n"
"ds_read_b128 v[32:35], v244 offset:8192\n"
"ds_read_b128 v[104:107], v244 offset:18432\n"
"ds_read_b128 v[36:39], v245 offset:9216\n"
"ds_read_b128 v[108:111], v245 offset:19456\n"
".p2align 3\n"
"s_waitcnt lgkmcnt(15)\n"
"s_nop 0\n"
"v_mfma_f32_16x16x32_bf16 v[144:147], v[0:3], v[224:227], 0\n"
"ds_read_b128 v[232:235], v249 offset:4096\n"
"v_lshlrev_b32_e64 v240, 16, v184\n"
"v_and_b32 v241, 0xffff0000, v184\n"
"v_mfma_f32_16x16x32_bf16 v[148:151], v[72:75], v[224:227], 0\n"
"v_lshlrev_b32_e64 v242, 16, v185\n"
"v_and_b32 v243, 0xffff0000, v185\n"
"v_mfma_f32_16x16x32_bf16 v[152:155], v[0:3], v[228:231], 0\n"
"ds_read_b128 v[236:239], v249 offset:6144\n"
"v_fma_f32 v240, v240, v217, v218\n"
"v_fma_f32 v241, v241, v217, v218\n"
"v_mfma_f32_16x16x32_bf16 v[156:159], v[72:75], v[228:231], 0\n"
"v_fma_f32 v242, v242, v217, v218\n"
"v_fma_f32 v243, v243, v217, v218\n"
"s_waitcnt lgkmcnt(1)\n"
"s_nop 0\n"
"v_mfma_f32_16x16x32_bf16 v[160:163], v[0:3], v[232:235], 0\n"
"ds_read_b128 v[224:227], v250 offset:0\n"
"v_cvt_pk_bf16_f32 v184, v240, v241\n"
"v_cvt_pk_bf16_f32 v185, v242, v243\n"
"v_mfma_f32_16x16x32_bf16 v[164:167], v[72:75], v[232:235], 0\n"
"v_lshlrev_b32_e64 v240, 16, v186\n"
"v_and_b32 v241, 0xffff0000, v186\n"
"s_waitcnt lgkmcnt(1)\n"
"s_nop 0\n"
"v_mfma_f32_16x16x32_bf16 v[168:171], v[0:3], v[236:239], 0\n"
"ds_read_b128 v[228:231], v250 offset:2048\n"
"v_lshlrev_b32_e64 v242, 16, v187\n"
"v_and_b32 v243, 0xffff0000, v187\n"
"v_mfma_f32_16x16x32_bf16 v[172:175], v[72:75], v[236:239], 0\n"
"v_fma_f32 v240, v240, v217, v218\n"
"v_fma_f32 v241, v241, v217, v218\n"
"s_waitcnt lgkmcnt(1)\n"
"s_nop 0\n"
"v_mfma_f32_16x16x32_bf16 v[144:147], v[4:7], v[224:227], v[144:147]\n"
"ds_read_b128 v[232:235], v250 offset:4096\n"
"v_fma_f32 v242, v242, v217, v218\n"
"v_fma_f32 v243, v243, v217, v218\n"
"v_mfma_f32_16x16x32_bf16 v[148:151], v[76:79], v[224:227], v[148:151]\n"
"v_cvt_pk_bf16_f32 v186, v240, v241\n"
"v_cvt_pk_bf16_f32 v187, v242, v243\n"
"s_waitcnt lgkmcnt(1)\n"
"s_nop 0\n"
"v_mfma_f32_16x16x32_bf16 v[152:155], v[4:7], v[228:231], v[152:155]\n"
"ds_read_b128 v[236:239], v250 offset:6144\n"
"v_lshlrev_b32_e64 v240, 16, v200\n"
"v_and_b32 v241, 0xffff0000, v200\n"
"v_mfma_f32_16x16x32_bf16 v[156:159], v[76:79], v[228:231], v[156:159]\n"
"v_lshlrev_b32_e64 v242, 16, v201\n"
"v_and_b32 v243, 0xffff0000, v201\n"
"s_waitcnt vmcnt(2) lgkmcnt(0)\n"
"s_barrier\n"
"v_mfma_f32_16x16x32_bf16 v[160:163], v[4:7], v[232:235], v[160:163]\n"
"ds_read_b128 v[224:227], v249 offset:16384\n"
"v_fma_f32 v240, v240, v220, v221\n"
"v_fma_f32 v241, v241, v220, v221\n"
"ds_write_b128 v246, v[184:187] offset:0\n"
"v_mfma_f32_16x16x32_bf16 v[164:167], v[76:79], v[232:235], v[164:167]\n"
"v_fma_f32 v242, v242, v220, v221\n"
"v_fma_f32 v243, v243, v220, v221\n"
"v_mfma_f32_16x16x32_bf16 v[168:171], v[4:7], v[236:239], v[168:171]\n"
"ds_read_b128 v[228:231], v249 offset:18432\n"
"s_add_u32 s36, s36, 128\n"
"s_mov_b32 m0, s43\n"
"s_nop 0\n"
"v_cvt_pk_bf16_f32 v200, v240, v241\n"
"v_cvt_pk_bf16_f32 v201, v242, v243\n"
"v_mfma_f32_16x16x32_bf16 v[172:175], v[76:79], v[236:239], v[172:175]\n"
"buffer_load_dwordx4 v254, s[16:19], s36 offen lds\n"
"v_lshlrev_b32_e64 v240, 16, v202\n"
"v_and_b32 v241, 0xffff0000, v202\n"
"s_waitcnt lgkmcnt(2)\n"
"s_nop 0\n"
"v_mfma_f32_16x16x32_bf16 v[144:147], v[8:11], v[224:227], v[144:147]\n"
"ds_read_b128 v[232:235], v249 offset:20480\n"
"v_lshlrev_b32_e64 v242, 16, v203\n"
"v_and_b32 v243, 0xffff0000, v203\n"
"v_mfma_f32_16x16x32_bf16 v[148:151], v[80:83], v[224:227], v[148:151]\n"
"v_fma_f32 v240, v240, v220, v221\n"
"v_fma_f32 v241, v241, v220, v221\n"
"s_waitcnt lgkmcnt(1)\n"
"s_nop 0\n"
"v_mfma_f32_16x16x32_bf16 v[152:155], v[8:11], v[228:231], v[152:155]\n"
"ds_read_b128 v[236:239], v249 offset:22528\n"
"v_fma_f32 v242, v242, v220, v221\n"
"v_fma_f32 v243, v243, v220, v221\n"
"v_mfma_f32_16x16x32_bf16 v[156:159], v[80:83], v[228:231], v[156:159]\n"
"v_cvt_pk_bf16_f32 v202, v240, v241\n"
"v_cvt_pk_bf16_f32 v203, v242, v243\n"
"ds_write_b128 v247, v[200:203] offset:0\n"
"s_waitcnt lgkmcnt(2)\n"
"s_nop 0\n"
"v_mfma_f32_16x16x32_bf16 v[160:163], v[8:11], v[232:235], v[160:163]\n"
"ds_read_b128 v[224:227], v250 offset:16384\n"
"v_lshlrev_b32_e64 v240, 16, v188\n"
"v_and_b32 v241, 0xffff0000, v188\n"
"v_mfma_f32_16x16x32_bf16 v[164:167], v[80:83], v[232:235], v[164:167]\n"
"buffer_load_dwordx4 v255, s[16:19], s36 offen offset:1024 lds\n"
"v_lshlrev_b32_e64 v242, 16, v189\n"
"v_and_b32 v243, 0xffff0000, v189\n"
"s_waitcnt lgkmcnt(2)\n"
"s_nop 0\n"
"v_mfma_f32_16x16x32_bf16 v[168:171], v[8:11], v[236:239], v[168:171]\n"
"ds_read_b128 v[228:231], v250 offset:18432\n"
"v_fma_f32 v240, v240, v217, v218\n"
"v_fma_f32 v241, v241, v217, v218\n"
"v_mfma_f32_16x16x32_bf16 v[172:175], v[80:83], v[236:239], v[172:175]\n"
"v_fma_f32 v242, v242, v217, v218\n"
"v_fma_f32 v243, v243, v217, v218\n"
"s_waitcnt lgkmcnt(1)\n"
"s_nop 0\n"
"v_mfma_f32_16x16x32_bf16 v[144:147], v[12:15], v[224:227], v[144:147]\n"
"ds_read_b128 v[232:235], v250 offset:20480\n"
"v_cvt_pk_bf16_f32 v188, v240, v241\n"
"v_cvt_pk_bf16_f32 v189, v242, v243\n"
"v_mfma_f32_16x16x32_bf16 v[148:151], v[84:87], v[224:227], v[148:151]\n"
"v_lshlrev_b32_e64 v240, 16, v190\n"
"v_and_b32 v241, 0xffff0000, v190\n"
"s_waitcnt lgkmcnt(1)\n"
"s_nop 0\n"
"v_mfma_f32_16x16x32_bf16 v[152:155], v[12:15], v[228:231], v[152:155]\n"
"ds_read_b128 v[236:239], v250 offset:22528\n"
"v_lshlrev_b32_e64 v242, 16, v191\n"
"v_and_b32 v243, 0xffff0000, v191\n"
"v_mfma_f32_16x16x32_bf16 v[156:159], v[84:87], v[228:231], v[156:159]\n"
"v_fma_f32 v240, v240, v217, v218\n"
"v_fma_f32 v241, v241, v217, v218\n"
"s_waitcnt vmcnt(2) lgkmcnt(0)\n"
"s_barrier\n"
"v_mfma_f32_16x16x32_bf16 v[160:163], v[12:15], v[232:235], v[160:163]\n"
"ds_read_b128 v[224:227], v249 offset:32768\n"
"v_fma_f32 v242, v242, v217, v218\n"
"v_fma_f32 v243, v243, v217, v218\n"
"v_mfma_f32_16x16x32_bf16 v[164:167], v[84:87], v[232:235], v[164:167]\n"
"v_cvt_pk_bf16_f32 v190, v240, v241\n"
"v_cvt_pk_bf16_f32 v191, v242, v243\n"
"ds_write_b128 v246, v[188:191] offset:2048\n"
"v_mfma_f32_16x16x32_bf16 v[168:171], v[12:15], v[236:239], v[168:171]\n"
"ds_read_b128 v[228:231], v249 offset:34816\n"
"s_add_u32 s36, s36, 128\n"
"s_add_u32 m0, s43, 16384\n"
"v_lshlrev_b32_e64 v240, 16, v204\n"
"v_and_b32 v241, 0xffff0000, v204\n"
"v_mfma_f32_16x16x32_bf16 v[172:175], v[84:87], v[236:239], v[172:175]\n"
"buffer_load_dwordx4 v254, s[16:19], s36 offen lds\n"
"v_lshlrev_b32_e64 v242, 16, v205\n"
"v_and_b32 v243, 0xffff0000, v205\n"
"s_waitcnt lgkmcnt(2)\n"
"s_nop 0\n"
"v_mfma_f32_16x16x32_bf16 v[144:147], v[16:19], v[224:227], v[144:147]\n"
"ds_read_b128 v[232:235], v249 offset:36864\n"
"v_fma_f32 v240, v240, v220, v221\n"
"v_fma_f32 v241, v241, v220, v221\n"
"v_mfma_f32_16x16x32_bf16 v[148:151], v[88:91], v[224:227], v[148:151]\n"
"v_fma_f32 v242, v242, v220, v221\n"
"v_fma_f32 v243, v243, v220, v221\n"
"s_waitcnt lgkmcnt(1)\n"
"s_nop 0\n"
"v_mfma_f32_16x16x32_bf16 v[152:155], v[16:19], v[228:231], v[152:155]\n"
"ds_read_b128 v[236:239], v249 offset:38912\n"
"v_cvt_pk_bf16_f32 v204, v240, v241\n"
"v_cvt_pk_bf16_f32 v205, v242, v243\n"
"v_mfma_f32_16x16x32_bf16 v[156:159], v[88:91], v[228:231], v[156:159]\n"
"v_lshlrev_b32_e64 v240, 16, v206\n"
"v_and_b32 v241, 0xffff0000, v206\n"
"s_waitcnt lgkmcnt(1)\n"
"s_nop 0\n"
"v_mfma_f32_16x16x32_bf16 v[160:163], v[16:19], v[232:235], v[160:163]\n"
"ds_read_b128 v[224:227], v250 offset:32768\n"
"v_lshlrev_b32_e64 v242, 16, v207\n"
"v_and_b32 v243, 0xffff0000, v207\n"
"v_mfma_f32_16x16x32_bf16 v[164:167], v[88:91], v[232:235], v[164:167]\n"
"buffer_load_dwordx4 v255, s[16:19], s36 offen offset:1024 lds\n"
"v_fma_f32 v240, v240, v220, v221\n"
"v_fma_f32 v241, v241, v220, v221\n"
"s_waitcnt lgkmcnt(1)\n"
"s_nop 0\n"
"v_mfma_f32_16x16x32_bf16 v[168:171], v[16:19], v[236:239], v[168:171]\n"
"ds_read_b128 v[228:231], v250 offset:34816\n"
"v_fma_f32 v242, v242, v220, v221\n"
"v_fma_f32 v243, v243, v220, v221\n"
"v_mfma_f32_16x16x32_bf16 v[172:175], v[88:91], v[236:239], v[172:175]\n"
"v_cvt_pk_bf16_f32 v206, v240, v241\n"
"v_cvt_pk_bf16_f32 v207, v242, v243\n"
"ds_write_b128 v247, v[204:207] offset:2048\n"
"s_waitcnt lgkmcnt(2)\n"
"s_nop 0\n"
"v_mfma_f32_16x16x32_bf16 v[144:147], v[20:23], v[224:227], v[144:147]\n"
"ds_read_b128 v[232:235], v250 offset:36864\n"
"v_lshlrev_b32_e64 v240, 16, v192\n"
"v_and_b32 v241, 0xffff0000, v192\n"
"v_mfma_f32_16x16x32_bf16 v[148:151], v[92:95], v[224:227], v[148:151]\n"
"v_lshlrev_b32_e64 v242, 16, v193\n"
"v_and_b32 v243, 0xffff0000, v193\n"
"s_waitcnt lgkmcnt(2)\n"
"s_nop 0\n"
"v_mfma_f32_16x16x32_bf16 v[152:155], v[20:23], v[228:231], v[152:155]\n"
"ds_read_b128 v[236:239], v250 offset:38912\n"
"v_fma_f32 v240, v240, v217, v218\n"
"v_fma_f32 v241, v241, v217, v218\n"
"v_mfma_f32_16x16x32_bf16 v[156:159], v[92:95], v[228:231], v[156:159]\n"
"v_fma_f32 v242, v242, v217, v218\n"
"v_fma_f32 v243, v243, v217, v218\n"
"s_waitcnt vmcnt(2) lgkmcnt(0)\n"
"s_barrier\n"
"v_mfma_f32_16x16x32_bf16 v[160:163], v[20:23], v[232:235], v[160:163]\n"
"ds_read_b128 v[224:227], v249 offset:0\n"
"v_cvt_pk_bf16_f32 v192, v240, v241\n"
"v_cvt_pk_bf16_f32 v193, v242, v243\n"
"v_mfma_f32_16x16x32_bf16 v[164:167], v[92:95], v[232:235], v[164:167]\n"
"v_lshlrev_b32_e64 v240, 16, v194\n"
"v_and_b32 v241, 0xffff0000, v194\n"
"v_mfma_f32_16x16x32_bf16 v[168:171], v[20:23], v[236:239], v[168:171]\n"
"ds_read_b128 v[228:231], v249 offset:2048\n"
"s_add_u32 s36, s36, 128\n"
"s_add_u32 m0, s43, 32768\n"
"v_lshlrev_b32_e64 v242, 16, v195\n"
"v_and_b32 v243, 0xffff0000, v195\n"
"v_mfma_f32_16x16x32_bf16 v[172:175], v[92:95], v[236:239], v[172:175]\n"
"buffer_load_dwordx4 v254, s[16:19], s36 offen lds\n"
"v_fma_f32 v240, v240, v217, v218\n"
"v_fma_f32 v241, v241, v217, v218\n"
"s_waitcnt lgkmcnt(1)\n"
"s_nop 0\n"
"v_mfma_f32_16x16x32_bf16 v[144:147], v[24:27], v[224:227], v[144:147]\n"
"ds_read_b128 v[232:235], v249 offset:4096\n"
"v_fma_f32 v242, v242, v217, v218\n"
"v_fma_f32 v243, v243, v217, v218\n"
"v_mfma_f32_16x16x32_bf16 v[148:151], v[96:99], v[224:227], v[148:151]\n"
"v_cvt_pk_bf16_f32 v194, v240, v241\n"
"v_cvt_pk_bf16_f32 v195, v242, v243\n"
"ds_write_b128 v246, v[192:195] offset:4096\n"
"s_waitcnt lgkmcnt(2)\n"
"s_nop 0\n"
"v_mfma_f32_16x16x32_bf16 v[152:155], v[24:27], v[228:231], v[152:155]\n"
"ds_read_b128 v[236:239], v249 offset:6144\n"
"v_lshlrev_b32_e64 v240, 16, v208\n"
"v_and_b32 v241, 0xffff0000, v208\n"
"v_mfma_f32_16x16x32_bf16 v[156:159], v[96:99], v[228:231], v[156:159]\n"
"v_lshlrev_b32_e64 v242, 16, v209\n"
"v_and_b32 v243, 0xffff0000, v209\n"
"s_waitcnt lgkmcnt(2)\n"
"s_nop 0\n"
"v_mfma_f32_16x16x32_bf16 v[160:163], v[24:27], v[232:235], v[160:163]\n"
"ds_read_b128 v[224:227], v250 offset:0\n"
"v_fma_f32 v240, v240, v220, v221\n"
"v_fma_f32 v241, v241, v220, v221\n"
"v_mfma_f32_16x16x32_bf16 v[164:167], v[96:99], v[232:235], v[164:167]\n"
"buffer_load_dwordx4 v255, s[16:19], s36 offen offset:1024 lds\n"
"v_fma_f32 v242, v242, v220, v221\n"
"v_fma_f32 v243, v243, v220, v221\n"
"s_waitcnt lgkmcnt(1)\n"
"s_nop 0\n"
"v_mfma_f32_16x16x32_bf16 v[168:171], v[24:27], v[236:239], v[168:171]\n"
"ds_read_b128 v[228:231], v250 offset:2048\n"
"v_cvt_pk_bf16_f32 v208, v240, v241\n"
"v_cvt_pk_bf16_f32 v209, v242, v243\n"
"v_mfma_f32_16x16x32_bf16 v[172:175], v[96:99], v[236:239], v[172:175]\n"
"v_lshlrev_b32_e64 v240, 16, v210\n"
"v_and_b32 v241, 0xffff0000, v210\n"
"s_waitcnt lgkmcnt(1)\n"
"s_nop 0\n"
"v_mfma_f32_16x16x32_bf16 v[144:147], v[28:31], v[224:227], v[144:147]\n"
"ds_read_b128 v[232:235], v250 offset:4096\n"
"v_lshlrev_b32_e64 v242, 16, v211\n"
"v_and_b32 v243, 0xffff0000, v211\n"
"v_mfma_f32_16x16x32_bf16 v[148:151], v[100:103], v[224:227], v[148:151]\n"
"v_fma_f32 v240, v240, v220, v221\n"
"v_fma_f32 v241, v241, v220, v221\n"
"s_waitcnt lgkmcnt(1)\n"
"s_nop 0\n"
"v_mfma_f32_16x16x32_bf16 v[152:155], v[28:31], v[228:231], v[152:155]\n"
"ds_read_b128 v[236:239], v250 offset:6144\n"
"v_fma_f32 v242, v242, v220, v221\n"
"v_fma_f32 v243, v243, v220, v221\n"
"v_mfma_f32_16x16x32_bf16 v[156:159], v[100:103], v[228:231], v[156:159]\n"
"v_cvt_pk_bf16_f32 v210, v240, v241\n"
"v_cvt_pk_bf16_f32 v211, v242, v243\n"
"ds_write_b128 v247, v[208:211] offset:4096\n"
"s_waitcnt vmcnt(2) lgkmcnt(0)\n"
"s_barrier\n"
"v_mfma_f32_16x16x32_bf16 v[160:163], v[28:31], v[232:235], v[160:163]\n"
"ds_read_b128 v[224:227], v249 offset:16384\n"
"v_lshlrev_b32_e64 v240, 16, v196\n"
"v_and_b32 v241, 0xffff0000, v196\n"
"v_mfma_f32_16x16x32_bf16 v[164:167], v[100:103], v[232:235], v[164:167]\n"
"v_lshlrev_b32_e64 v242, 16, v197\n"
"v_and_b32 v243, 0xffff0000, v197\n"
"v_mfma_f32_16x16x32_bf16 v[168:171], v[28:31], v[236:239], v[168:171]\n"
"ds_read_b128 v[228:231], v249 offset:18432\n"
"s_add_u32 s36, s36, 128\n"
"s_mov_b32 m0, s43\n"
"s_nop 0\n"
"v_fma_f32 v240, v240, v217, v218\n"
"v_fma_f32 v241, v241, v217, v218\n"
"v_mfma_f32_16x16x32_bf16 v[172:175], v[100:103], v[236:239], v[172:175]\n"
"buffer_load_dwordx4 v254, s[16:19], s36 offen lds\n"
"v_fma_f32 v242, v242, v217, v218\n"
"v_fma_f32 v243, v243, v217, v218\n"
"s_waitcnt lgkmcnt(1)\n"
"s_nop 0\n"
"v_mfma_f32_16x16x32_bf16 v[144:147], v[32:35], v[224:227], v[144:147]\n"
"ds_read_b128 v[232:235], v249 offset:20480\n"
"ds_read_b128 v[40:43], v244 offset:0\n"
"ds_read_b128 v[112:115], v244 offset:10240\n"
"v_cvt_pk_bf16_f32 v196, v240, v241\n"
"v_cvt_pk_bf16_f32 v197, v242, v243\n"
"v_mfma_f32_16x16x32_bf16 v[148:151], v[104:107], v[224:227], v[148:151]\n"
"v_lshlrev_b32_e64 v240, 16, v198\n"
"v_and_b32 v241, 0xffff0000, v198\n"
"s_waitcnt lgkmcnt(3)\n"
"s_nop 0\n"
"v_mfma_f32_16x16x32_bf16 v[152:155], v[32:35], v[228:231], v[152:155]\n"
"ds_read_b128 v[236:239], v249 offset:22528\n"
"ds_read_b128 v[44:47], v245 offset:1024\n"
"ds_read_b128 v[116:119], v245 offset:11264\n"
"v_lshlrev_b32_e64 v242, 16, v199\n"
"v_and_b32 v243, 0xffff0000, v199\n"
"v_mfma_f32_16x16x32_bf16 v[156:159], v[104:107], v[228:231], v[156:159]\n"
"v_fma_f32 v240, v240, v217, v218\n"
"v_fma_f32 v241, v241, v217, v218\n"
"s_waitcnt lgkmcnt(5)\n"
"s_nop 0\n"
"v_mfma_f32_16x16x32_bf16 v[160:163], v[32:35], v[232:235], v[160:163]\n"
"ds_read_b128 v[224:227], v250 offset:16384\n"
"ds_read_b128 v[48:51], v244 offset:2048\n"
"ds_read_b128 v[120:123], v244 offset:12288\n"
"v_fma_f32 v242, v242, v217, v218\n"
"v_fma_f32 v243, v243, v217, v218\n"
"v_mfma_f32_16x16x32_bf16 v[164:167], v[104:107], v[232:235], v[164:167]\n"
"buffer_load_dwordx4 v255, s[16:19], s36 offen offset:1024 lds\n"
"v_cvt_pk_bf16_f32 v198, v240, v241\n"
"v_cvt_pk_bf16_f32 v199, v242, v243\n"
"ds_write_b128 v246, v[196:199] offset:6144\n"
"s_waitcnt lgkmcnt(6)\n"
"s_nop 0\n"
"v_mfma_f32_16x16x32_bf16 v[168:171], v[32:35], v[236:239], v[168:171]\n"
"ds_read_b128 v[228:231], v250 offset:18432\n"
"ds_read_b128 v[52:55], v245 offset:3072\n"
"ds_read_b128 v[124:127], v245 offset:13312\n"
"v_lshlrev_b32_e64 v240, 16, v212\n"
"v_and_b32 v241, 0xffff0000, v212\n"
"v_mfma_f32_16x16x32_bf16 v[172:175], v[104:107], v[236:239], v[172:175]\n"
"v_lshlrev_b32_e64 v242, 16, v213\n"
"v_and_b32 v243, 0xffff0000, v213\n"
"s_waitcnt lgkmcnt(6)\n"
"s_nop 0\n"
"v_mfma_f32_16x16x32_bf16 v[144:147], v[36:39], v[224:227], v[144:147]\n"
"ds_read_b128 v[232:235], v250 offset:20480\n"
"v_fma_f32 v240, v240, v220, v221\n"
"v_fma_f32 v241, v241, v220, v221\n"
"v_mfma_f32_16x16x32_bf16 v[148:151], v[108:111], v[224:227], v[148:151]\n"
"v_fma_f32 v242, v242, v220, v221\n"
"v_fma_f32 v243, v243, v220, v221\n"
"s_waitcnt lgkmcnt(3)\n"
"s_nop 0\n"
"v_mfma_f32_16x16x32_bf16 v[152:155], v[36:39], v[228:231], v[152:155]\n"
"ds_read_b128 v[236:239], v250 offset:22528\n"
"v_cvt_pk_bf16_f32 v212, v240, v241\n"
"v_cvt_pk_bf16_f32 v213, v242, v243\n"
"v_mfma_f32_16x16x32_bf16 v[156:159], v[108:111], v[228:231], v[156:159]\n"
"v_lshlrev_b32_e64 v240, 16, v214\n"
"v_and_b32 v241, 0xffff0000, v214\n"
"s_waitcnt vmcnt(2) lgkmcnt(0)\n"
"s_barrier\n"
"v_mfma_f32_16x16x32_bf16 v[160:163], v[36:39], v[232:235], v[160:163]\n"
"ds_read_b128 v[224:227], v249 offset:32768\n"
"v_lshlrev_b32_e64 v242, 16, v215\n"
"v_and_b32 v243, 0xffff0000, v215\n"
"v_mfma_f32_16x16x32_bf16 v[164:167], v[108:111], v[232:235], v[164:167]\n"
"v_fma_f32 v240, v240, v220, v221\n"
"v_fma_f32 v241, v241, v220, v221\n"
"v_mfma_f32_16x16x32_bf16 v[168:171], v[36:39], v[236:239], v[168:171]\n"
"ds_read_b128 v[228:231], v249 offset:34816\n"
"s_add_u32 s36, s36, 128\n"
"s_add_u32 m0, s43, 16384\n"
"v_fma_f32 v242, v242, v220, v221\n"
"v_fma_f32 v243, v243, v220, v221\n"
"v_mfma_f32_16x16x32_bf16 v[172:175], v[108:111], v[236:239], v[172:175]\n"
"buffer_load_dwordx4 v254, s[16:19], s36 offen lds\n"
"v_cvt_pk_bf16_f32 v214, v240, v241\n"
"v_cvt_pk_bf16_f32 v215, v242, v243\n"
"ds_write_b128 v247, v[212:215] offset:6144\n"
"s_waitcnt lgkmcnt(2)\n"
"s_nop 0\n"
"v_mfma_f32_16x16x32_bf16 v[144:147], v[40:43], v[224:227], v[144:147]\n"
"ds_read_b128 v[232:235], v249 offset:36864\n"
"v_mfma_f32_16x16x32_bf16 v[148:151], v[112:115], v[224:227], v[148:151]\n"
"s_waitcnt lgkmcnt(2)\n"
"s_nop 0\n"
"v_mfma_f32_16x16x32_bf16 v[152:155], v[40:43], v[228:231], v[152:155]\n"
"ds_read_b128 v[236:239], v249 offset:38912\n"
"v_mfma_f32_16x16x32_bf16 v[156:159], v[112:115], v[228:231], v[156:159]\n"
"s_waitcnt lgkmcnt(1)\n"
"s_nop 0\n"
"v_mfma_f32_16x16x32_bf16 v[160:163], v[40:43], v[232:235], v[160:163]\n"
"ds_read_b128 v[224:227], v250 offset:32768\n"
"v_mfma_f32_16x16x32_bf16 v[164:167], v[112:115], v[232:235], v[164:167]\n"
"buffer_load_dwordx4 v255, s[16:19], s36 offen offset:1024 lds\n"
"s_waitcnt lgkmcnt(1)\n"
"s_nop 0\n"
"v_mfma_f32_16x16x32_bf16 v[168:171], v[40:43], v[236:239], v[168:171]\n"
"ds_read_b128 v[228:231], v250 offset:34816\n"
"v_mfma_f32_16x16x32_bf16 v[172:175], v[112:115], v[236:239], v[172:175]\n"
"s_waitcnt lgkmcnt(1)\n"
"s_nop 0\n"
"v_mfma_f32_16x16x32_bf16 v[144:147], v[44:47], v[224:227], v[144:147]\n"
"ds_read_b128 v[232:235], v250 offset:36864\n"
"v_mfma_f32_16x16x32_bf16 v[148:151], v[116:119], v[224:227], v[148:151]\n"
"s_waitcnt lgkmcnt(1)\n"
"s_nop 0\n"
"v_mfma_f32_16x16x32_bf16 v[152:155], v[44:47], v[228:231], v[152:155]\n"
"ds_read_b128 v[236:239], v250 offset:38912\n"
"v_mfma_f32_16x16x32_bf16 v[156:159], v[116:119], v[228:231], v[156:159]\n"
"s_waitcnt vmcnt(2) lgkmcnt(0)\n"
"s_barrier\n"
"v_mfma_f32_16x16x32_bf16 v[160:163], v[44:47], v[232:235], v[160:163]\n"
"ds_read_b128 v[224:227], v249 offset:0\n"
"v_mfma_f32_16x16x32_bf16 v[164:167], v[116:119], v[232:235], v[164:167]\n"
"v_mfma_f32_16x16x32_bf16 v[168:171], v[44:47], v[236:239], v[168:171]\n"
"ds_read_b128 v[228:231], v249 offset:2048\n"
"s_add_u32 s36, s36, 128\n"
"s_add_u32 m0, s43, 32768\n"
"v_mfma_f32_16x16x32_bf16 v[172:175], v[116:119], v[236:239], v[172:175]\n"
"buffer_load_dwordx4 v254, s[16:19], s36 offen lds\n"
"s_waitcnt lgkmcnt(1)\n"
"s_nop 0\n"
"v_mfma_f32_16x16x32_bf16 v[144:147], v[48:51], v[224:227], v[144:147]\n"
"ds_read_b128 v[232:235], v249 offset:4096\n"
"ds_read_b128 v[56:59], v244 offset:4096\n"
"ds_read_b128 v[128:131], v244 offset:14336\n"
"v_mfma_f32_16x16x32_bf16 v[148:151], v[120:123], v[224:227], v[148:151]\n"
"s_waitcnt lgkmcnt(3)\n"
"s_nop 0\n"
"v_mfma_f32_16x16x32_bf16 v[152:155], v[48:51], v[228:231], v[152:155]\n"
"ds_read_b128 v[236:239], v249 offset:6144\n"
"ds_read_b128 v[60:63], v245 offset:5120\n"
"ds_read_b128 v[132:135], v245 offset:15360\n"
"v_mfma_f32_16x16x32_bf16 v[156:159], v[120:123], v[228:231], v[156:159]\n"
"s_waitcnt lgkmcnt(5)\n"
"s_nop 0\n"
"v_mfma_f32_16x16x32_bf16 v[160:163], v[48:51], v[232:235], v[160:163]\n"
"ds_read_b128 v[224:227], v250 offset:0\n"
"ds_read_b128 v[64:67], v244 offset:6144\n"
"ds_read_b128 v[136:139], v244 offset:16384\n"
"v_mfma_f32_16x16x32_bf16 v[164:167], v[120:123], v[232:235], v[164:167]\n"
"buffer_load_dwordx4 v255, s[16:19], s36 offen offset:1024 lds\n"
"s_waitcnt lgkmcnt(5)\n"
"s_nop 0\n"
"v_mfma_f32_16x16x32_bf16 v[168:171], v[48:51], v[236:239], v[168:171]\n"
"ds_read_b128 v[228:231], v250 offset:2048\n"
"ds_read_b128 v[68:71], v245 offset:7168\n"
"ds_read_b128 v[140:143], v245 offset:17408\n"
"v_mfma_f32_16x16x32_bf16 v[172:175], v[120:123], v[236:239], v[172:175]\n"
"s_waitcnt lgkmcnt(5)\n"
"s_nop 0\n"
"v_mfma_f32_16x16x32_bf16 v[144:147], v[52:55], v[224:227], v[144:147]\n"
"ds_read_b128 v[232:235], v250 offset:4096\n"
"v_mfma_f32_16x16x32_bf16 v[148:151], v[124:127], v[224:227], v[148:151]\n"
"s_waitcnt lgkmcnt(3)\n"
"s_nop 0\n"
"v_mfma_f32_16x16x32_bf16 v[152:155], v[52:55], v[228:231], v[152:155]\n"
"ds_read_b128 v[236:239], v250 offset:6144\n"
"v_mfma_f32_16x16x32_bf16 v[156:159], v[124:127], v[228:231], v[156:159]\n"
"s_waitcnt vmcnt(2) lgkmcnt(0)\n"
"s_barrier\n"
"v_mfma_f32_16x16x32_bf16 v[160:163], v[52:55], v[232:235], v[160:163]\n"
"ds_read_b128 v[224:227], v249 offset:16384\n"
"v_mfma_f32_16x16x32_bf16 v[164:167], v[124:127], v[232:235], v[164:167]\n"
"v_mfma_f32_16x16x32_bf16 v[168:171], v[52:55], v[236:239], v[168:171]\n"
"ds_read_b128 v[228:231], v249 offset:18432\n"
"s_mov_b32 s36, s65\n"
"s_mov_b32 m0, s43\n"
"v_mfma_f32_16x16x32_bf16 v[172:175], v[124:127], v[236:239], v[172:175]\n"
"buffer_load_dwordx4 v254, s[16:19], s36 offen lds\n"
"s_waitcnt lgkmcnt(1)\n"
"s_nop 0\n"
"v_mfma_f32_16x16x32_bf16 v[144:147], v[56:59], v[224:227], v[144:147]\n"
"ds_read_b128 v[232:235], v249 offset:20480\n"
"v_mfma_f32_16x16x32_bf16 v[148:151], v[128:131], v[224:227], v[148:151]\n"
"s_waitcnt lgkmcnt(1)\n"
"s_nop 0\n"
"v_mfma_f32_16x16x32_bf16 v[152:155], v[56:59], v[228:231], v[152:155]\n"
"ds_read_b128 v[236:239], v249 offset:22528\n"
"v_mfma_f32_16x16x32_bf16 v[156:159], v[128:131], v[228:231], v[156:159]\n"
"s_waitcnt lgkmcnt(1)\n"
"s_nop 0\n"
"v_mfma_f32_16x16x32_bf16 v[160:163], v[56:59], v[232:235], v[160:163]\n"
"ds_read_b128 v[224:227], v250 offset:16384\n"
"v_mfma_f32_16x16x32_bf16 v[164:167], v[128:131], v[232:235], v[164:167]\n"
"buffer_load_dwordx4 v255, s[16:19], s36 offen offset:1024 lds\n"
"s_waitcnt lgkmcnt(1)\n"
"s_nop 0\n"
"v_mfma_f32_16x16x32_bf16 v[168:171], v[56:59], v[236:239], v[168:171]\n"
"ds_read_b128 v[228:231], v250 offset:18432\n"
"v_mfma_f32_16x16x32_bf16 v[172:175], v[128:131], v[236:239], v[172:175]\n"
"s_waitcnt lgkmcnt(1)\n"
"s_nop 0\n"
"v_mfma_f32_16x16x32_bf16 v[144:147], v[60:63], v[224:227], v[144:147]\n"
"ds_read_b128 v[232:235], v250 offset:20480\n"
"v_mfma_f32_16x16x32_bf16 v[148:151], v[132:135], v[224:227], v[148:151]\n"
"s_waitcnt lgkmcnt(1)\n"
"s_nop 0\n"
"v_mfma_f32_16x16x32_bf16 v[152:155], v[60:63], v[228:231], v[152:155]\n"
"ds_read_b128 v[236:239], v250 offset:22528\n"
"v_mfma_f32_16x16x32_bf16 v[156:159], v[132:135], v[228:231], v[156:159]\n"
"s_waitcnt vmcnt(2) lgkmcnt(0)\n"
"s_barrier\n"
"v_mfma_f32_16x16x32_bf16 v[160:163], v[60:63], v[232:235], v[160:163]\n"
"ds_read_b128 v[224:227], v249 offset:32768\n"
"v_mfma_f32_16x16x32_bf16 v[164:167], v[132:135], v[232:235], v[164:167]\n"
"v_mfma_f32_16x16x32_bf16 v[168:171], v[60:63], v[236:239], v[168:171]\n"
"ds_read_b128 v[228:231], v249 offset:34816\n"
"s_add_u32 s36, s36, 128\n"
"s_add_u32 m0, s43, 16384\n"
"v_mfma_f32_16x16x32_bf16 v[172:175], v[132:135], v[236:239], v[172:175]\n"
"buffer_load_dwordx4 v254, s[16:19], s36 offen lds\n"
"s_waitcnt lgkmcnt(1)\n"
"s_nop 0\n"
"v_mfma_f32_16x16x32_bf16 v[144:147], v[64:67], v[224:227], v[144:147]\n"
"ds_read_b128 v[232:235], v249 offset:36864\n"
"v_mfma_f32_16x16x32_bf16 v[148:151], v[136:139], v[224:227], v[148:151]\n"
"s_waitcnt lgkmcnt(1)\n"
"s_nop 0\n"
"v_mfma_f32_16x16x32_bf16 v[152:155], v[64:67], v[228:231], v[152:155]\n"
"ds_read_b128 v[236:239], v249 offset:38912\n"
"v_mfma_f32_16x16x32_bf16 v[156:159], v[136:139], v[228:231], v[156:159]\n"
"s_waitcnt lgkmcnt(1)\n"
"s_nop 0\n"
"v_mfma_f32_16x16x32_bf16 v[160:163], v[64:67], v[232:235], v[160:163]\n"
"ds_read_b128 v[224:227], v250 offset:32768\n"
"v_mfma_f32_16x16x32_bf16 v[164:167], v[136:139], v[232:235], v[164:167]\n"
"buffer_load_dwordx4 v255, s[16:19], s36 offen offset:1024 lds\n"
"s_waitcnt lgkmcnt(1)\n"
"s_nop 0\n"
"v_mfma_f32_16x16x32_bf16 v[168:171], v[64:67], v[236:239], v[168:171]\n"
"ds_read_b128 v[228:231], v250 offset:34816\n"
"v_mfma_f32_16x16x32_bf16 v[172:175], v[136:139], v[236:239], v[172:175]\n"
"s_waitcnt lgkmcnt(1)\n"
"s_nop 0\n"
"v_mfma_f32_16x16x32_bf16 v[144:147], v[68:71], v[224:227], v[144:147]\n"
"ds_read_b128 v[232:235], v250 offset:36864\n"
"v_mfma_f32_16x16x32_bf16 v[148:151], v[140:143], v[224:227], v[148:151]\n"
"s_waitcnt lgkmcnt(1)\n"
"s_nop 0\n"
"v_mfma_f32_16x16x32_bf16 v[152:155], v[68:71], v[228:231], v[152:155]\n"
"ds_read_b128 v[236:239], v250 offset:38912\n"
"v_mfma_f32_16x16x32_bf16 v[156:159], v[140:143], v[228:231], v[156:159]\n"
"s_waitcnt vmcnt(2) lgkmcnt(0)\n"
"s_barrier\n"
"v_mfma_f32_16x16x32_bf16 v[160:163], v[68:71], v[232:235], v[160:163]\n"
"ds_read_b128 v[224:227], v249 offset:0\n"
"v_mfma_f32_16x16x32_bf16 v[164:167], v[140:143], v[232:235], v[164:167]\n"
"v_mfma_f32_16x16x32_bf16 v[168:171], v[68:71], v[236:239], v[168:171]\n"
"ds_read_b128 v[228:231], v249 offset:2048\n"
"s_add_u32 s36, s36, 128\n"
"s_add_u32 m0, s43, 32768\n"
"v_mfma_f32_16x16x32_bf16 v[172:175], v[140:143], v[236:239], v[172:175]\n"
"buffer_load_dwordx4 v254, s[16:19], s36 offen lds\n"
"s_waitcnt lgkmcnt(1)\n"
"s_nop 0\n"
"v_mfma_f32_16x16x32_bf16 v[176:179], v[0:3], v[224:227], 0\n"
"ds_read_b128 v[232:235], v249 offset:4096\n"
"v_mfma_f32_16x16x32_bf16 v[180:183], v[72:75], v[224:227], 0\n"
"s_waitcnt lgkmcnt(1)\n"
"s_nop 0\n"
"v_mfma_f32_16x16x32_bf16 v[184:187], v[0:3], v[228:231], 0\n"
"ds_read_b128 v[236:239], v249 offset:6144\n"
"v_mfma_f32_16x16x32_bf16 v[188:191], v[72:75], v[228:231], 0\n"
"s_waitcnt lgkmcnt(1)\n"
"s_nop 0\n"
"v_mfma_f32_16x16x32_bf16 v[192:195], v[0:3], v[232:235], 0\n"
"ds_read_b128 v[224:227], v250 offset:0\n"
"v_mfma_f32_16x16x32_bf16 v[196:199], v[72:75], v[232:235], 0\n"
"buffer_load_dwordx4 v255, s[16:19], s36 offen offset:1024 lds\n"
"s_waitcnt lgkmcnt(1)\n"
"s_nop 0\n"
"v_mfma_f32_16x16x32_bf16 v[200:203], v[0:3], v[236:239], 0\n"
"ds_read_b128 v[228:231], v250 offset:2048\n"
"v_lshrrev_b32 v244, 4, v248\n"
"v_and_b32 v245, 15, v248\n"
"s_and_b32 s78, s11, 3\n"
"s_lshr_b32 s79, s11, 2\n"
"s_mul_i32 s76, s79, 1280\n"
"v_lshlrev_b32 v252, 4, v245\n"
"v_add_u32 v252, s76, v252\n"
"s_lshl_b32 s76, s78, 2\n"
"v_add_u32 v246, s76, v244\n"
"v_mul_u32_u24 v251, 144, v246\n"
"v_add_u32_e64 v251, v251, v245\n"
"s_mul_i32 s76, s79, 80\n"
"v_add_u32 v251, s76, v251\n"
"v_lshlrev_b32 v251, 3, v251\n"
"v_add_u32 v251, 139264, v251\n"
"v_and_b32 v253, 3, v245\n"
"v_lshlrev_b32 v253, 1, v253\n"
"v_bfe_u32 v244, v245, 2, 1\n"
"v_or_b32 v253, v253, v244\n"
"v_and_b32 v244, 8, v245\n"
"v_or_b32 v253, v253, v244\n"
"v_xor_b32 v253, v253, v246\n"
"v_lshlrev_b32_e64 v253, 4, v253\n"
"v_lshl_add_u32 v253, v245, 8, v253\n"
"s_mul_i32 s76, s79, 40960\n"
"s_add_u32 s76, s76, 65536\n"
"v_add_u32_e64 v253, s76, v253\n"
"ds_read_b128 v[240:243], v252 offset:49152\n"
"v_mfma_f32_16x16x32_bf16 v[204:207], v[72:75], v[236:239], 0\n"
"s_waitcnt lgkmcnt(2)\n"
"s_nop 0\n"
"v_mfma_f32_16x16x32_bf16 v[176:179], v[4:7], v[224:227], v[176:179]\n"
"ds_read_b128 v[232:235], v250 offset:4096\n"
"v_mfma_f32_16x16x32_bf16 v[180:183], v[76:79], v[224:227], v[180:183]\n"
"s_waitcnt lgkmcnt(2)\n"
"s_nop 0\n"
"v_mfma_f32_16x16x32_bf16 v[184:187], v[4:7], v[228:231], v[184:187]\n"
"ds_read_b128 v[236:239], v250 offset:6144\n"
"v_mfma_f32_16x16x32_bf16 v[188:191], v[76:79], v[228:231], v[188:191]\n"
"s_waitcnt vmcnt(2) lgkmcnt(0)\n"
"s_barrier\n"
"v_mfma_f32_16x16x32_bf16 v[192:195], v[4:7], v[232:235], v[192:195]\n"
"ds_read_b128 v[224:227], v249 offset:16384\n"
"v_mfma_f32_16x16x32_bf16 v[196:199], v[76:79], v[232:235], v[196:199]\n"
"v_mfma_f32_16x16x32_bf16 v[200:203], v[4:7], v[236:239], v[200:203]\n"
"ds_read_b128 v[228:231], v249 offset:18432\n"
"s_add_u32 s36, s36, 128\n"
"s_mov_b32 m0, s43\n"
"s_nop 0\n"
"v_mfma_f32_16x16x32_bf16 v[204:207], v[76:79], v[236:239], v[204:207]\n"
"buffer_load_dwordx4 v254, s[16:19], s36 offen lds\n"
"v_mul_f32 v244, v243, v152\n"
"v_mul_f32 v245, v243, v144\n"
"s_waitcnt lgkmcnt(1)\n"
"s_nop 0\n"
"v_mfma_f32_16x16x32_bf16 v[176:179], v[8:11], v[224:227], v[176:179]\n"
"ds_read_b128 v[232:235], v249 offset:20480\n"
"v_fma_f32 v144, v242, v144, -v244\n"
"v_mfma_f32_16x16x32_bf16 v[180:183], v[80:83], v[224:227], v[180:183]\n"
"v_fma_f32 v152, v242, v152, v245\n"
"s_waitcnt lgkmcnt(1)\n"
"s_nop 0\n"
"v_mfma_f32_16x16x32_bf16 v[184:187], v[8:11], v[228:231], v[184:187]\n"
"ds_read_b128 v[236:239], v249 offset:22528\n"
"v_mul_f32_e64 v244, v243, v153\n"
"v_mfma_f32_16x16x32_bf16 v[188:191], v[80:83], v[228:231], v[188:191]\n"
"v_mul_f32 v245, v243, v145\n"
"s_waitcnt lgkmcnt(1)\n"
"v_mfma_f32_16x16x32_bf16 v[192:195], v[8:11], v[232:235], v[192:195]\n"
"ds_read_b128 v[224:227], v250 offset:16384\n"
"v_fma_f32 v145, v242, v145, -v244\n"
"v_fma_f32 v153, v242, v153, v245\n"
"v_mfma_f32_16x16x32_bf16 v[196:199], v[80:83], v[232:235], v[196:199]\n"
"buffer_load_dwordx4 v255, s[16:19], s36 offen offset:1024 lds\n"
"v_fma_f32 v244, v240, v144, v145\n"
"s_waitcnt lgkmcnt(1)\n"
"s_nop 0\n"
"v_mfma_f32_16x16x32_bf16 v[200:203], v[8:11], v[236:239], v[200:203]\n"
"ds_read_b128 v[228:231], v250 offset:18432\n"
"v_fma_f32 v245, v240, v152, v153\n"
"v_mfma_f32_16x16x32_bf16 v[204:207], v[80:83], v[236:239], v[204:207]\n"
"v_fma_f32 v246, -v241, v152, v244\n"
"s_waitcnt lgkmcnt(1)\n"
"s_nop 0\n"
"v_mfma_f32_16x16x32_bf16 v[176:179], v[12:15], v[224:227], v[176:179]\n"
"ds_read_b128 v[232:235], v250 offset:20480\n"
"v_fma_f32 v247, v241, v144, v245\n"
"v_mul_f32_e64 v244, v243, v154\n"
"v_mfma_f32_16x16x32_bf16 v[180:183], v[84:87], v[224:227], v[180:183]\n"
"v_mul_f32 v245, v243, v146\n"
"s_waitcnt lgkmcnt(1)\n"
"v_mfma_f32_16x16x32_bf16 v[184:187], v[12:15], v[228:231], v[184:187]\n"
"ds_read_b128 v[236:239], v250 offset:22528\n"
"v_fma_f32 v146, v242, v146, -v244\n"
"v_mfma_f32_16x16x32_bf16 v[188:191], v[84:87], v[228:231], v[188:191]\n"
"v_fma_f32 v154, v242, v154, v245\n"
"s_waitcnt vmcnt(2) lgkmcnt(0)\n"
"s_barrier\n"
"v_mfma_f32_16x16x32_bf16 v[192:195], v[12:15], v[232:235], v[192:195]\n"
"ds_read_b128 v[224:227], v249 offset:32768\n"
"v_fma_f32 v244, v240, v246, v146\n"
"v_mfma_f32_16x16x32_bf16 v[196:199], v[84:87], v[232:235], v[196:199]\n"
"v_fma_f32 v245, v240, v247, v154\n"
"v_fma_f32 v244, -v241, v247, v244\n"
"v_mfma_f32_16x16x32_bf16 v[200:203], v[12:15], v[236:239], v[200:203]\n"
"ds_read_b128 v[228:231], v249 offset:34816\n"
"s_add_u32 s36, s36, 128\n"
"s_add_u32 m0, s43, 16384\n"
"v_fma_f32 v247, v241, v246, v245\n"
"v_mfma_f32_16x16x32_bf16 v[204:207], v[84:87], v[236:239], v[204:207]\n"
"buffer_load_dwordx4 v254, s[16:19], s36 offen lds\n"
"v_mov_b32 v246, v244\n"
"s_waitcnt lgkmcnt(1)\n"
"v_mfma_f32_16x16x32_bf16 v[176:179], v[16:19], v[224:227], v[176:179]\n"
"ds_read_b128 v[232:235], v249 offset:36864\n"
"v_mul_f32_e64 v244, v243, v155\n"
"v_mfma_f32_16x16x32_bf16 v[180:183], v[88:91], v[224:227], v[180:183]\n"
"v_mul_f32 v245, v243, v147\n"
"s_waitcnt lgkmcnt(1)\n"
"v_mfma_f32_16x16x32_bf16 v[184:187], v[16:19], v[228:231], v[184:187]\n"
"ds_read_b128 v[236:239], v249 offset:38912\n"
"v_fma_f32 v147, v242, v147, -v244\n"
"v_fma_f32 v155, v242, v155, v245\n"
"v_mfma_f32_16x16x32_bf16 v[188:191], v[88:91], v[228:231], v[188:191]\n"
"v_fma_f32 v244, v240, v246, v147\n"
"s_waitcnt lgkmcnt(1)\n"
"s_nop 0\n"
"v_mfma_f32_16x16x32_bf16 v[192:195], v[16:19], v[232:235], v[192:195]\n"
"ds_read_b128 v[224:227], v250 offset:32768\n"
"v_fma_f32 v245, v240, v247, v155\n"
"v_mfma_f32_16x16x32_bf16 v[196:199], v[88:91], v[232:235], v[196:199]\n"
"buffer_load_dwordx4 v255, s[16:19], s36 offen offset:1024 lds\n"
"v_fma_f32 v244, -v241, v247, v244\n"
"s_waitcnt lgkmcnt(1)\n"
"s_nop 0\n"
"v_mfma_f32_16x16x32_bf16 v[200:203], v[16:19], v[236:239], v[200:203]\n"
"ds_read_b128 v[228:231], v250 offset:34816\n"
"v_fma_f32 v247, v241, v246, v245\n"
"v_mfma_f32_16x16x32_bf16 v[204:207], v[88:91], v[236:239], v[204:207]\n"
"v_mov_b32 v246, v244\n"
"v_mul_f32 v244, v243, v156\n"
"s_waitcnt lgkmcnt(1)\n"
"s_nop 0\n"
"v_mfma_f32_16x16x32_bf16 v[176:179], v[20:23], v[224:227], v[176:179]\n"
"ds_read_b128 v[232:235], v250 offset:36864\n"
"v_mul_f32_e64 v245, v243, v148\n"
"v_mfma_f32_16x16x32_bf16 v[180:183], v[92:95], v[224:227], v[180:183]\n"
"v_fma_f32 v148, v242, v148, -v244\n"
"s_waitcnt lgkmcnt(1)\n"
"s_nop 0\n"
"v_mfma_f32_16x16x32_bf16 v[184:187], v[20:23], v[228:231], v[184:187]\n"
"ds_read_b128 v[236:239], v250 offset:38912\n"
"v_fma_f32 v156, v242, v156, v245\n"
"v_mfma_f32_16x16x32_bf16 v[188:191], v[92:95], v[228:231], v[188:191]\n"
"v_fma_f32 v244, v240, v246, v148\n"
"v_fma_f32 v245, v240, v247, v156\n"
"s_waitcnt vmcnt(2) lgkmcnt(0)\n"
"s_barrier\n"
"v_mfma_f32_16x16x32_bf16 v[192:195], v[20:23], v[232:235], v[192:195]\n"
"ds_read_b128 v[224:227], v249 offset:0\n"
"v_fma_f32 v244, -v241, v247, v244\n"
"v_mfma_f32_16x16x32_bf16 v[196:199], v[92:95], v[232:235], v[196:199]\n"
"v_fma_f32 v247, v241, v246, v245\n"
"v_mfma_f32_16x16x32_bf16 v[200:203], v[20:23], v[236:239], v[200:203]\n"
"ds_read_b128 v[228:231], v249 offset:2048\n"
"s_add_u32 s36, s36, 128\n"
"s_add_u32 m0, s43, 32768\n"
"v_mov_b32_e64 v246, v244\n"
"v_mfma_f32_16x16x32_bf16 v[204:207], v[92:95], v[236:239], v[204:207]\n"
"buffer_load_dwordx4 v254, s[16:19], s36 offen lds\n"
"v_mul_f32 v244, v243, v157\n"
"s_waitcnt lgkmcnt(1)\n"
"v_mfma_f32_16x16x32_bf16 v[176:179], v[24:27], v[224:227], v[176:179]\n"
"ds_read_b128 v[232:235], v249 offset:4096\n"
"v_mul_f32_e64 v245, v243, v149\n"
"v_fma_f32 v149, v242, v149, -v244\n"
"v_mfma_f32_16x16x32_bf16 v[180:183], v[96:99], v[224:227], v[180:183]\n"
"v_fma_f32 v157, v242, v157, v245\n"
"s_waitcnt lgkmcnt(1)\n"
"s_nop 0\n"
"v_mfma_f32_16x16x32_bf16 v[184:187], v[24:27], v[228:231], v[184:187]\n"
"ds_read_b128 v[236:239], v249 offset:6144\n"
"v_fma_f32 v244, v240, v246, v149\n"
"v_mfma_f32_16x16x32_bf16 v[188:191], v[96:99], v[228:231], v[188:191]\n"
"v_fma_f32 v245, v240, v247, v157\n"
"s_waitcnt lgkmcnt(1)\n"
"s_nop 0\n"
"v_mfma_f32_16x16x32_bf16 v[192:195], v[24:27], v[232:235], v[192:195]\n"
"ds_read_b128 v[224:227], v250 offset:0\n"
"v_fma_f32 v244, -v241, v247, v244\n"
"v_mfma_f32_16x16x32_bf16 v[196:199], v[96:99], v[232:235], v[196:199]\n"
"buffer_load_dwordx4 v255, s[16:19], s36 offen offset:1024 lds\n"
"v_fma_f32 v247, v241, v246, v245\n"
"v_mov_b32 v246, v244\n"
"s_waitcnt lgkmcnt(1)\n"
"v_mfma_f32_16x16x32_bf16 v[200:203], v[24:27], v[236:239], v[200:203]\n"
"ds_read_b128 v[228:231], v250 offset:2048\n"
"v_mul_f32_e64 v244, v243, v158\n"
"v_mfma_f32_16x16x32_bf16 v[204:207], v[96:99], v[236:239], v[204:207]\n"
"v_mul_f32 v245, v243, v150\n"
"s_waitcnt lgkmcnt(1)\n"
"v_mfma_f32_16x16x32_bf16 v[176:179], v[28:31], v[224:227], v[176:179]\n"
"ds_read_b128 v[232:235], v250 offset:4096\n"
"v_fma_f32 v150, v242, v150, -v244\n"
"v_mfma_f32_16x16x32_bf16 v[180:183], v[100:103], v[224:227], v[180:183]\n"
"v_fma_f32 v158, v242, v158, v245\n"
"s_waitcnt lgkmcnt(1)\n"
"s_nop 0\n"
"v_mfma_f32_16x16x32_bf16 v[184:187], v[28:31], v[228:231], v[184:187]\n"
"ds_read_b128 v[236:239], v250 offset:6144\n"
"v_fma_f32 v244, v240, v246, v150\n"
"v_fma_f32 v245, v240, v247, v158\n"
"v_mfma_f32_16x16x32_bf16 v[188:191], v[100:103], v[228:231], v[188:191]\n"
"v_fma_f32 v244, -v241, v247, v244\n"
"s_waitcnt vmcnt(2) lgkmcnt(0)\n"
"s_barrier\n"
"v_mfma_f32_16x16x32_bf16 v[192:195], v[28:31], v[232:235], v[192:195]\n"
"ds_read_b128 v[224:227], v249 offset:16384\n"
"v_fma_f32 v247, v241, v246, v245\n"
"v_mfma_f32_16x16x32_bf16 v[196:199], v[100:103], v[232:235], v[196:199]\n"
"v_mov_b32_e64 v246, v244\n"
"v_mfma_f32_16x16x32_bf16 v[200:203], v[28:31], v[236:239], v[200:203]\n"
"ds_read_b128 v[228:231], v249 offset:18432\n"
"s_add_u32 s36, s36, 128\n"
"s_mov_b32 m0, s43\n"
"v_mul_f32 v244, v243, v159\n"
"v_mul_f32_e64 v245, v243, v151\n"
"v_mfma_f32_16x16x32_bf16 v[204:207], v[100:103], v[236:239], v[204:207]\n"
"buffer_load_dwordx4 v254, s[16:19], s36 offen lds\n"
"v_fma_f32 v151, v242, v151, -v244\n"
"s_waitcnt lgkmcnt(1)\n"
"s_nop 0\n"
"v_mfma_f32_16x16x32_bf16 v[176:179], v[32:35], v[224:227], v[176:179]\n"
"ds_read_b128 v[232:235], v249 offset:20480\n"
"v_fma_f32 v159, v242, v159, v245\n"
"v_mfma_f32_16x16x32_bf16 v[180:183], v[104:107], v[224:227], v[180:183]\n"
"v_fma_f32 v244, v240, v246, v151\n"
"s_waitcnt lgkmcnt(1)\n"
"s_nop 0\n"
"v_mfma_f32_16x16x32_bf16 v[184:187], v[32:35], v[228:231], v[184:187]\n"
"ds_read_b128 v[236:239], v249 offset:22528\n"
"v_fma_f32 v245, v240, v247, v159\n"
"v_mfma_f32_16x16x32_bf16 v[188:191], v[104:107], v[228:231], v[188:191]\n"
"v_fma_f32 v244, -v241, v247, v244\n"
"v_fma_f32 v247, v241, v246, v245\n"
"s_waitcnt lgkmcnt(1)\n"
"s_nop 0\n"
"v_mfma_f32_16x16x32_bf16 v[192:195], v[32:35], v[232:235], v[192:195]\n"
"ds_read_b128 v[224:227], v250 offset:16384\n"
"v_mov_b32_e64 v246, v244\n"
"v_mfma_f32_16x16x32_bf16 v[196:199], v[104:107], v[232:235], v[196:199]\n"
"buffer_load_dwordx4 v255, s[16:19], s36 offen offset:1024 lds\n"
"ds_write_b64 v251, v[246:247] offset:0\n"
"s_waitcnt lgkmcnt(2)\n"
"s_nop 0\n"
"v_mfma_f32_16x16x32_bf16 v[200:203], v[32:35], v[236:239], v[200:203]\n"
"ds_read_b128 v[228:231], v250 offset:18432\n"
"ds_read_b128 v[240:243], v252 offset:49408\n"
"v_mfma_f32_16x16x32_bf16 v[204:207], v[104:107], v[236:239], v[204:207]\n"
"s_waitcnt lgkmcnt(3)\n"
"s_nop 0\n"
"v_mfma_f32_16x16x32_bf16 v[176:179], v[36:39], v[224:227], v[176:179]\n"
"ds_read_b128 v[232:235], v250 offset:20480\n"
"v_mfma_f32_16x16x32_bf16 v[180:183], v[108:111], v[224:227], v[180:183]\n"
"s_waitcnt lgkmcnt(2)\n"
"s_nop 0\n"
"v_mfma_f32_16x16x32_bf16 v[184:187], v[36:39], v[228:231], v[184:187]\n"
"ds_read_b128 v[236:239], v250 offset:22528\n"
"v_mfma_f32_16x16x32_bf16 v[188:191], v[108:111], v[228:231], v[188:191]\n"
"s_waitcnt vmcnt(2) lgkmcnt(0)\n"
"s_barrier\n"
"v_mfma_f32_16x16x32_bf16 v[192:195], v[36:39], v[232:235], v[192:195]\n"
"ds_read_b128 v[224:227], v249 offset:32768\n"
"v_mfma_f32_16x16x32_bf16 v[196:199], v[108:111], v[232:235], v[196:199]\n"
"v_mfma_f32_16x16x32_bf16 v[200:203], v[36:39], v[236:239], v[200:203]\n"
"ds_read_b128 v[228:231], v249 offset:34816\n"
"s_add_u32 s36, s36, 128\n"
"s_add_u32 m0, s43, 16384\n"
"v_mul_f32_e64 v244, v243, v168\n"
"v_mfma_f32_16x16x32_bf16 v[204:207], v[108:111], v[236:239], v[204:207]\n"
"buffer_load_dwordx4 v254, s[16:19], s36 offen lds\n"
"v_mul_f32 v245, v243, v160\n"
"s_waitcnt lgkmcnt(1)\n"
"v_mfma_f32_16x16x32_bf16 v[176:179], v[40:43], v[224:227], v[176:179]\n"
"ds_read_b128 v[232:235], v249 offset:36864\n"
"v_fma_f32 v160, v242, v160, -v244\n"
"v_mfma_f32_16x16x32_bf16 v[180:183], v[112:115], v[224:227], v[180:183]\n"
"v_fma_f32 v168, v242, v168, v245\n"
"v_mul_f32 v244, v243, v169\n"
"s_waitcnt lgkmcnt(1)\n"
"v_mfma_f32_16x16x32_bf16 v[184:187], v[40:43], v[228:231], v[184:187]\n"
"ds_read_b128 v[236:239], v249 offset:38912\n"
"v_mul_f32_e64 v245, v243, v161\n"
"v_mfma_f32_16x16x32_bf16 v[188:191], v[112:115], v[228:231], v[188:191]\n"
"v_fma_f32 v161, v242, v161, -v244\n"
"s_waitcnt lgkmcnt(1)\n"
"s_nop 0\n"
"v_mfma_f32_16x16x32_bf16 v[192:195], v[40:43], v[232:235], v[192:195]\n"
"ds_read_b128 v[224:227], v250 offset:32768\n"
"v_fma_f32 v169, v242, v169, v245\n"
"v_mfma_f32_16x16x32_bf16 v[196:199], v[112:115], v[232:235], v[196:199]\n"
"buffer_load_dwordx4 v255, s[16:19], s36 offen offset:1024 lds\n"
"v_fma_f32 v244, v240, v160, v161\n"
"s_waitcnt lgkmcnt(1)\n"
"s_nop 0\n"
"v_mfma_f32_16x16x32_bf16 v[200:203], v[40:43], v[236:239], v[200:203]\n"
"ds_read_b128 v[228:231], v250 offset:34816\n"
"v_fma_f32 v245, v240, v168, v169\n"
"v_fma_f32 v246, -v241, v168, v244\n"
"v_mfma_f32_16x16x32_bf16 v[204:207], v[112:115], v[236:239], v[204:207]\n"
"v_fma_f32 v247, v241, v160, v245\n"
"s_waitcnt lgkmcnt(1)\n"
"s_nop 0\n"
"v_mfma_f32_16x16x32_bf16 v[176:179], v[44:47], v[224:227], v[176:179]\n"
"ds_read_b128 v[232:235], v250 offset:36864\n"
"v_mul_f32_e64 v244, v243, v170\n"
"v_mfma_f32_16x16x32_bf16 v[180:183], v[116:119], v[224:227], v[180:183]\n"
"v_mul_f32 v245, v243, v162\n"
"s_waitcnt lgkmcnt(1)\n"
"v_mfma_f32_16x16x32_bf16 v[184:187], v[44:47], v[228:231], v[184:187]\n"
"ds_read_b128 v[236:239], v250 offset:38912\n"
"v_fma_f32 v162, v242, v162, -v244\n"
"v_mfma_f32_16x16x32_bf16 v[188:191], v[116:119], v[228:231], v[188:191]\n"
"v_fma_f32 v170, v242, v170, v245\n"
"v_fma_f32 v244, v240, v246, v162\n"
"s_waitcnt vmcnt(2) lgkmcnt(0)\n"
"s_barrier\n"
"v_mfma_f32_16x16x32_bf16 v[192:195], v[44:47], v[232:235], v[192:195]\n"
"ds_read_b128 v[224:227], v249 offset:0\n"
"v_fma_f32 v245, v240, v247, v170\n"
"v_mfma_f32_16x16x32_bf16 v[196:199], v[116:119], v[232:235], v[196:199]\n"
"v_fma_f32 v244, -v241, v247, v244\n"
"v_mfma_f32_16x16x32_bf16 v[200:203], v[44:47], v[236:239], v[200:203]\n"
"ds_read_b128 v[228:231], v249 offset:2048\n"
"s_add_u32 s36, s36, 128\n"
"s_add_u32 m0, s43, 32768\n"
"v_fma_f32 v247, v241, v246, v245\n"
"v_mfma_f32_16x16x32_bf16 v[204:207], v[116:119], v[236:239], v[204:207]\n"
"buffer_load_dwordx4 v254, s[16:19], s36 offen lds\n"
"v_mov_b32 v246, v244\n"
"s_waitcnt lgkmcnt(1)\n"
"v_mfma_f32_16x16x32_bf16 v[176:179], v[48:51], v[224:227], v[176:179]\n"
"ds_read_b128 v[232:235], v249 offset:4096\n"
"v_mul_f32 v244, v243, v171\n"
"v_mul_f32 v245, v243, v163\n"
"v_mfma_f32_16x16x32_bf16 v[180:183], v[120:123], v[224:227], v[180:183]\n"
"v_fma_f32 v163, v242, v163, -v244\n"
"s_waitcnt lgkmcnt(1)\n"
"s_nop 0\n"
"v_mfma_f32_16x16x32_bf16 v[184:187], v[48:51], v[228:231], v[184:187]\n"
"ds_read_b128 v[236:239], v249 offset:6144\n"
"v_fma_f32 v171, v242, v171, v245\n"
"v_mfma_f32_16x16x32_bf16 v[188:191], v[120:123], v[228:231], v[188:191]\n"
"v_fma_f32 v244, v240, v246, v163\n"
"s_waitcnt lgkmcnt(1)\n"
"s_nop 0\n"
"v_mfma_f32_16x16x32_bf16 v[192:195], v[48:51], v[232:235], v[192:195]\n"
"ds_read_b128 v[224:227], v250 offset:0\n"
"v_fma_f32 v245, v240, v247, v171\n"
"v_fma_f32 v244, -v241, v247, v244\n"
"v_mfma_f32_16x16x32_bf16 v[196:199], v[120:123], v[232:235], v[196:199]\n"
"buffer_load_dwordx4 v255, s[16:19], s36 offen offset:1024 lds\n"
"v_fma_f32 v247, v241, v246, v245\n"
"s_waitcnt lgkmcnt(1)\n"
"s_nop 0\n"
"v_mfma_f32_16x16x32_bf16 v[200:203], v[48:51], v[236:239], v[200:203]\n"
"ds_read_b128 v[228:231], v250 offset:2048\n"
"v_mov_b32_e64 v246, v244\n"
"v_mfma_f32_16x16x32_bf16 v[204:207], v[120:123], v[236:239], v[204:207]\n"
"v_mul_f32 v244, v243, v172\n"
"s_waitcnt lgkmcnt(1)\n"
"v_mfma_f32_16x16x32_bf16 v[176:179], v[52:55], v[224:227], v[176:179]\n"
"ds_read_b128 v[232:235], v250 offset:4096\n"
"v_mul_f32_e64 v245, v243, v164\n"
"v_mfma_f32_16x16x32_bf16 v[180:183], v[124:127], v[224:227], v[180:183]\n"
"v_fma_f32 v164, v242, v164, -v244\n"
"v_fma_f32 v172, v242, v172, v245\n"
"s_waitcnt lgkmcnt(1)\n"
"s_nop 0\n"
"v_mfma_f32_16x16x32_bf16 v[184:187], v[52:55], v[228:231], v[184:187]\n"
"ds_read_b128 v[236:239], v250 offset:6144\n"
"v_fma_f32 v244, v240, v246, v164\n"
"v_mfma_f32_16x16x32_bf16 v[188:191], v[124:127], v[228:231], v[188:191]\n"
"v_fma_f32 v245, v240, v247, v172\n"
"s_waitcnt vmcnt(2) lgkmcnt(0)\n"
"s_barrier\n"
"v_mfma_f32_16x16x32_bf16 v[192:195], v[52:55], v[232:235], v[192:195]\n"
"ds_read_b128 v[224:227], v249 offset:16384\n"
"v_fma_f32 v244, -v241, v247, v244\n"
"v_mfma_f32_16x16x32_bf16 v[196:199], v[124:127], v[232:235], v[196:199]\n"
"v_fma_f32 v247, v241, v246, v245\n"
"v_mfma_f32_16x16x32_bf16 v[200:203], v[52:55], v[236:239], v[200:203]\n"
"ds_read_b128 v[228:231], v249 offset:18432\n"
"s_mov_b32 s36, s66\n"
"s_mov_b32 m0, s43\n"
"v_mov_b32 v246, v244\n"
"v_mul_f32 v244, v243, v173\n"
"v_mfma_f32_16x16x32_bf16 v[204:207], v[124:127], v[236:239], v[204:207]\n"
"buffer_load_dwordx4 v254, s[16:19], s36 offen lds\n"
"v_mul_f32 v245, v243, v165\n"
"s_waitcnt lgkmcnt(1)\n"
"v_mfma_f32_16x16x32_bf16 v[176:179], v[56:59], v[224:227], v[176:179]\n"
"ds_read_b128 v[232:235], v249 offset:20480\n"
"v_fma_f32 v165, v242, v165, -v244\n"
"v_mfma_f32_16x16x32_bf16 v[180:183], v[128:131], v[224:227], v[180:183]\n"
"v_fma_f32 v173, v242, v173, v245\n"
"s_waitcnt lgkmcnt(1)\n"
"s_nop 0\n"
"v_mfma_f32_16x16x32_bf16 v[184:187], v[56:59], v[228:231], v[184:187]\n"
"ds_read_b128 v[236:239], v249 offset:22528\n"
"v_fma_f32 v244, v240, v246, v165\n"
"v_mfma_f32_16x16x32_bf16 v[188:191], v[128:131], v[228:231], v[188:191]\n"
"v_fma_f32 v245, v240, v247, v173\n"
"v_fma_f32 v244, -v241, v247, v244\n"
"s_waitcnt lgkmcnt(1)\n"
"s_nop 0\n"
"v_mfma_f32_16x16x32_bf16 v[192:195], v[56:59], v[232:235], v[192:195]\n"
"ds_read_b128 v[224:227], v250 offset:16384\n"
"v_fma_f32 v247, v241, v246, v245\n"
"v_mfma_f32_16x16x32_bf16 v[196:199], v[128:131], v[232:235], v[196:199]\n"
"buffer_load_dwordx4 v255, s[16:19], s36 offen offset:1024 lds\n"
"v_mov_b32 v246, v244\n"
"s_waitcnt lgkmcnt(1)\n"
"v_mfma_f32_16x16x32_bf16 v[200:203], v[56:59], v[236:239], v[200:203]\n"
"ds_read_b128 v[228:231], v250 offset:18432\n"
"v_mul_f32_e64 v244, v243, v174\n"
"v_mfma_f32_16x16x32_bf16 v[204:207], v[128:131], v[236:239], v[204:207]\n"
"v_mul_f32_e64 v245, v243, v166\n"
"v_fma_f32 v166, v242, v166, -v244\n"
"s_waitcnt lgkmcnt(1)\n"
"s_nop 0\n"
"v_mfma_f32_16x16x32_bf16 v[176:179], v[60:63], v[224:227], v[176:179]\n"
"ds_read_b128 v[232:235], v250 offset:20480\n"
"v_fma_f32 v174, v242, v174, v245\n"
"v_mfma_f32_16x16x32_bf16 v[180:183], v[132:135], v[224:227], v[180:183]\n"
"v_fma_f32 v244, v240, v246, v166\n"
"s_waitcnt lgkmcnt(1)\n"
"s_nop 0\n"
"v_mfma_f32_16x16x32_bf16 v[184:187], v[60:63], v[228:231], v[184:187]\n"
"ds_read_b128 v[236:239], v250 offset:22528\n"
"v_fma_f32 v245, v240, v247, v174\n"
"v_mfma_f32_16x16x32_bf16 v[188:191], v[132:135], v[228:231], v[188:191]\n"
"v_fma_f32 v244, -v241, v247, v244\n"
"s_waitcnt vmcnt(2) lgkmcnt(0)\n"
"s_barrier\n"
"v_mfma_f32_16x16x32_bf16 v[192:195], v[60:63], v[232:235], v[192:195]\n"
"ds_read_b128 v[224:227], v249 offset:32768\n"
"v_fma_f32 v247, v241, v246, v245\n"
"v_mov_b32_e64 v246, v244\n"
"v_mfma_f32_16x16x32_bf16 v[196:199], v[132:135], v[232:235], v[196:199]\n"
"v_mul_f32_e64 v244, v243, v175\n"
"v_mfma_f32_16x16x32_bf16 v[200:203], v[60:63], v[236:239], v[200:203]\n"
"ds_read_b128 v[228:231], v249 offset:34816\n"
"s_add_u32 s36, s36, 384\n"
"s_add_u32 m0, s43, 16384\n"
"v_mul_f32_e64 v245, v243, v167\n"
"v_mfma_f32_16x16x32_bf16 v[204:207], v[132:135], v[236:239], v[204:207]\n"
"buffer_load_dwordx4 v254, s[16:19], s36 offen lds\n"
"v_fma_f32 v167, v242, v167, -v244\n"
"s_waitcnt lgkmcnt(1)\n"
"s_nop 0\n"
"v_mfma_f32_16x16x32_bf16 v[176:179], v[64:67], v[224:227], v[176:179]\n"
"ds_read_b128 v[232:235], v249 offset:36864\n"
"v_fma_f32 v175, v242, v175, v245\n"
"v_mfma_f32_16x16x32_bf16 v[180:183], v[136:139], v[224:227], v[180:183]\n"
"v_fma_f32 v244, v240, v246, v167\n"
"v_fma_f32 v245, v240, v247, v175\n"
"s_waitcnt lgkmcnt(1)\n"
"s_nop 0\n"
"v_mfma_f32_16x16x32_bf16 v[184:187], v[64:67], v[228:231], v[184:187]\n"
"ds_read_b128 v[236:239], v249 offset:38912\n"
"v_fma_f32 v244, -v241, v247, v244\n"
"v_mfma_f32_16x16x32_bf16 v[188:191], v[136:139], v[228:231], v[188:191]\n"
"v_fma_f32 v247, v241, v246, v245\n"
"s_waitcnt lgkmcnt(1)\n"
"s_nop 0\n"
"v_mfma_f32_16x16x32_bf16 v[192:195], v[64:67], v[232:235], v[192:195]\n"
"ds_read_b128 v[224:227], v250 offset:32768\n"
"v_mov_b32_e64 v246, v244\n"
"v_mfma_f32_16x16x32_bf16 v[196:199], v[136:139], v[232:235], v[196:199]\n"
"buffer_load_dwordx4 v255, s[16:19], s36 offen offset:1024 lds\n"
"ds_write_b64 v251, v[246:247] offset:128\n"
"s_waitcnt lgkmcnt(2)\n"
"s_nop 0\n"
"v_mfma_f32_16x16x32_bf16 v[200:203], v[64:67], v[236:239], v[200:203]\n"
"ds_read_b128 v[228:231], v250 offset:34816\n"
"v_mfma_f32_16x16x32_bf16 v[204:207], v[136:139], v[236:239], v[204:207]\n"
"s_waitcnt lgkmcnt(2)\n"
"s_nop 0\n"
"v_mfma_f32_16x16x32_bf16 v[176:179], v[68:71], v[224:227], v[176:179]\n"
"ds_read_b128 v[232:235], v250 offset:36864\n"
"v_mfma_f32_16x16x32_bf16 v[180:183], v[140:143], v[224:227], v[180:183]\n"
"s_waitcnt lgkmcnt(1)\n"
"s_nop 0\n"
"v_mfma_f32_16x16x32_bf16 v[184:187], v[68:71], v[228:231], v[184:187]\n"
"ds_read_b128 v[236:239], v250 offset:38912\n"
"v_mfma_f32_16x16x32_bf16 v[188:191], v[140:143], v[228:231], v[188:191]\n"
"s_waitcnt vmcnt(2) lgkmcnt(0)\n"
"s_barrier\n"
"v_mfma_f32_16x16x32_bf16 v[192:195], v[68:71], v[232:235], v[192:195]\n"
"ds_read_b128 v[224:227], v249 offset:0\n"
"v_mfma_f32_16x16x32_bf16 v[196:199], v[140:143], v[232:235], v[196:199]\n"
"v_mfma_f32_16x16x32_bf16 v[200:203], v[68:71], v[236:239], v[200:203]\n"
"ds_read_b128 v[228:231], v249 offset:2048\n"
"s_add_u32 s36, s36, 384\n"
"s_add_u32 m0, s43, 32768\n"
"v_mfma_f32_16x16x32_bf16 v[204:207], v[140:143], v[236:239], v[204:207]\n"
"buffer_load_dwordx4 v254, s[16:19], s36 offen lds\n"
"s_cmp_ge_u32 s11, 4\n"
"s_cbranch_scc1 S1_IDLE\n"
"s_waitcnt lgkmcnt(1)\n"
"s_nop 0\n"
"v_mfma_f32_16x16x32_bf16 v[208:211], v[0:3], v[224:227], 0\n"
"ds_read_b128 v[232:235], v250 offset:0\n"
"v_mfma_f32_16x16x32_bf16 v[212:215], v[72:75], v[224:227], 0\n"
"s_waitcnt lgkmcnt(1)\n"
"s_nop 0\n"
"v_mfma_f32_16x16x32_bf16 v[216:219], v[0:3], v[228:231], 0\n"
"ds_read_b128 v[236:239], v250 offset:2048\n"
"v_mfma_f32_16x16x32_bf16 v[220:223], v[72:75], v[228:231], 0\n"
"s_waitcnt lgkmcnt(1)\n"
"s_nop 0\n"
"v_mfma_f32_16x16x32_bf16 v[208:211], v[4:7], v[232:235], v[208:211]\n"
"ds_read_b128 v[224:227], v249 offset:4096\n"
"v_mfma_f32_16x16x32_bf16 v[212:215], v[76:79], v[232:235], v[212:215]\n"
"buffer_load_dwordx4 v255, s[16:19], s36 offen offset:1024 lds\n"
"s_waitcnt lgkmcnt(1)\n"
"s_nop 0\n"
"v_mfma_f32_16x16x32_bf16 v[216:219], v[4:7], v[236:239], v[216:219]\n"
"ds_read_b128 v[228:231], v249 offset:6144\n"
"ds_read_b128 v[240:243], v252 offset:49664\n"
"v_mfma_f32_16x16x32_bf16 v[220:223], v[76:79], v[236:239], v[220:223]\n"
"s_waitcnt lgkmcnt(2)\n"
"s_nop 0\n"
"v_mfma_f32_16x16x32_bf16 v[208:211], v[8:11], v[224:227], v[208:211]\n"
"ds_read_b128 v[232:235], v250 offset:4096\n"
"v_mfma_f32_16x16x32_bf16 v[212:215], v[80:83], v[224:227], v[212:215]\n"
"s_waitcnt lgkmcnt(1)\n"
"s_nop 0\n"
"v_mfma_f32_16x16x32_bf16 v[216:219], v[8:11], v[228:231], v[216:219]\n"
"ds_read_b128 v[236:239], v250 offset:6144\n"
"v_mul_f32 v244, v243, v184\n"
"v_mul_f32 v245, v243, v176\n"
"v_fma_f32 v176, v242, v176, -v244\n"
"v_mfma_f32_16x16x32_bf16 v[220:223], v[80:83], v[228:231], v[220:223]\n"
"v_fma_f32 v184, v242, v184, v245\n"
"v_mul_f32 v244, v243, v185\n"
"s_waitcnt lgkmcnt(1)\n"
"v_mfma_f32_16x16x32_bf16 v[208:211], v[12:15], v[232:235], v[208:211]\n"
"ds_read_b128 v[224:227], v249 offset:8192\n"
"v_mul_f32_e64 v245, v243, v177\n"
"v_fma_f32 v177, v242, v177, -v244\n"
"v_mfma_f32_16x16x32_bf16 v[212:215], v[84:87], v[232:235], v[212:215]\n"
"v_fma_f32 v185, v242, v185, v245\n"
"v_fma_f32 v244, v240, v176, v177\n"
"v_fma_f32 v245, v240, v184, v185\n"
"s_waitcnt lgkmcnt(1)\n"
"s_nop 0\n"
"v_mfma_f32_16x16x32_bf16 v[216:219], v[12:15], v[236:239], v[216:219]\n"
"ds_read_b128 v[228:231], v249 offset:10240\n"
"v_fma_f32 v246, -v241, v184, v244\n"
"v_fma_f32 v247, v241, v176, v245\n"
"v_mfma_f32_16x16x32_bf16 v[220:223], v[84:87], v[236:239], v[220:223]\n"
"v_mul_f32 v244, v243, v186\n"
"v_mul_f32 v245, v243, v178\n"
"v_fma_f32 v178, v242, v178, -v244\n"
"s_waitcnt lgkmcnt(1)\n"
"s_nop 0\n"
"v_mfma_f32_16x16x32_bf16 v[208:211], v[16:19], v[224:227], v[208:211]\n"
"ds_read_b128 v[232:235], v250 offset:8192\n"
"v_fma_f32 v186, v242, v186, v245\n"
"v_fma_f32 v244, v240, v246, v178\n"
"v_mfma_f32_16x16x32_bf16 v[212:215], v[88:91], v[224:227], v[212:215]\n"
"v_fma_f32 v245, v240, v247, v186\n"
"v_fma_f32 v244, -v241, v247, v244\n"
"s_waitcnt lgkmcnt(1)\n"
"s_nop 0\n"
"v_mfma_f32_16x16x32_bf16 v[216:219], v[16:19], v[228:231], v[216:219]\n"
"ds_read_b128 v[236:239], v250 offset:10240\n"
"v_fma_f32 v247, v241, v246, v245\n"
"v_mov_b32 v246, v244\n"
"v_mul_f32 v244, v243, v187\n"
"v_mfma_f32_16x16x32_bf16 v[220:223], v[88:91], v[228:231], v[220:223]\n"
"v_mul_f32_e64 v245, v243, v179\n"
"v_fma_f32 v179, v242, v179, -v244\n"
"s_waitcnt vmcnt(2) lgkmcnt(0)\n"
"s_barrier\n"
"v_mfma_f32_16x16x32_bf16 v[208:211], v[20:23], v[232:235], v[208:211]\n"
"ds_read_b128 v[224:227], v249 offset:16384\n"
"v_fma_f32 v187, v242, v187, v245\n"
"v_fma_f32 v244, v240, v246, v179\n"
"v_fma_f32 v245, v240, v247, v187\n"
"v_mfma_f32_16x16x32_bf16 v[212:215], v[92:95], v[232:235], v[212:215]\n"
"v_fma_f32 v244, -v241, v247, v244\n"
"v_fma_f32 v247, v241, v246, v245\n"
"v_mfma_f32_16x16x32_bf16 v[216:219], v[20:23], v[236:239], v[216:219]\n"
"ds_read_b128 v[228:231], v249 offset:18432\n"
"v_mov_b32 v246, v244\n"
"v_mul_f32 v244, v243, v188\n"
"v_mfma_f32_16x16x32_bf16 v[220:223], v[92:95], v[236:239], v[220:223]\n"
"s_mov_b32 s16, s24\n"
"s_mov_b32 s17, s25\n"
"s_mov_b32 s18, s26\n"
"s_mov_b32 s19, s27\n"
"s_mov_b32 s70, 0\n"
"s_lshl_b32 s68, s82, 8\n"
"s_add_u32 s69, s68, 128\n"
"s_and_b32 s76, s11, 3\n"
"s_nop 0\n"
"s_mul_i32 s76, s76, 18432\n"
"v_add_u32 v254, s76, v254\n"
"v_add_u32 v255, s76, v255\n"
"s_add_u32 s36, s70, s68\n"
"s_mov_b32 m0, s43\n"
"s_nop 0\n"
"s_nop 0\n"
"buffer_load_dwordx4 v254, s[16:19], s36 offen lds\n"
"v_mul_f32_e64 v245, v243, v180\n"
"v_fma_f32 v180, v242, v180, -v244\n"
"v_fma_f32 v188, v242, v188, v245\n"
"s_waitcnt lgkmcnt(1)\n"
"s_nop 0\n"
"v_mfma_f32_16x16x32_bf16 v[208:211], v[24:27], v[224:227], v[208:211]\n"
"ds_read_b128 v[232:235], v250 offset:16384\n"
"v_fma_f32 v244, v240, v246, v180\n"
"v_fma_f32 v245, v240, v247, v188\n"
"v_mfma_f32_16x16x32_bf16 v[212:215], v[96:99], v[224:227], v[212:215]\n"
"buffer_load_dwordx4 v255, s[16:19], s36 offen offset:1024 lds\n"
"v_fma_f32 v244, -v241, v247, v244\n"
"v_fma_f32 v247, v241, v246, v245\n"
"v_mov_b32 v246, v244\n"
"s_waitcnt lgkmcnt(1)\n"
"v_mfma_f32_16x16x32_bf16 v[216:219], v[24:27], v[228:231], v[216:219]\n"
"ds_read_b128 v[236:239], v250 offset:18432\n"
"v_mul_f32 v244, v243, v189\n"
"v_mul_f32 v245, v243, v181\n"
"v_mfma_f32_16x16x32_bf16 v[220:223], v[96:99], v[228:231], v[220:223]\n"
"v_fma_f32 v181, v242, v181, -v244\n"
"v_fma_f32 v189, v242, v189, v245\n"
"s_waitcnt lgkmcnt(1)\n"
"s_nop 0\n"
"v_mfma_f32_16x16x32_bf16 v[208:211], v[28:31], v[232:235], v[208:211]\n"
"ds_read_b128 v[224:227], v249 offset:20480\n"
"v_fma_f32 v244, v240, v246, v181\n"
"v_fma_f32 v245, v240, v247, v189\n"
"v_fma_f32 v244, -v241, v247, v244\n"
"v_mfma_f32_16x16x32_bf16 v[212:215], v[100:103], v[232:235], v[212:215]\n"
"v_fma_f32 v247, v241, v246, v245\n"
"v_mov_b32 v246, v244\n"
"s_waitcnt lgkmcnt(1)\n"
"v_mfma_f32_16x16x32_bf16 v[216:219], v[28:31], v[236:239], v[216:219]\n"
"ds_read_b128 v[228:231], v249 offset:22528\n"
"v_mul_f32 v244, v243, v190\n"
"v_mul_f32 v245, v243, v182\n"
"v_fma_f32 v182, v242, v182, -v244\n"
"v_mfma_f32_16x16x32_bf16 v[220:223], v[100:103], v[236:239], v[220:223]\n"
"v_fma_f32 v190, v242, v190, v245\n"
"v_fma_f32 v244, v240, v246, v182\n"
"s_waitcnt lgkmcnt(1)\n"
"s_nop 0\n"
"v_mfma_f32_16x16x32_bf16 v[208:211], v[32:35], v[224:227], v[208:211]\n"
"ds_read_b128 v[232:235], v250 offset:20480\n"
"v_fma_f32 v245, v240, v247, v190\n"
"v_fma_f32 v244, -v241, v247, v244\n"
"v_mfma_f32_16x16x32_bf16 v[212:215], v[104:107], v[224:227], v[212:215]\n"
"v_fma_f32 v247, v241, v246, v245\n"
"v_mov_b32 v246, v244\n"
"v_mul_f32 v244, v243, v191\n"
"s_waitcnt lgkmcnt(1)\n"
"s_nop 0\n"
"v_mfma_f32_16x16x32_bf16 v[216:219], v[32:35], v[228:231], v[216:219]\n"
"ds_read_b128 v[236:239], v250 offset:22528\n"
"v_mul_f32_e64 v245, v243, v183\n"
"v_fma_f32 v183, v242, v183, -v244\n"
"v_mfma_f32_16x16x32_bf16 v[220:223], v[104:107], v[228:231], v[220:223]\n"
"v_fma_f32 v191, v242, v191, v245\n"
"v_fma_f32 v244, v240, v246, v183\n"
"v_fma_f32 v245, v240, v247, v191\n"
"s_waitcnt lgkmcnt(1)\n"
"s_nop 0\n"
"v_mfma_f32_16x16x32_bf16 v[208:211], v[36:39], v[232:235], v[208:211]\n"
"ds_read_b128 v[224:227], v249 offset:24576\n"
"v_fma_f32 v244, -v241, v247, v244\n"
"v_fma_f32 v247, v241, v246, v245\n"
"v_mfma_f32_16x16x32_bf16 v[212:215], v[108:111], v[232:235], v[212:215]\n"
"v_mov_b32_e64 v246, v244\n"
"ds_write_b64 v251, v[246:247] offset:256\n"
"s_waitcnt lgkmcnt(2)\n"
"s_nop 0\n"
"v_mfma_f32_16x16x32_bf16 v[216:219], v[36:39], v[236:239], v[216:219]\n"
"ds_read_b128 v[228:231], v249 offset:26624\n"
"ds_read_b128 v[240:243], v252 offset:49920\n"
"v_mfma_f32_16x16x32_bf16 v[220:223], v[108:111], v[236:239], v[220:223]\n"
"s_waitcnt lgkmcnt(3)\n"
"s_nop 0\n"
"v_mfma_f32_16x16x32_bf16 v[208:211], v[40:43], v[224:227], v[208:211]\n"
"ds_read_b128 v[232:235], v250 offset:24576\n"
"v_mfma_f32_16x16x32_bf16 v[212:215], v[112:115], v[224:227], v[212:215]\n"
"s_waitcnt lgkmcnt(1)\n"
"s_nop 0\n"
"v_mfma_f32_16x16x32_bf16 v[216:219], v[40:43], v[228:231], v[216:219]\n"
"ds_read_b128 v[236:239], v250 offset:26624\n"
"v_mul_f32 v244, v243, v200\n"
"v_mul_f32 v245, v243, v192\n"
"v_fma_f32 v192, v242, v192, -v244\n"
"v_mfma_f32_16x16x32_bf16 v[220:223], v[112:115], v[228:231], v[220:223]\n"
"v_fma_f32 v200, v242, v200, v245\n"
"v_mul_f32 v244, v243, v201\n"
"s_waitcnt vmcnt(2) lgkmcnt(0)\n"
"s_barrier\n"
"s_nop 0\n"
"v_mfma_f32_16x16x32_bf16 v[208:211], v[44:47], v[232:235], v[208:211]\n"
"ds_read_b128 v[224:227], v249 offset:32768\n"
"v_mul_f32_e64 v245, v243, v193\n"
"v_fma_f32 v193, v242, v193, -v244\n"
"v_mfma_f32_16x16x32_bf16 v[212:215], v[116:119], v[232:235], v[212:215]\n"
"v_fma_f32 v201, v242, v201, v245\n"
"v_fma_f32 v244, v240, v192, v193\n"
"v_fma_f32 v245, v240, v200, v201\n"
"v_mfma_f32_16x16x32_bf16 v[216:219], v[44:47], v[236:239], v[216:219]\n"
"ds_read_b128 v[228:231], v249 offset:34816\n"
"v_fma_f32 v246, -v241, v200, v244\n"
"v_fma_f32 v247, v241, v192, v245\n"
"v_mfma_f32_16x16x32_bf16 v[220:223], v[116:119], v[236:239], v[220:223]\n"
"s_add_u32 s36, s70, s69\n"
"s_nop 0\n"
"s_add_u32 m0, s43, 16384\n"
"s_nop 0\n"
"s_nop 0\n"
"buffer_load_dwordx4 v254, s[16:19], s36 offen lds\n"
"v_mul_f32 v244, v243, v202\n"
"v_mul_f32 v245, v243, v194\n"
"v_fma_f32 v194, v242, v194, -v244\n"
"s_waitcnt lgkmcnt(1)\n"
"s_nop 0\n"
"v_mfma_f32_16x16x32_bf16 v[208:211], v[48:51], v[224:227], v[208:211]\n"
"ds_read_b128 v[232:235], v250 offset:32768\n"
"v_fma_f32 v202, v242, v202, v245\n"
"v_fma_f32 v244, v240, v246, v194\n"
"v_mfma_f32_16x16x32_bf16 v[212:215], v[120:123], v[224:227], v[212:215]\n"
"buffer_load_dwordx4 v255, s[16:19], s36 offen offset:1024 lds\n"
"v_fma_f32 v245, v240, v247, v202\n"
"v_fma_f32 v244, -v241, v247, v244\n"
"s_waitcnt lgkmcnt(1)\n"
"s_nop 0\n"
"v_mfma_f32_16x16x32_bf16 v[216:219], v[48:51], v[228:231], v[216:219]\n"
"ds_read_b128 v[236:239], v250 offset:34816\n"
"v_fma_f32 v247, v241, v246, v245\n"
"v_mov_b32 v246, v244\n"
"v_mul_f32 v244, v243, v203\n"
"v_mfma_f32_16x16x32_bf16 v[220:223], v[120:123], v[228:231], v[220:223]\n"
"v_mul_f32_e64 v245, v243, v195\n"
"v_fma_f32 v195, v242, v195, -v244\n"
"s_waitcnt lgkmcnt(1)\n"
"s_nop 0\n"
"v_mfma_f32_16x16x32_bf16 v[208:211], v[52:55], v[232:235], v[208:211]\n"
"ds_read_b128 v[224:227], v249 offset:36864\n"
"v_fma_f32 v203, v242, v203, v245\n"
"v_fma_f32 v244, v240, v246, v195\n"
"v_fma_f32 v245, v240, v247, v203\n"
"v_mfma_f32_16x16x32_bf16 v[212:215], v[124:127], v[232:235], v[212:215]\n"
"v_fma_f32 v244, -v241, v247, v244\n"
"v_fma_f32 v247, v241, v246, v245\n"
"s_waitcnt lgkmcnt(1)\n"
"s_nop 0\n"
"v_mfma_f32_16x16x32_bf16 v[216:219], v[52:55], v[236:239], v[216:219]\n"
"ds_read_b128 v[228:231], v249 offset:38912\n"
"v_mov_b32 v246, v244\n"
"v_mul_f32 v244, v243, v204\n"
"v_mfma_f32_16x16x32_bf16 v[220:223], v[124:127], v[236:239], v[220:223]\n"
"v_mul_f32_e64 v245, v243, v196\n"
"v_fma_f32 v196, v242, v196, -v244\n"
"v_fma_f32 v204, v242, v204, v245\n"
"s_waitcnt lgkmcnt(1)\n"
"s_nop 0\n"
"v_mfma_f32_16x16x32_bf16 v[208:211], v[56:59], v[224:227], v[208:211]\n"
"ds_read_b128 v[232:235], v250 offset:36864\n"
"v_fma_f32 v244, v240, v246, v196\n"
"v_fma_f32 v245, v240, v247, v204\n"
"v_mfma_f32_16x16x32_bf16 v[212:215], v[128:131], v[224:227], v[212:215]\n"
"v_fma_f32 v244, -v241, v247, v244\n"
"v_fma_f32 v247, v241, v246, v245\n"
"v_mov_b32 v246, v244\n"
"s_waitcnt lgkmcnt(1)\n"
"v_mfma_f32_16x16x32_bf16 v[216:219], v[56:59], v[228:231], v[216:219]\n"
"ds_read_b128 v[236:239], v250 offset:38912\n"
"v_mul_f32 v244, v243, v205\n"
"v_mul_f32 v245, v243, v197\n"
"v_mfma_f32_16x16x32_bf16 v[220:223], v[128:131], v[228:231], v[220:223]\n"
"v_fma_f32 v197, v242, v197, -v244\n"
"v_fma_f32 v205, v242, v205, v245\n"
"s_waitcnt lgkmcnt(1)\n"
"s_nop 0\n"
"v_mfma_f32_16x16x32_bf16 v[208:211], v[60:63], v[232:235], v[208:211]\n"
"ds_read_b128 v[224:227], v249 offset:40960\n"
"v_fma_f32 v244, v240, v246, v197\n"
"v_fma_f32 v245, v240, v247, v205\n"
"v_fma_f32 v244, -v241, v247, v244\n"
"v_mfma_f32_16x16x32_bf16 v[212:215], v[132:135], v[232:235], v[212:215]\n"
"v_fma_f32 v247, v241, v246, v245\n"
"v_mov_b32 v246, v244\n"
"s_waitcnt lgkmcnt(1)\n"
"v_mfma_f32_16x16x32_bf16 v[216:219], v[60:63], v[236:239], v[216:219]\n"
"ds_read_b128 v[228:231], v249 offset:43008\n"
"v_mul_f32 v244, v243, v206\n"
"v_mul_f32 v245, v243, v198\n"
"v_fma_f32 v198, v242, v198, -v244\n"
"v_mfma_f32_16x16x32_bf16 v[220:223], v[132:135], v[236:239], v[220:223]\n"
"v_fma_f32 v206, v242, v206, v245\n"
"v_fma_f32 v244, v240, v246, v198\n"
"s_waitcnt lgkmcnt(1)\n"
"s_nop 0\n"
"v_mfma_f32_16x16x32_bf16 v[208:211], v[64:67], v[224:227], v[208:211]\n"
"ds_read_b128 v[232:235], v250 offset:40960\n"
"v_fma_f32 v245, v240, v247, v206\n"
"v_fma_f32 v244, -v241, v247, v244\n"
"v_mfma_f32_16x16x32_bf16 v[212:215], v[136:139], v[224:227], v[212:215]\n"
"v_fma_f32 v247, v241, v246, v245\n"
"v_mov_b32 v246, v244\n"
"v_mul_f32 v244, v243, v207\n"
"s_waitcnt lgkmcnt(1)\n"
"s_nop 0\n"
"v_mfma_f32_16x16x32_bf16 v[216:219], v[64:67], v[228:231], v[216:219]\n"
"ds_read_b128 v[236:239], v250 offset:43008\n"
"v_mul_f32_e64 v245, v243, v199\n"
"v_fma_f32 v199, v242, v199, -v244\n"
"v_mfma_f32_16x16x32_bf16 v[220:223], v[136:139], v[228:231], v[220:223]\n"
"v_fma_f32 v207, v242, v207, v245\n"
"v_fma_f32 v244, v240, v246, v199\n"
"v_fma_f32 v245, v240, v247, v207\n"
"s_waitcnt vmcnt(2) lgkmcnt(0)\n"
"s_barrier\n"
"v_mfma_f32_16x16x32_bf16 v[208:211], v[68:71], v[232:235], v[208:211]\n"
"v_fma_f32 v244, -v241, v247, v244\n"
"v_fma_f32 v247, v241, v246, v245\n"
"v_mfma_f32_16x16x32_bf16 v[212:215], v[140:143], v[232:235], v[212:215]\n"
"v_mov_b32_e64 v246, v244\n"
"ds_write_b64 v251, v[246:247] offset:384\n"
"v_mfma_f32_16x16x32_bf16 v[216:219], v[68:71], v[236:239], v[216:219]\n"
"v_mfma_f32_16x16x32_bf16 v[220:223], v[140:143], v[236:239], v[220:223]\n"
"s_nop 7\n"
"s_nop 0\n"
"ds_read_b128 v[240:243], v252 offset:50176\n"
"s_waitcnt lgkmcnt(0)\n"
"v_mul_f32 v244, v243, v216\n"
"v_mul_f32_e64 v245, v243, v208\n"
"v_fma_f32 v208, v242, v208, -v244\n"
"v_fma_f32 v216, v242, v216, v245\n"
"v_mul_f32 v244, v243, v217\n"
"v_mul_f32 v245, v243, v209\n"
"v_fma_f32 v209, v242, v209, -v244\n"
"v_fma_f32 v217, v242, v217, v245\n"
"v_fma_f32 v244, v240, v208, v209\n"
"v_fma_f32 v245, v240, v216, v217\n"
"v_fma_f32 v246, -v241, v216, v244\n"
"v_fma_f32 v247, v241, v208, v245\n"
"v_mul_f32 v244, v243, v218\n"
"v_mul_f32 v245, v243, v210\n"
"v_fma_f32 v210, v242, v210, -v244\n"
"v_fma_f32 v218, v242, v218, v245\n"
"v_fma_f32 v244, v240, v246, v210\n"
"v_fma_f32 v245, v240, v247, v218\n"
"v_fma_f32 v244, -v241, v247, v244\n"
"v_fma_f32 v247, v241, v246, v245\n"
"v_mov_b32 v246, v244\n"
"v_mul_f32 v244, v243, v219\n"
"v_mul_f32_e64 v245, v243, v211\n"
"v_fma_f32 v211, v242, v211, -v244\n"
"v_fma_f32 v219, v242, v219, v245\n"
"v_fma_f32 v244, v240, v246, v211\n"
"v_fma_f32 v245, v240, v247, v219\n"
"v_fma_f32 v244, -v241, v247, v244\n"
"v_fma_f32 v247, v241, v246, v245\n"
"v_mov_b32 v246, v244\n"
"v_mul_f32 v244, v243, v220\n"
"v_mul_f32_e64 v245, v243, v212\n"
"v_fma_f32 v212, v242, v212, -v244\n"
"v_fma_f32 v220, v242, v220, v245\n"
"v_fma_f32 v244, v240, v246, v212\n"
"v_fma_f32 v245, v240, v247, v220\n"
"v_fma_f32 v244, -v241, v247, v244\n"
"v_fma_f32 v247, v241, v246, v245\n"
"v_mov_b32 v246, v244\n"
"v_mul_f32 v244, v243, v221\n"
"v_mul_f32_e64 v245, v243, v213\n"
"v_fma_f32 v213, v242, v213, -v244\n"
"v_fma_f32 v221, v242, v221, v245\n"
"v_fma_f32 v244, v240, v246, v213\n"
"v_fma_f32 v245, v240, v247, v221\n"
"v_fma_f32 v244, -v241, v247, v244\n"
"v_fma_f32 v247, v241, v246, v245\n"
"v_mov_b32 v246, v244\n"
"v_mul_f32 v244, v243, v222\n"
"v_mul_f32_e64 v245, v243, v214\n"
"v_fma_f32 v214, v242, v214, -v244\n"
"v_fma_f32 v222, v242, v222, v245\n"
"v_fma_f32 v244, v240, v246, v214\n"
"v_fma_f32 v245, v240, v247, v222\n"
"v_fma_f32 v244, -v241, v247, v244\n"
"v_fma_f32 v247, v241, v246, v245\n"
"v_mov_b32 v246, v244\n"
"v_mul_f32 v244, v243, v223\n"
"v_mul_f32_e64 v245, v243, v215\n"
"v_fma_f32 v215, v242, v215, -v244\n"
"v_fma_f32 v223, v242, v223, v245\n"
"v_fma_f32 v244, v240, v246, v215\n"
"v_fma_f32 v245, v240, v247, v223\n"
"v_fma_f32 v244, -v241, v247, v244\n"
"v_fma_f32 v247, v241, v246, v245\n"
"v_mov_b32_e64 v246, v244\n"
"ds_write_b64 v251, v[246:247] offset:512\n"
"s_branch S1_JOIN\n"
"S1_IDLE:\n"
"s_nop 0\n"
"buffer_load_dwordx4 v255, s[16:19], s36 offen offset:1024 lds\n"
"s_nop 7\n"
"s_nop 0\n"
"ds_read_b128 v[240:243], v252 offset:49664\n"
"s_waitcnt lgkmcnt(0)\n"
"v_mul_f32 v244, v243, v184\n"
"v_mul_f32_e64 v245, v243, v176\n"
"v_fma_f32 v176, v242, v176, -v244\n"
"v_fma_f32 v184, v242, v184, v245\n"
"v_mul_f32 v244, v243, v185\n"
"v_mul_f32 v245, v243, v177\n"
"v_fma_f32 v177, v242, v177, -v244\n"
"v_fma_f32 v185, v242, v185, v245\n"
"v_fma_f32 v244, v240, v176, v177\n"
"v_fma_f32 v245, v240, v184, v185\n"
"v_fma_f32 v246, -v241, v184, v244\n"
"v_fma_f32 v247, v241, v176, v245\n"
"v_mul_f32 v244, v243, v186\n"
"v_mul_f32 v245, v243, v178\n"
"v_fma_f32 v178, v242, v178, -v244\n"
"v_fma_f32 v186, v242, v186, v245\n"
"v_fma_f32 v244, v240, v246, v178\n"
"v_fma_f32 v245, v240, v247, v186\n"
"v_fma_f32 v244, -v241, v247, v244\n"
"v_fma_f32 v247, v241, v246, v245\n"
"v_mov_b32 v246, v244\n"
"v_mul_f32 v244, v243, v187\n"
"v_mul_f32_e64 v245, v243, v179\n"
"v_fma_f32 v179, v242, v179, -v244\n"
"v_fma_f32 v187, v242, v187, v245\n"
"v_fma_f32 v244, v240, v246, v179\n"
"v_fma_f32 v245, v240, v247, v187\n"
"v_fma_f32 v244, -v241, v247, v244\n"
"v_fma_f32 v247, v241, v246, v245\n"
"v_mov_b32 v246, v244\n"
"v_mul_f32 v244, v243, v188\n"
"v_mul_f32_e64 v245, v243, v180\n"
"v_fma_f32 v180, v242, v180, -v244\n"
"v_fma_f32 v188, v242, v188, v245\n"
"v_fma_f32 v244, v240, v246, v180\n"
"v_fma_f32 v245, v240, v247, v188\n"
"v_fma_f32 v244, -v241, v247, v244\n"
"v_fma_f32 v247, v241, v246, v245\n"
"v_mov_b32 v246, v244\n"
"v_mul_f32 v244, v243, v189\n"
"v_mul_f32_e64 v245, v243, v181\n"
"v_fma_f32 v181, v242, v181, -v244\n"
"v_fma_f32 v189, v242, v189, v245\n"
"v_fma_f32 v244, v240, v246, v181\n"
"v_fma_f32 v245, v240, v247, v189\n"
"v_fma_f32 v244, -v241, v247, v244\n"
"v_fma_f32 v247, v241, v246, v245\n"
"v_mov_b32 v246, v244\n"
"v_mul_f32 v244, v243, v190\n"
"v_mul_f32_e64 v245, v243, v182\n"
"v_fma_f32 v182, v242, v182, -v244\n"
"v_fma_f32 v190, v242, v190, v245\n"
"v_fma_f32 v244, v240, v246, v182\n"
"v_fma_f32 v245, v240, v247, v190\n"
"v_fma_f32 v244, -v241, v247, v244\n"
"v_fma_f32 v247, v241, v246, v245\n"
"v_mov_b32 v246, v244\n"
"v_mul_f32 v244, v243, v191\n"
"v_mul_f32_e64 v245, v243, v183\n"
"v_fma_f32 v183, v242, v183, -v244\n"
"v_fma_f32 v191, v242, v191, v245\n"
"v_fma_f32 v244, v240, v246, v183\n"
"v_fma_f32 v245, v240, v247, v191\n"
"v_fma_f32 v244, -v241, v247, v244\n"
"v_fma_f32 v247, v241, v246, v245\n"
"v_mov_b32_e64 v246, v244\n"
"ds_write_b64 v251, v[246:247] offset:256\n"
"ds_read_b128 v[240:243], v252 offset:49920\n"
"s_waitcnt lgkmcnt(0)\n"
"v_mul_f32 v244, v243, v200\n"
"v_mul_f32_e64 v245, v243, v192\n"
"v_fma_f32 v192, v242, v192, -v244\n"
"v_fma_f32 v200, v242, v200, v245\n"
"v_mul_f32 v244, v243, v201\n"
"v_mul_f32 v245, v243, v193\n"
"v_fma_f32 v193, v242, v193, -v244\n"
"v_fma_f32 v201, v242, v201, v245\n"
"v_fma_f32 v244, v240, v192, v193\n"
"v_fma_f32 v245, v240, v200, v201\n"
"v_fma_f32 v246, -v241, v200, v244\n"
"v_fma_f32 v247, v241, v192, v245\n"
"v_mul_f32 v244, v243, v202\n"
"v_mul_f32 v245, v243, v194\n"
"v_fma_f32 v194, v242, v194, -v244\n"
"v_fma_f32 v202, v242, v202, v245\n"
"v_fma_f32 v244, v240, v246, v194\n"
"v_fma_f32 v245, v240, v247, v202\n"
"v_fma_f32 v244, -v241, v247, v244\n"
"v_fma_f32 v247, v241, v246, v245\n"
"v_mov_b32 v246, v244\n"
"v_mul_f32 v244, v243, v203\n"
"v_mul_f32_e64 v245, v243, v195\n"
"v_fma_f32 v195, v242, v195, -v244\n"
"v_fma_f32 v203, v242, v203, v245\n"
"v_fma_f32 v244, v240, v246, v195\n"
"v_fma_f32 v245, v240, v247, v203\n"
"v_fma_f32 v244, -v241, v247, v244\n"
"v_fma_f32 v247, v241, v246, v245\n"
"v_mov_b32 v246, v244\n"
"v_mul_f32 v244, v243, v204\n"
"v_mul_f32_e64 v245, v243, v196\n"
"v_fma_f32 v196, v242, v196, -v244\n"
"v_fma_f32 v204, v242, v204, v245\n"
"v_fma_f32 v244, v240, v246, v196\n"
"v_fma_f32 v245, v240, v247, v204\n"
"v_fma_f32 v244, -v241, v247, v244\n"
"v_fma_f32 v247, v241, v246, v245\n"
"v_mov_b32 v246, v244\n"
"v_mul_f32 v244, v243, v205\n"
"v_mul_f32_e64 v245, v243, v197\n"
"v_fma_f32 v197, v242, v197, -v244\n"
"v_fma_f32 v205, v242, v205, v245\n"
"v_fma_f32 v244, v240, v246, v197\n"
"v_fma_f32 v245, v240, v247, v205\n"
"v_fma_f32 v244, -v241, v247, v244\n"
"v_fma_f32 v247, v241, v246, v245\n"
"v_mov_b32 v246, v244\n"
"v_mul_f32 v244, v243, v206\n"
"v_mul_f32_e64 v245, v243, v198\n"
"v_fma_f32 v198, v242, v198, -v244\n"
"v_fma_f32 v206, v242, v206, v245\n"
"v_fma_f32 v244, v240, v246, v198\n"
"v_fma_f32 v245, v240, v247, v206\n"
"v_fma_f32 v244, -v241, v247, v244\n"
"v_fma_f32 v247, v241, v246, v245\n"
"v_mov_b32 v246, v244\n"
"v_mul_f32 v244, v243, v207\n"
"v_mul_f32_e64 v245, v243, v199\n"
"v_fma_f32 v199, v242, v199, -v244\n"
"v_fma_f32 v207, v242, v207, v245\n"
"v_fma_f32 v244, v240, v246, v199\n"
"v_fma_f32 v245, v240, v247, v207\n"
"v_fma_f32 v244, -v241, v247, v244\n"
"v_fma_f32 v247, v241, v246, v245\n"
"v_mov_b32_e64 v246, v244\n"
"ds_write_b64 v251, v[246:247] offset:384\n"
"s_load_dwordx4 s[60:63], s[8:9], 40\n"
"s_and_b32 s76, s34, 7\n"
"s_lshl_b32 s76, s76, 2\n"
"s_add_u32 s76, s76, s11\n"
"s_sub_u32 s76, s76, 4\n"
"s_lshl_b32 s76, s76, 6\n"
"s_waitcnt lgkmcnt(0)\n"
"s_cmp_ge_u32 s76, 1024\n"
"s_cselect_b64 s[60:61], s[62:63], s[60:61]\n"
"s_nop 0\n"
"s_and_b32 s76, s76, 1023\n"
"v_add_u32 v126, s76, v248\n"
"v_lshrrev_b32 v128, 1, v126\n"
"v_mul_u32_u24 v128, 1152, v128\n"
"v_and_b32_e64 v126, 1, v126\n"
"v_lshl_add_u32 v127, v126, 7, v128\n"
"s_lshl_b32 s76, s82, 8\n"
"s_nop 0\n"
"s_add_u32 s76, s76, 73728\n"
"v_add_u32_e64 v127, s76, v127\n"
"global_load_dword v240, v127, s[60:61]\n"
"s_waitcnt vmcnt(3) lgkmcnt(0)\n"
"s_barrier\n"
"s_mov_b32 s16, s24\n"
"s_mov_b32 s17, s25\n"
"s_mov_b32 s18, s26\n"
"s_mov_b32 s19, s27\n"
"s_mov_b32 s70, 0\n"
"s_lshl_b32 s68, s82, 8\n"
"s_add_u32 s69, s68, 128\n"
"s_and_b32 s76, s11, 3\n"
"s_nop 0\n"
"s_mul_i32 s76, s76, 18432\n"
"v_add_u32 v254, s76, v254\n"
"v_add_u32 v255, s76, v255\n"
"s_add_u32 s36, s70, s68\n"
"s_mov_b32 m0, s43\n"
"s_nop 0\n"
"s_nop 0\n"
"buffer_load_dwordx4 v254, s[16:19], s36 offen lds\n"
"buffer_load_dwordx4 v255, s[16:19], s36 offen offset:1024 lds\n"
"s_waitcnt vmcnt(3)\n"
"s_barrier\n"
"s_add_u32 s36, s70, s69\n"
"s_nop 0\n"
"s_add_u32 m0, s43, 16384\n"
"s_nop 0\n"
"s_nop 0\n"
"buffer_load_dwordx4 v254, s[16:19], s36 offen lds\n"
"buffer_load_dwordx4 v255, s[16:19], s36 offen offset:1024 lds\n"
"s_waitcnt vmcnt(2)\n"
"s_barrier\n"
"S1_JOIN:\n"
"s_waitcnt lgkmcnt(0)\n"
"s_barrier\n"
"s_cmp_ge_u32 s11, 3\n"
"s_cbranch_scc1 SC_IDLE\n"
"s_lshl_b32 s76, s11, 6\n"
"v_add_u32 v100, s76, v248\n"
"v_cmp_gt_u32 vcc, 144, v100\n"
"s_and_saveexec_b64 s[78:79], vcc\n"
"v_lshlrev_b32 v101, 3, v100\n"
"v_add_u32 v101, 139264, v101\n"
"v_lshlrev_b32_e64 v102, 4, v100\n"
"ds_read_b128 v[104:107], v102 offset:51456\n"
"ds_read_b64 v[0:1], v101 offset:0\n"
"ds_read_b64 v[2:3], v101 offset:1152\n"
"ds_read_b64 v[4:5], v101 offset:2304\n"
"ds_read_b64 v[6:7], v101 offset:3456\n"
"ds_read_b64 v[8:9], v101 offset:4608\n"
"ds_read_b64 v[10:11], v101 offset:5760\n"
"ds_read_b64 v[12:13], v101 offset:6912\n"
"ds_read_b64 v[14:15], v101 offset:8064\n"
"ds_read_b64 v[16:17], v101 offset:9216\n"
"ds_read_b64 v[18:19], v101 offset:10368\n"
"ds_read_b64 v[20:21], v101 offset:11520\n"
"ds_read_b64 v[22:23], v101 offset:12672\n"
"ds_read_b64 v[24:25], v101 offset:13824\n"
"ds_read_b64 v[26:27], v101 offset:14976\n"
"ds_read_b64 v[28:29], v101 offset:16128\n"
"ds_read_b64 v[30:31], v101 offset:17280\n"
"s_mul_i32 s76, s82, 36864\n"
"v_add_u32_e64 v102, s76, v102\n"
"s_add_u32 s58, s72, 41943040\n"
"s_addc_u32 s59, s73, 0\n"
"s_nop 0\n"
"s_mul_i32 s76, s34, 2304\n"
"v_add_u32 v114, s76, v102\n"
"s_waitcnt lgkmcnt(14)\n"
"v_fma_f32 v112, v104, v0, v2\n"
"v_fma_f32 v113, v104, v1, v3\n"
"v_fma_f32 v108, -v105, v1, v112\n"
"v_fma_f32 v109, v105, v0, v113\n"
"s_waitcnt lgkmcnt(13)\n"
"s_nop 0\n"
"v_fma_f32 v112, v104, v108, v4\n"
"v_fma_f32 v113, v104, v109, v5\n"
"v_fma_f32 v110, -v105, v109, v112\n"
"v_fma_f32 v111, v105, v108, v113\n"
"s_waitcnt lgkmcnt(12)\n"
"s_nop 0\n"
"v_fma_f32 v112, v104, v110, v6\n"
"v_fma_f32 v113, v104, v111, v7\n"
"v_fma_f32 v108, -v105, v111, v112\n"
"v_fma_f32 v109, v105, v110, v113\n"
"s_waitcnt lgkmcnt(11)\n"
"s_nop 0\n"
"v_fma_f32 v112, v104, v108, v8\n"
"v_fma_f32 v113, v104, v109, v9\n"
"v_fma_f32 v110, -v105, v109, v112\n"
"v_fma_f32 v111, v105, v108, v113\n"
"s_waitcnt lgkmcnt(10)\n"
"s_nop 0\n"
"v_fma_f32 v112, v104, v110, v10\n"
"v_fma_f32 v113, v104, v111, v11\n"
"v_fma_f32 v108, -v105, v111, v112\n"
"v_fma_f32 v109, v105, v110, v113\n"
"s_waitcnt lgkmcnt(9)\n"
"s_nop 0\n"
"v_fma_f32 v112, v104, v108, v12\n"
"v_fma_f32 v113, v104, v109, v13\n"
"v_fma_f32 v110, -v105, v109, v112\n"
"v_fma_f32 v111, v105, v108, v113\n"
"s_waitcnt lgkmcnt(8)\n"
"s_nop 0\n"
"v_fma_f32 v112, v104, v110, v14\n"
"v_fma_f32 v113, v104, v111, v15\n"
"v_fma_f32 v108, -v105, v111, v112\n"
"v_fma_f32 v109, v105, v110, v113\n"
"s_waitcnt lgkmcnt(7)\n"
"s_nop 0\n"
"v_fma_f32 v112, v104, v108, v16\n"
"v_fma_f32 v113, v104, v109, v17\n"
"v_fma_f32 v110, -v105, v109, v112\n"
"v_fma_f32 v111, v105, v108, v113\n"
"s_waitcnt lgkmcnt(6)\n"
"s_nop 0\n"
"v_fma_f32 v112, v104, v110, v18\n"
"v_fma_f32 v113, v104, v111, v19\n"
"v_fma_f32 v108, -v105, v111, v112\n"
"v_fma_f32 v109, v105, v110, v113\n"
"s_waitcnt lgkmcnt(5)\n"
"s_nop 0\n"
"v_fma_f32 v112, v104, v108, v20\n"
"v_fma_f32 v113, v104, v109, v21\n"
"v_fma_f32 v110, -v105, v109, v112\n"
"v_fma_f32 v111, v105, v108, v113\n"
"s_waitcnt lgkmcnt(4)\n"
"s_nop 0\n"
"v_fma_f32 v112, v104, v110, v22\n"
"v_fma_f32 v113, v104, v111, v23\n"
"v_fma_f32 v108, -v105, v111, v112\n"
"v_fma_f32 v109, v105, v110, v113\n"
"s_waitcnt lgkmcnt(3)\n"
"s_nop 0\n"
"v_fma_f32 v112, v104, v108, v24\n"
"v_fma_f32 v113, v104, v109, v25\n"
"v_fma_f32 v110, -v105, v109, v112\n"
"v_fma_f32 v111, v105, v108, v113\n"
"s_waitcnt lgkmcnt(2)\n"
"s_nop 0\n"
"v_fma_f32 v112, v104, v110, v26\n"
"v_fma_f32 v113, v104, v111, v27\n"
"v_fma_f32 v108, -v105, v111, v112\n"
"v_fma_f32 v109, v105, v110, v113\n"
"s_waitcnt lgkmcnt(1)\n"
"s_nop 0\n"
"v_fma_f32 v112, v104, v108, v28\n"
"v_fma_f32 v113, v104, v109, v29\n"
"v_fma_f32 v110, -v105, v109, v112\n"
"v_fma_f32 v111, v105, v108, v113\n"
"s_waitcnt lgkmcnt(0)\n"
"s_nop 0\n"
"v_fma_f32 v112, v104, v110, v30\n"
"v_fma_f32 v113, v104, v111, v31\n"
"v_fma_f32 v108, -v105, v111, v112\n"
"v_fma_f32 v109, v105, v110, v113\n"
"v_mov_b32 v116, v108\n"
"v_mov_b32 v117, s4\n"
"v_mov_b32 v118, v109\n"
"v_mov_b32 v119, s5\n"
"global_store_dwordx4 v114, v[116:119], s[58:59] sc1\n"
"v_mov_b32 v120, 0\n"
"v_mov_b32 v121, 0\n"
"s_cmp_eq_u32 s34, 0\n"
"s_cbranch_scc1 SC_CARRY\n"
"s_mov_b32 s71, 0\n"
"SC_POLL:\n"
"v_mov_b32 v112, v102\n"
"global_load_dwordx4 v[32:35], v112, s[58:59] sc1\n"
"s_cmp_le_u32 s34, 1\n"
"s_cbranch_scc1 SC_LD\n"
"v_add_u32 v112, 2304, v112\n"
"global_load_dwordx4 v[36:39], v112, s[58:59] sc1\n"
"s_cmp_le_u32 s34, 2\n"
"s_cbranch_scc1 SC_LD\n"
"v_add_u32 v112, 2304, v112\n"
"global_load_dwordx4 v[40:43], v112, s[58:59] sc1\n"
"s_cmp_le_u32 s34, 3\n"
"s_cbranch_scc1 SC_LD\n"
"v_add_u32 v112, 2304, v112\n"
"global_load_dwordx4 v[44:47], v112, s[58:59] sc1\n"
"s_cmp_le_u32 s34, 4\n"
"s_cbranch_scc1 SC_LD\n"
"v_add_u32 v112, 2304, v112\n"
"global_load_dwordx4 v[48:51], v112, s[58:59] sc1\n"
"s_cmp_le_u32 s34, 5\n"
"s_cbranch_scc1 SC_LD\n"
"v_add_u32 v112, 2304, v112\n"
"global_load_dwordx4 v[52:55], v112, s[58:59] sc1\n"
"s_cmp_le_u32 s34, 6\n"
"s_cbranch_scc1 SC_LD\n"
"v_add_u32 v112, 2304, v112\n"
"global_load_dwordx4 v[56:59], v112, s[58:59] sc1\n"
"s_cmp_le_u32 s34, 7\n"
"s_cbranch_scc1 SC_LD\n"
"v_add_u32 v112, 2304, v112\n"
"global_load_dwordx4 v[60:63], v112, s[58:59] sc1\n"
"s_cmp_le_u32 s34, 8\n"
"s_cbranch_scc1 SC_LD\n"
"v_add_u32 v112, 2304, v112\n"
"global_load_dwordx4 v[64:67], v112, s[58:59] sc1\n"
"s_cmp_le_u32 s34, 9\n"
"s_cbranch_scc1 SC_LD\n"
"v_add_u32 v112, 2304, v112\n"
"global_load_dwordx4 v[68:71], v112, s[58:59] sc1\n"
"s_cmp_le_u32 s34, 10\n"
"s_cbranch_scc1 SC_LD\n"
"v_add_u32 v112, 2304, v112\n"
"global_load_dwordx4 v[72:75], v112, s[58:59] sc1\n"
"s_cmp_le_u32 s34, 11\n"
"s_cbranch_scc1 SC_LD\n"
"v_add_u32 v112, 2304, v112\n"
"global_load_dwordx4 v[76:79], v112, s[58:59] sc1\n"
"s_cmp_le_u32 s34, 12\n"
"s_cbranch_scc1 SC_LD\n"
"v_add_u32 v112, 2304, v112\n"
"global_load_dwordx4 v[80:83], v112, s[58:59] sc1\n"
"s_cmp_le_u32 s34, 13\n"
"s_cbranch_scc1 SC_LD\n"
"v_add_u32 v112, 2304, v112\n"
"global_load_dwordx4 v[84:87], v112, s[58:59] sc1\n"
"s_cmp_le_u32 s34, 14\n"
"s_cbranch_scc1 SC_LD\n"
"v_add_u32 v112, 2304, v112\n"
"global_load_dwordx4 v[88:91], v112, s[58:59] sc1\n"
"SC_LD:\n"
"s_waitcnt vmcnt(0)\n"
"s_mov_b64 s[76:77], 0\n"
"v_cmp_ne_u32 vcc, s4, v33\n"
"s_nop 0\n"
"v_cmp_ne_u32 s[62:63], s5, v35\n"
"s_nop 3\n"
"s_or_b64 s[76:77], s[76:77], vcc\n"
"s_or_b64 s[76:77], s[76:77], s[62:63]\n"
"s_cmp_le_u32 s34, 1\n"
"s_cbranch_scc1 SC_CK\n"
"v_cmp_ne_u32 vcc, s4, v37\n"
"v_cmp_ne_u32 s[62:63], s5, v39\n"
"s_nop 3\n"
"s_or_b64 s[76:77], s[76:77], vcc\n"
"s_or_b64 s[76:77], s[76:77], s[62:63]\n"
"s_cmp_le_u32 s34, 2\n"
"s_cbranch_scc1 SC_CK\n"
"v_cmp_ne_u32 vcc, s4, v41\n"
"v_cmp_ne_u32 s[62:63], s5, v43\n"
"s_nop 3\n"
"s_or_b64 s[76:77], s[76:77], vcc\n"
"s_or_b64 s[76:77], s[76:77], s[62:63]\n"
"s_cmp_le_u32 s34, 3\n"
"s_cbranch_scc1 SC_CK\n"
"v_cmp_ne_u32 vcc, s4, v45\n"
"v_cmp_ne_u32 s[62:63], s5, v47\n"
"s_nop 3\n"
"s_or_b64 s[76:77], s[76:77], vcc\n"
"s_or_b64 s[76:77], s[76:77], s[62:63]\n"
"s_cmp_le_u32 s34, 4\n"
"s_cbranch_scc1 SC_CK\n"
"v_cmp_ne_u32 vcc, s4, v49\n"
"v_cmp_ne_u32 s[62:63], s5, v51\n"
"s_nop 3\n"
"s_or_b64 s[76:77], s[76:77], vcc\n"
"s_or_b64 s[76:77], s[76:77], s[62:63]\n"
"s_cmp_le_u32 s34, 5\n"
"s_cbranch_scc1 SC_CK\n"
"v_cmp_ne_u32 vcc, s4, v53\n"
"v_cmp_ne_u32 s[62:63], s5, v55\n"
"s_nop 3\n"
"s_or_b64 s[76:77], s[76:77], vcc\n"
"s_or_b64 s[76:77], s[76:77], s[62:63]\n"
"s_cmp_le_u32 s34, 6\n"
"s_cbranch_scc1 SC_CK\n"
"v_cmp_ne_u32 vcc, s4, v57\n"
"v_cmp_ne_u32 s[62:63], s5, v59\n"
"s_nop 3\n"
"s_or_b64 s[76:77], s[76:77], vcc\n"
"s_or_b64 s[76:77], s[76:77], s[62:63]\n"
"s_cmp_le_u32 s34, 7\n"
"s_cbranch_scc1 SC_CK\n"
"v_cmp_ne_u32 vcc, s4, v61\n"
"v_cmp_ne_u32 s[62:63], s5, v63\n"
"s_nop 3\n"
"s_or_b64 s[76:77], s[76:77], vcc\n"
"s_or_b64 s[76:77], s[76:77], s[62:63]\n"
"s_cmp_le_u32 s34, 8\n"
"s_cbranch_scc1 SC_CK\n"
"v_cmp_ne_u32 vcc, s4, v65\n"
"v_cmp_ne_u32 s[62:63], s5, v67\n"
"s_nop 3\n"
"s_or_b64 s[76:77], s[76:77], vcc\n"
"s_or_b64 s[76:77], s[76:77], s[62:63]\n"
"s_cmp_le_u32 s34, 9\n"
"s_cbranch_scc1 SC_CK\n"
"v_cmp_ne_u32 vcc, s4, v69\n"
"v_cmp_ne_u32 s[62:63], s5, v71\n"
"s_nop 3\n"
"s_or_b64 s[76:77], s[76:77], vcc\n"
"s_or_b64 s[76:77], s[76:77], s[62:63]\n"
"s_cmp_le_u32 s34, 10\n"
"s_cbranch_scc1 SC_CK\n"
"v_cmp_ne_u32 vcc, s4, v73\n"
"v_cmp_ne_u32 s[62:63], s5, v75\n"
"s_nop 3\n"
"s_or_b64 s[76:77], s[76:77], vcc\n"
"s_or_b64 s[76:77], s[76:77], s[62:63]\n"
"s_cmp_le_u32 s34, 11\n"
"s_cbranch_scc1 SC_CK\n"
"v_cmp_ne_u32 vcc, s4, v77\n"
"v_cmp_ne_u32 s[62:63], s5, v79\n"
"s_nop 3\n"
"s_or_b64 s[76:77], s[76:77], vcc\n"
"s_or_b64 s[76:77], s[76:77], s[62:63]\n"
"s_cmp_le_u32 s34, 12\n"
"s_cbranch_scc1 SC_CK\n"
"v_cmp_ne_u32 vcc, s4, v81\n"
"v_cmp_ne_u32 s[62:63], s5, v83\n"
"s_nop 3\n"
"s_or_b64 s[76:77], s[76:77], vcc\n"
"s_or_b64 s[76:77], s[76:77], s[62:63]\n"
"s_cmp_le_u32 s34, 13\n"
"s_cbranch_scc1 SC_CK\n"
"v_cmp_ne_u32 vcc, s4, v85\n"
"v_cmp_ne_u32 s[62:63], s5, v87\n"
"s_nop 3\n"
"s_or_b64 s[76:77], s[76:77], vcc\n"
"s_or_b64 s[76:77], s[76:77], s[62:63]\n"
"s_cmp_le_u32 s34, 14\n"
"s_cbranch_scc1 SC_CK\n"
"v_cmp_ne_u32 vcc, s4, v89\n"
"v_cmp_ne_u32 s[62:63], s5, v91\n"
"s_nop 3\n"
"s_or_b64 s[76:77], s[76:77], vcc\n"
"s_or_b64 s[76:77], s[76:77], s[62:63]\n"
"SC_CK:\n"
"s_cmp_eq_u64 s[76:77], 0\n"
"s_cbranch_scc1 SC_HORN\n"
"s_add_u32 s71, s71, 1\n"
"s_cmp_gt_u32 s71, 200000\n"
"s_cbranch_scc1 SC_HORN\n"
"s_sleep 1\n"
"s_branch SC_POLL\n"
"SC_HORN:\n"
"s_nop 0\n"
"v_fma_f32 v112, v106, v120, v32\n"
"v_fma_f32 v113, v106, v121, v34\n"
"v_fma_f32 v112, -v107, v121, v112\n"
"v_fma_f32 v121, v107, v120, v113\n"
"v_mov_b32 v120, v112\n"
"s_cmp_le_u32 s34, 1\n"
"s_cbranch_scc1 SC_CARRY\n"
"s_nop 0\n"
"v_fma_f32 v112, v106, v120, v36\n"
"v_fma_f32 v113, v106, v121, v38\n"
"v_fma_f32 v112, -v107, v121, v112\n"
"v_fma_f32 v121, v107, v120, v113\n"
"v_mov_b32 v120, v112\n"
"s_cmp_le_u32 s34, 2\n"
"s_cbranch_scc1 SC_CARRY\n"
"s_nop 0\n"
"v_fma_f32 v112, v106, v120, v40\n"
"v_fma_f32 v113, v106, v121, v42\n"
"v_fma_f32 v112, -v107, v121, v112\n"
"v_fma_f32 v121, v107, v120, v113\n"
"v_mov_b32 v120, v112\n"
"s_cmp_le_u32 s34, 3\n"
"s_cbranch_scc1 SC_CARRY\n"
"s_nop 0\n"
"v_fma_f32 v112, v106, v120, v44\n"
"v_fma_f32 v113, v106, v121, v46\n"
"v_fma_f32 v112, -v107, v121, v112\n"
"v_fma_f32 v121, v107, v120, v113\n"
"v_mov_b32 v120, v112\n"
"s_cmp_le_u32 s34, 4\n"
"s_cbranch_scc1 SC_CARRY\n"
"s_nop 0\n"
"v_fma_f32 v112, v106, v120, v48\n"
"v_fma_f32 v113, v106, v121, v50\n"
"v_fma_f32 v112, -v107, v121, v112\n"
"v_fma_f32 v121, v107, v120, v113\n"
"v_mov_b32 v120, v112\n"
"s_cmp_le_u32 s34, 5\n"
"s_cbranch_scc1 SC_CARRY\n"
"s_nop 0\n"
"v_fma_f32 v112, v106, v120, v52\n"
"v_fma_f32 v113, v106, v121, v54\n"
"v_fma_f32 v112, -v107, v121, v112\n"
"v_fma_f32 v121, v107, v120, v113\n"
"v_mov_b32 v120, v112\n"
"s_cmp_le_u32 s34, 6\n"
"s_cbranch_scc1 SC_CARRY\n"
"s_nop 0\n"
"v_fma_f32 v112, v106, v120, v56\n"
"v_fma_f32 v113, v106, v121, v58\n"
"v_fma_f32 v112, -v107, v121, v112\n"
"v_fma_f32 v121, v107, v120, v113\n"
"v_mov_b32 v120, v112\n"
"s_cmp_le_u32 s34, 7\n"
"s_cbranch_scc1 SC_CARRY\n"
"s_nop 0\n"
"v_fma_f32 v112, v106, v120, v60\n"
"v_fma_f32 v113, v106, v121, v62\n"
"v_fma_f32 v112, -v107, v121, v112\n"
"v_fma_f32 v121, v107, v120, v113\n"
"v_mov_b32 v120, v112\n"
"s_cmp_le_u32 s34, 8\n"
"s_cbranch_scc1 SC_CARRY\n"
"s_nop 0\n"
"v_fma_f32 v112, v106, v120, v64\n"
"v_fma_f32 v113, v106, v121, v66\n"
"v_fma_f32 v112, -v107, v121, v112\n"
"v_fma_f32 v121, v107, v120, v113\n"
"v_mov_b32 v120, v112\n"
"s_cmp_le_u32 s34, 9\n"
"s_cbranch_scc1 SC_CARRY\n"
"s_nop 0\n"
"v_fma_f32 v112, v106, v120, v68\n"
"v_fma_f32 v113, v106, v121, v70\n"
"v_fma_f32 v112, -v107, v121, v112\n"
"v_fma_f32 v121, v107, v120, v113\n"
"v_mov_b32 v120, v112\n"
"s_cmp_le_u32 s34, 10\n"
"s_cbranch_scc1 SC_CARRY\n"
"s_nop 0\n"
"v_fma_f32 v112, v106, v120, v72\n"
"v_fma_f32 v113, v106, v121, v74\n"
"v_fma_f32 v112, -v107, v121, v112\n"
"v_fma_f32 v121, v107, v120, v113\n"
"v_mov_b32 v120, v112\n"
"s_cmp_le_u32 s34, 11\n"
"s_cbranch_scc1 SC_CARRY\n"
"s_nop 0\n"
"v_fma_f32 v112, v106, v120, v76\n"
"v_fma_f32 v113, v106, v121, v78\n"
"v_fma_f32 v112, -v107, v121, v112\n"
"v_fma_f32 v121, v107, v120, v113\n"
"v_mov_b32 v120, v112\n"
"s_cmp_le_u32 s34, 12\n"
"s_cbranch_scc1 SC_CARRY\n"
"s_nop 0\n"
"v_fma_f32 v112, v106, v120, v80\n"
"v_fma_f32 v113, v106, v121, v82\n"
"v_fma_f32 v112, -v107, v121, v112\n"
"v_fma_f32 v121, v107, v120, v113\n"
"v_mov_b32 v120, v112\n"
"s_cmp_le_u32 s34, 13\n"
"s_cbranch_scc1 SC_CARRY\n"
"s_nop 0\n"
"v_fma_f32 v112, v106, v120, v84\n"
"v_fma_f32 v113, v106, v121, v86\n"
"v_fma_f32 v112, -v107, v121, v112\n"
"v_fma_f32 v121, v107, v120, v113\n"
"v_mov_b32 v120, v112\n"
"s_cmp_le_u32 s34, 14\n"
"s_cbranch_scc1 SC_CARRY\n"
"s_nop 0\n"
"v_fma_f32 v112, v106, v120, v88\n"
"v_fma_f32 v113, v106, v121, v90\n"
"v_fma_f32 v112, -v107, v121, v112\n"
"v_fma_f32 v121, v107, v120, v113\n"
"v_mov_b32_e64 v120, v112\n"
"SC_CARRY:\n"
"ds_write_b64 v101, v[120:121] offset:0\n"
"v_fma_f32 v112, v104, v120, v0\n"
"v_fma_f32 v113, v104, v121, v1\n"
"v_fma_f32 v124, -v105, v121, v112\n"
"v_fma_f32 v125, v105, v120, v113\n"
"ds_write_b64 v101, v[124:125] offset:1152\n"
"v_fma_f32 v112, v104, v124, v2\n"
"v_fma_f32 v113, v104, v125, v3\n"
"v_fma_f32 v122, -v105, v125, v112\n"
"v_fma_f32 v123, v105, v124, v113\n"
"ds_write_b64 v101, v[122:123] offset:2304\n"
"v_fma_f32 v112, v104, v122, v4\n"
"v_fma_f32 v113, v104, v123, v5\n"
"v_fma_f32 v124, -v105, v123, v112\n"
"v_fma_f32 v125, v105, v122, v113\n"
"ds_write_b64 v101, v[124:125] offset:3456\n"
"v_fma_f32 v112, v104, v124, v6\n"
"v_fma_f32 v113, v104, v125, v7\n"
"v_fma_f32 v122, -v105, v125, v112\n"
"v_fma_f32 v123, v105, v124, v113\n"
"ds_write_b64 v101, v[122:123] offset:4608\n"
"v_fma_f32 v112, v104, v122, v8\n"
"v_fma_f32 v113, v104, v123, v9\n"
"v_fma_f32 v124, -v105, v123, v112\n"
"v_fma_f32 v125, v105, v122, v113\n"
"ds_write_b64 v101, v[124:125] offset:5760\n"
"v_fma_f32 v112, v104, v124, v10\n"
"v_fma_f32 v113, v104, v125, v11\n"
"v_fma_f32 v122, -v105, v125, v112\n"
"v_fma_f32 v123, v105, v124, v113\n"
"ds_write_b64 v101, v[122:123] offset:6912\n"
"v_fma_f32 v112, v104, v122, v12\n"
"v_fma_f32 v113, v104, v123, v13\n"
"v_fma_f32 v124, -v105, v123, v112\n"
"v_fma_f32 v125, v105, v122, v113\n"
"ds_write_b64 v101, v[124:125] offset:8064\n"
"v_fma_f32 v112, v104, v124, v14\n"
"v_fma_f32 v113, v104, v125, v15\n"
"v_fma_f32 v122, -v105, v125, v112\n"
"v_fma_f32 v123, v105, v124, v113\n"
"ds_write_b64 v101, v[122:123] offset:9216\n"
"v_fma_f32 v112, v104, v122, v16\n"
"v_fma_f32 v113, v104, v123, v17\n"
"v_fma_f32 v124, -v105, v123, v112\n"
"v_fma_f32 v125, v105, v122, v113\n"
"ds_write_b64 v101, v[124:125] offset:10368\n"
"v_fma_f32 v112, v104, v124, v18\n"
"v_fma_f32 v113, v104, v125, v19\n"
"v_fma_f32 v122, -v105, v125, v112\n"
"v_fma_f32 v123, v105, v124, v113\n"
"ds_write_b64 v101, v[122:123] offset:11520\n"
"v_fma_f32 v112, v104, v122, v20\n"
"v_fma_f32 v113, v104, v123, v21\n"
"v_fma_f32 v124, -v105, v123, v112\n"
"v_fma_f32 v125, v105, v122, v113\n"
"ds_write_b64 v101, v[124:125] offset:12672\n"
"v_fma_f32 v112, v104, v124, v22\n"
"v_fma_f32 v113, v104, v125, v23\n"
"v_fma_f32 v122, -v105, v125, v112\n"
"v_fma_f32 v123, v105, v124, v113\n"
"ds_write_b64 v101, v[122:123] offset:13824\n"
"v_fma_f32 v112, v104, v122, v24\n"
"v_fma_f32 v113, v104, v123, v25\n"
"v_fma_f32 v124, -v105, v123, v112\n"
"v_fma_f32 v125, v105, v122, v113\n"
"ds_write_b64 v101, v[124:125] offset:14976\n"
"v_fma_f32 v112, v104, v124, v26\n"
"v_fma_f32 v113, v104, v125, v27\n"
"v_fma_f32 v122, -v105, v125, v112\n"
"v_fma_f32 v123, v105, v124, v113\n"
"ds_write_b64 v101, v[122:123] offset:16128\n"
"v_fma_f32 v112, v104, v122, v28\n"
"v_fma_f32 v113, v104, v123, v29\n"
"v_fma_f32 v124, -v105, v123, v112\n"
"v_fma_f32 v125, v105, v122, v113\n"
"ds_write_b64 v101, v[124:125] offset:17280\n"
"s_mov_b64 exec, s[78:79]\n"
"s_waitcnt vmcnt(0)\n"
"s_branch SC_DONE\n"
"SC_IDLE:\n"
"SC_DONE:\n"
"s_waitcnt lgkmcnt(0)\n"
"s_barrier\n"
"s_nop 0\n"
"s_load_dwordx4 s[60:63], s[8:9], 40\n"
"v_lshrrev_b32 v226, 4, v248\n"
"v_and_b32 v227, 15, v248\n"
"s_and_b32 s76, s11, 3\n"
"s_lshl_b32 s76, s76, 4\n"
"v_add_u32_e64 v227, s76, v227\n"
"v_mul_u32_u24 v228, 1152, v227\n"
"v_and_b32_e64 v227, 1, v226\n"
"v_lshl_add_u32 v228, v227, 4, v228\n"
"s_lshl_b32 s76, s82, 5\n"
"s_nop 0\n"
"s_add_u32 s76, s76, 1024\n"
"v_add_u32 v228, s76, v228\n"
"v_cmp_lt_u32 vcc, 1, v226\n"
"s_waitcnt lgkmcnt(0)\n"
"v_mov_b32 v224, s60\n"
"v_mov_b32 v225, s61\n"
"v_mov_b32 v226, s62\n"
"v_mov_b32 v227, s63\n"
"v_cndmask_b32 v224, v224, v226, vcc\n"
"v_cndmask_b32 v225, v225, v227, vcc\n"
"v_add_co_u32 v224, vcc, v224, v228\n"
"s_nop 1\n"
"v_addc_co_u32 v225, vcc, 0, v225, vcc\n"
"s_lshr_b32 s77, s11, 2\n"
"s_nop 0\n"
"s_mul_i32 s78, s77, 73728\n"
"v_add_co_u32 v224, vcc, s78, v224\n"
"s_nop 1\n"
"v_addc_co_u32 v225, vcc, 0, v225, vcc\n"
"s_lshl_b32 s76, s11, 10\n"
"s_add_u32 m0, s76, 32768\n"
"s_nop 0\n"
"s_nop 0\n"
"global_load_lds_dwordx4 v[224:225], off\n"
"ds_read_b64 v[0:1], v252 offset:49152\n"
"ds_read_b64 v[2:3], v251 offset:0\n"
"ds_read_b64 v[4:5], v252 offset:49408\n"
"ds_read_b64 v[6:7], v251 offset:128\n"
"ds_read_b64 v[8:9], v252 offset:49664\n"
"ds_read_b64 v[10:11], v251 offset:256\n"
"ds_read_b64 v[12:13], v252 offset:49920\n"
"ds_read_b64 v[14:15], v251 offset:384\n"
"s_cmp_ge_u32 s11, 4\n"
"s_cbranch_scc1 SC2_RD\n"
"ds_read_b64 v[16:17], v252 offset:50176\n"
"ds_read_b64 v[18:19], v251 offset:512\n"
"SC2_RD:\n"
"s_waitcnt lgkmcnt(0)\n"
"s_nop 0\n"
"v_fma_f32 v144, v0, v2, v144\n"
"v_fma_f32 v152, v0, v3, v152\n"
"v_fma_f32 v160, v4, v6, v160\n"
"v_fma_f32 v168, v4, v7, v168\n"
"v_fma_f32 v144, -v1, v3, v144\n"
"v_fma_f32 v152, v1, v2, v152\n"
"v_fma_f32 v160, -v5, v7, v160\n"
"v_fma_f32 v168, v5, v6, v168\n"
"v_fma_f32 v145, v0, v144, v145\n"
"v_fma_f32 v153, v0, v152, v153\n"
"v_fma_f32 v161, v4, v160, v161\n"
"v_fma_f32 v169, v4, v168, v169\n"
"v_fma_f32 v145, -v1, v152, v145\n"
"v_fma_f32 v153, v1, v144, v153\n"
"v_fma_f32 v161, -v5, v168, v161\n"
"v_fma_f32 v169, v5, v160, v169\n"
"v_fma_f32 v146, v0, v145, v146\n"
"v_fma_f32 v154, v0, v153, v154\n"
"v_fma_f32 v162, v4, v161, v162\n"
"v_fma_f32 v170, v4, v169, v170\n"
"v_fma_f32 v146, -v1, v153, v146\n"
"v_fma_f32 v154, v1, v145, v154\n"
"v_fma_f32 v162, -v5, v169, v162\n"
"v_fma_f32 v170, v5, v161, v170\n"
"v_fma_f32 v147, v0, v146, v147\n"
"v_fma_f32 v155, v0, v154, v155\n"
"v_fma_f32 v163, v4, v162, v163\n"
"v_fma_f32 v171, v4, v170, v171\n"
"v_fma_f32 v147, -v1, v154, v147\n"
"v_fma_f32 v155, v1, v146, v155\n"
"v_fma_f32 v163, -v5, v170, v163\n"
"v_fma_f32 v171, v5, v162, v171\n"
"v_fma_f32 v148, v0, v147, v148\n"
"v_fma_f32 v156, v0, v155, v156\n"
"v_fma_f32 v164, v4, v163, v164\n"
"v_fma_f32 v172, v4, v171, v172\n"
"v_fma_f32 v148, -v1, v155, v148\n"
"v_fma_f32 v156, v1, v147, v156\n"
"v_fma_f32 v164, -v5, v171, v164\n"
"v_fma_f32 v172, v5, v163, v172\n"
"v_fma_f32 v149, v0, v148, v149\n"
"v_fma_f32 v157, v0, v156, v157\n"
"v_fma_f32 v165, v4, v164, v165\n"
"v_fma_f32 v173, v4, v172, v173\n"
"v_fma_f32 v149, -v1, v156, v149\n"
"v_fma_f32 v157, v1, v148, v157\n"
"v_fma_f32 v165, -v5, v172, v165\n"
"v_fma_f32 v173, v5, v164, v173\n"
"v_fma_f32 v150, v0, v149, v150\n"
"v_fma_f32 v158, v0, v157, v158\n"
"v_fma_f32 v166, v4, v165, v166\n"
"v_fma_f32 v174, v4, v173, v174\n"
"v_fma_f32 v150, -v1, v157, v150\n"
"v_fma_f32 v158, v1, v149, v158\n"
"v_fma_f32 v166, -v5, v173, v166\n"
"v_fma_f32 v174, v5, v165, v174\n"
"v_fma_f32 v151, v0, v150, v151\n"
"v_fma_f32 v159, v0, v158, v159\n"
"v_fma_f32 v167, v4, v166, v167\n"
"v_fma_f32 v175, v4, v174, v175\n"
"v_fma_f32 v151, -v1, v158, v151\n"
"v_fma_f32 v159, v1, v150, v159\n"
"v_fma_f32 v167, -v5, v174, v167\n"
"v_fma_f32 v175, v5, v166, v175\n"
"v_cvt_pk_bf16_f32 v24, v144, v145\n"
"v_cvt_pk_bf16_f32 v28, -v152, -v153\n"
"v_cvt_pk_bf16_f32 v25, v146, v147\n"
"v_cvt_pk_bf16_f32 v29, -v154, -v155\n"
"v_cvt_pk_bf16_f32 v26, v148, v149\n"
"v_cvt_pk_bf16_f32 v30, -v156, -v157\n"
"v_cvt_pk_bf16_f32 v27, v150, v151\n"
"v_cvt_pk_bf16_f32 v31, -v158, -v159\n"
"ds_write_b128 v253, v[24:27] offset:0\n"
"ds_write_b128 v253, v[28:31] offset:4096\n"
"v_cvt_pk_bf16_f32 v32, v160, v161\n"
"v_cvt_pk_bf16_f32 v36, -v168, -v169\n"
"v_cvt_pk_bf16_f32 v33, v162, v163\n"
"v_cvt_pk_bf16_f32 v37, -v170, -v171\n"
"v_cvt_pk_bf16_f32 v34, v164, v165\n"
"v_cvt_pk_bf16_f32 v38, -v172, -v173\n"
"v_cvt_pk_bf16_f32 v35, v166, v167\n"
"v_cvt_pk_bf16_f32 v39, -v174, -v175\n"
"ds_write_b128 v253, v[32:35] offset:8192\n"
"ds_write_b128 v253, v[36:39] offset:12288\n"
"v_fma_f32 v176, v8, v10, v176\n"
"v_fma_f32 v184, v8, v11, v184\n"
"v_fma_f32 v192, v12, v14, v192\n"
"v_fma_f32 v200, v12, v15, v200\n"
"v_fma_f32 v176, -v9, v11, v176\n"
"v_fma_f32 v184, v9, v10, v184\n"
"v_fma_f32 v192, -v13, v15, v192\n"
"v_fma_f32 v200, v13, v14, v200\n"
"v_fma_f32 v177, v8, v176, v177\n"
"v_fma_f32 v185, v8, v184, v185\n"
"v_fma_f32 v193, v12, v192, v193\n"
"v_fma_f32 v201, v12, v200, v201\n"
"v_fma_f32 v177, -v9, v184, v177\n"
"v_fma_f32 v185, v9, v176, v185\n"
"v_fma_f32 v193, -v13, v200, v193\n"
"v_fma_f32 v201, v13, v192, v201\n"
"v_fma_f32 v178, v8, v177, v178\n"
"v_fma_f32 v186, v8, v185, v186\n"
"v_fma_f32 v194, v12, v193, v194\n"
"v_fma_f32 v202, v12, v201, v202\n"
"v_fma_f32 v178, -v9, v185, v178\n"
"v_fma_f32 v186, v9, v177, v186\n"
"v_fma_f32 v194, -v13, v201, v194\n"
"v_fma_f32 v202, v13, v193, v202\n"
"v_fma_f32 v179, v8, v178, v179\n"
"v_fma_f32 v187, v8, v186, v187\n"
"v_fma_f32 v195, v12, v194, v195\n"
"v_fma_f32 v203, v12, v202, v203\n"
"v_fma_f32 v179, -v9, v186, v179\n"
"v_fma_f32 v187, v9, v178, v187\n"
"v_fma_f32 v195, -v13, v202, v195\n"
"v_fma_f32 v203, v13, v194, v203\n"
"v_fma_f32 v180, v8, v179, v180\n"
"v_fma_f32 v188, v8, v187, v188\n"
"v_fma_f32 v196, v12, v195, v196\n"
"v_fma_f32 v204, v12, v203, v204\n"
"v_fma_f32 v180, -v9, v187, v180\n"
"v_fma_f32 v188, v9, v179, v188\n"
"v_fma_f32 v196, -v13, v203, v196\n"
"v_fma_f32 v204, v13, v195, v204\n"
"v_fma_f32 v181, v8, v180, v181\n"
"v_fma_f32 v189, v8, v188, v189\n"
"v_fma_f32 v197, v12, v196, v197\n"
"v_fma_f32 v205, v12, v204, v205\n"
"v_fma_f32 v181, -v9, v188, v181\n"
"v_fma_f32 v189, v9, v180, v189\n"
"v_fma_f32 v197, -v13, v204, v197\n"
"v_fma_f32 v205, v13, v196, v205\n"
"v_fma_f32 v182, v8, v181, v182\n"
"v_fma_f32 v190, v8, v189, v190\n"
"v_fma_f32 v198, v12, v197, v198\n"
"v_fma_f32 v206, v12, v205, v206\n"
"v_fma_f32 v182, -v9, v189, v182\n"
"v_fma_f32 v190, v9, v181, v190\n"
"v_fma_f32 v198, -v13, v205, v198\n"
"v_fma_f32 v206, v13, v197, v206\n"
"v_fma_f32 v183, v8, v182, v183\n"
"v_fma_f32 v191, v8, v190, v191\n"
"v_fma_f32 v199, v12, v198, v199\n"
"v_fma_f32 v207, v12, v206, v207\n"
"v_fma_f32 v183, -v9, v190, v183\n"
"v_fma_f32 v191, v9, v182, v191\n"
"v_fma_f32 v199, -v13, v206, v199\n"
"v_fma_f32 v207, v13, v198, v207\n"
"v_cvt_pk_bf16_f32 v24, v176, v177\n"
"v_cvt_pk_bf16_f32 v28, -v184, -v185\n"
"v_cvt_pk_bf16_f32 v25, v178, v179\n"
"v_cvt_pk_bf16_f32 v29, -v186, -v187\n"
"v_cvt_pk_bf16_f32 v26, v180, v181\n"
"v_cvt_pk_bf16_f32 v30, -v188, -v189\n"
"v_cvt_pk_bf16_f32 v27, v182, v183\n"
"v_cvt_pk_bf16_f32 v31, -v190, -v191\n"
"ds_write_b128 v253, v[24:27] offset:16384\n"
"ds_write_b128 v253, v[28:31] offset:20480\n"
"v_cvt_pk_bf16_f32 v32, v192, v193\n"
"v_cvt_pk_bf16_f32 v36, -v200, -v201\n"
"v_cvt_pk_bf16_f32 v33, v194, v195\n"
"v_cvt_pk_bf16_f32 v37, -v202, -v203\n"
"v_cvt_pk_bf16_f32 v34, v196, v197\n"
"v_cvt_pk_bf16_f32 v38, -v204, -v205\n"
"v_cvt_pk_bf16_f32 v35, v198, v199\n"
"v_cvt_pk_bf16_f32 v39, -v206, -v207\n"
"ds_write_b128 v253, v[32:35] offset:24576\n"
"ds_write_b128 v253, v[36:39] offset:28672\n"
"s_cmp_ge_u32 s11, 4\n"
"s_cbranch_scc1 SC2_SKIP\n"
"v_fma_f32 v208, v16, v18, v208\n"
"v_fma_f32 v216, v16, v19, v216\n"
"v_fma_f32 v208, -v17, v19, v208\n"
"v_fma_f32 v216, v17, v18, v216\n"
"v_fma_f32 v209, v16, v208, v209\n"
"v_fma_f32 v217, v16, v216, v217\n"
"v_fma_f32 v209, -v17, v216, v209\n"
"v_fma_f32 v217, v17, v208, v217\n"
"v_fma_f32 v210, v16, v209, v210\n"
"v_fma_f32 v218, v16, v217, v218\n"
"v_fma_f32 v210, -v17, v217, v210\n"
"v_fma_f32 v218, v17, v209, v218\n"
"v_fma_f32 v211, v16, v210, v211\n"
"v_fma_f32 v219, v16, v218, v219\n"
"v_fma_f32 v211, -v17, v218, v211\n"
"v_fma_f32 v219, v17, v210, v219\n"
"v_fma_f32 v212, v16, v211, v212\n"
"v_fma_f32 v220, v16, v219, v220\n"
"v_fma_f32 v212, -v17, v219, v212\n"
"v_fma_f32 v220, v17, v211, v220\n"
"v_fma_f32 v213, v16, v212, v213\n"
"v_fma_f32 v221, v16, v220, v221\n"
"v_fma_f32 v213, -v17, v220, v213\n"
"v_fma_f32 v221, v17, v212, v221\n"
"v_fma_f32 v214, v16, v213, v214\n"
"v_fma_f32 v222, v16, v221, v222\n"
"v_fma_f32 v214, -v17, v221, v214\n"
"v_fma_f32 v222, v17, v213, v222\n"
"v_fma_f32 v215, v16, v214, v215\n"
"v_fma_f32 v223, v16, v222, v223\n"
"v_fma_f32 v215, -v17, v222, v215\n"
"v_fma_f32 v223, v17, v214, v223\n"
"v_cvt_pk_bf16_f32 v24, v208, v209\n"
"v_cvt_pk_bf16_f32 v28, -v216, -v217\n"
"v_cvt_pk_bf16_f32 v25, v210, v211\n"
"v_cvt_pk_bf16_f32 v29, -v218, -v219\n"
"v_cvt_pk_bf16_f32 v26, v212, v213\n"
"v_cvt_pk_bf16_f32 v30, -v220, -v221\n"
"v_cvt_pk_bf16_f32 v27, v214, v215\n"
"v_cvt_pk_bf16_f32 v31, -v222, -v223\n"
"ds_write_b128 v253, v[24:27] offset:32768\n"
"ds_write_b128 v253, v[28:31] offset:36864\n"
"SC2_SKIP:\n"
"s_waitcnt lgkmcnt(0)\n"
"v_lshrrev_b32 v200, 4, v248\n"
"v_and_b32 v201, 15, v248\n"
"s_and_b32 s76, s11, 3\n"
"s_lshr_b32 s77, s11, 2\n"
"v_lshrrev_b32 v202, 1, v201\n"
"v_xor_b32 v203, v202, v200\n"
"v_lshlrev_b32 v203, 4, v203\n"
"v_lshl_add_u32 v203, v201, 7, v203\n"
"s_lshl_b32 s78, s76, 11\n"
"v_add_u32 v196, s78, v203\n"
"v_xor_b32 v197, 64, v196\n"
"s_lshl_b32 s78, s34, 1\n"
"s_add_u32 s78, s78, s77\n"
"s_mul_i32 s78, s78, 36\n"
"s_add_u32 s78, s78, s76\n"
"s_lshl_b32 s78, s78, 5\n"
"s_add_u32 s78, s78, s35\n"
"s_lshl_b32 s81, s78, 10\n"
"v_lshlrev_b32_e64 v198, 4, v248\n"
"v_add_u32 v199, 16384, v198\n"
"s_mov_b32 s74, s43\n"
"s_nop 0\n"
"s_add_u32 s75, s43, 65536\n"
"s_add_u32 s80, s43, 98304\n"
"v_bfe_u32 v160, v248, 2, 2\n"
"v_and_b32 v161, 3, v248\n"
"v_and_b32 v202, 1, v200\n"
"v_lshlrev_b32_e64 v162, 3, v202\n"
"v_lshl_add_u32 v162, v160, 1, v162\n"
"v_lshrrev_b32 v203, 1, v161\n"
"s_lshl_b32 s78, s77, 3\n"
"v_add_u32 v203, s78, v203\n"
"v_xor_b32 v162, v162, v203\n"
"v_lshlrev_b32 v162, 4, v162\n"
"v_and_b32 v203, 1, v161\n"
"v_lshl_add_u32 v162, v203, 3, v162\n"
"v_add_u32 v162, 65536, v162\n"
"v_lshrrev_b32 v203, 1, v200\n"
"v_lshlrev_b32 v163, 5, v203\n"
"v_lshl_add_u32 v163, v202, 3, v163\n"
"v_add_u32_e64 v163, v163, v160\n"
"v_lshl_add_u32 v163, v163, 8, v162\n"
"v_lshl_add_u32 v164, v200, 3, v160\n"
"v_lshl_add_u32 v164, v164, 8, v162\n"
"v_add_u32 v164, 65536, v164\n"
"s_barrier\n"
"v_xor_b32 v166, 0, v163\n"
"v_xor_b32 v167, 16, v163\n"
"v_xor_b32 v168, 32, v163\n"
"v_xor_b32 v169, 48, v163\n"
"v_xor_b32 v170, 64, v163\n"
"v_xor_b32 v171, 80, v163\n"
"v_xor_b32 v172, 96, v163\n"
"v_xor_b32 v173, 112, v163\n"
"ds_read_b64_tr_b16 v[0:1], v166 offset:0\n"
"ds_read_b64_tr_b16 v[2:3], v167 offset:1024\n"
"ds_read_b64_tr_b16 v[36:37], v168 offset:0\n"
"ds_read_b64_tr_b16 v[38:39], v169 offset:1024\n"
"ds_read_b64_tr_b16 v[72:73], v170 offset:0\n"
"ds_read_b64_tr_b16 v[74:75], v171 offset:1024\n"
"ds_read_b64_tr_b16 v[108:109], v172 offset:0\n"
"ds_read_b64_tr_b16 v[110:111], v173 offset:1024\n"
"ds_read_b64_tr_b16 v[4:5], v166 offset:16384\n"
"ds_read_b64_tr_b16 v[6:7], v167 offset:17408\n"
"ds_read_b64_tr_b16 v[40:41], v168 offset:16384\n"
"ds_read_b64_tr_b16 v[42:43], v169 offset:17408\n"
"ds_read_b64_tr_b16 v[76:77], v170 offset:16384\n"
"ds_read_b64_tr_b16 v[78:79], v171 offset:17408\n"
"ds_read_b64_tr_b16 v[112:113], v172 offset:16384\n"
"ds_read_b64_tr_b16 v[114:115], v173 offset:17408\n"
"ds_read_b64_tr_b16 v[8:9], v166 offset:4096\n"
"ds_read_b64_tr_b16 v[10:11], v167 offset:5120\n"
"ds_read_b64_tr_b16 v[44:45], v168 offset:4096\n"
"ds_read_b64_tr_b16 v[46:47], v169 offset:5120\n"
"ds_read_b64_tr_b16 v[80:81], v170 offset:4096\n"
"ds_read_b64_tr_b16 v[82:83], v171 offset:5120\n"
"ds_read_b64_tr_b16 v[116:117], v172 offset:4096\n"
"ds_read_b64_tr_b16 v[118:119], v173 offset:5120\n"
"ds_read_b64_tr_b16 v[12:13], v166 offset:20480\n"
"ds_read_b64_tr_b16 v[14:15], v167 offset:21504\n"
"ds_read_b64_tr_b16 v[48:49], v168 offset:20480\n"
"ds_read_b64_tr_b16 v[50:51], v169 offset:21504\n"
"ds_read_b64_tr_b16 v[84:85], v170 offset:20480\n"
"ds_read_b64_tr_b16 v[86:87], v171 offset:21504\n"
"ds_read_b64_tr_b16 v[120:121], v172 offset:20480\n"
"ds_read_b64_tr_b16 v[122:123], v173 offset:21504\n"
"ds_read_b64_tr_b16 v[16:17], v166 offset:32768\n"
"ds_read_b64_tr_b16 v[18:19], v167 offset:33792\n"
"ds_read_b64_tr_b16 v[52:53], v168 offset:32768\n"
"ds_read_b64_tr_b16 v[54:55], v169 offset:33792\n"
"ds_read_b64_tr_b16 v[88:89], v170 offset:32768\n"
"ds_read_b64_tr_b16 v[90:91], v171 offset:33792\n"
"ds_read_b64_tr_b16 v[124:125], v172 offset:32768\n"
"ds_read_b64_tr_b16 v[126:127], v173 offset:33792\n"
"ds_read_b64_tr_b16 v[20:21], v166 offset:49152\n"
"ds_read_b64_tr_b16 v[22:23], v167 offset:50176\n"
"ds_read_b64_tr_b16 v[56:57], v168 offset:49152\n"
"ds_read_b64_tr_b16 v[58:59], v169 offset:50176\n"
"ds_read_b64_tr_b16 v[92:93], v170 offset:49152\n"
"ds_read_b64_tr_b16 v[94:95], v171 offset:50176\n"
"ds_read_b64_tr_b16 v[128:129], v172 offset:49152\n"
"ds_read_b64_tr_b16 v[130:131], v173 offset:50176\n"
"ds_read_b64_tr_b16 v[24:25], v166 offset:36864\n"
"ds_read_b64_tr_b16 v[26:27], v167 offset:37888\n"
"ds_read_b64_tr_b16 v[60:61], v168 offset:36864\n"
"ds_read_b64_tr_b16 v[62:63], v169 offset:37888\n"
"ds_read_b64_tr_b16 v[96:97], v170 offset:36864\n"
"ds_read_b64_tr_b16 v[98:99], v171 offset:37888\n"
"ds_read_b64_tr_b16 v[132:133], v172 offset:36864\n"
"ds_read_b64_tr_b16 v[134:135], v173 offset:37888\n"
"ds_read_b64_tr_b16 v[28:29], v166 offset:53248\n"
"ds_read_b64_tr_b16 v[30:31], v167 offset:54272\n"
"ds_read_b64_tr_b16 v[64:65], v168 offset:53248\n"
"ds_read_b64_tr_b16 v[66:67], v169 offset:54272\n"
"ds_read_b64_tr_b16 v[100:101], v170 offset:53248\n"
"ds_read_b64_tr_b16 v[102:103], v171 offset:54272\n"
"ds_read_b64_tr_b16 v[136:137], v172 offset:53248\n"
"ds_read_b64_tr_b16 v[138:139], v173 offset:54272\n"
"v_xor_b32 v166, 0, v164\n"
"v_xor_b32 v167, 16, v164\n"
"v_xor_b32 v168, 32, v164\n"
"v_xor_b32 v169, 48, v164\n"
"v_xor_b32_e64 v170, 64, v164\n"
"v_xor_b32 v171, 80, v164\n"
"v_xor_b32 v172, 96, v164\n"
"v_xor_b32 v173, 112, v164\n"
"ds_read_b64_tr_b16 v[32:33], v166 offset:0\n"
"ds_read_b64_tr_b16 v[34:35], v167 offset:1024\n"
"ds_read_b64_tr_b16 v[68:69], v168 offset:0\n"
"ds_read_b64_tr_b16 v[70:71], v169 offset:1024\n"
"ds_read_b64_tr_b16 v[104:105], v170 offset:0\n"
"ds_read_b64_tr_b16 v[106:107], v171 offset:1024\n"
"ds_read_b64_tr_b16 v[140:141], v172 offset:0\n"
"ds_read_b64_tr_b16 v[142:143], v173 offset:1024\n"
"s_and_b32 s76, s11, 3\n"
"s_lshl_b32 s76, s76, 10\n"
"s_add_u32 s76, s76, 32768\n"
"v_lshlrev_b32 v200, 4, v248\n"
"v_add_u32 v200, s76, v200\n"
"ds_read_b128 v[176:179], v196 offset:0\n"
"ds_read_b128 v[180:183], v197 offset:0\n"
"s_waitcnt vmcnt(0) lgkmcnt(0)\n"
"s_barrier\n"
".p2align 3\n"
"v_mfma_f32_16x16x32_bf16 v[144:147], v[176:179], v[0:3], 0\n"
"ds_read_b128 v[184:187], v196 offset:8192\n"
"s_add_u32 s70, s70, 73728\n"
"s_add_u32 s36, s70, s68\n"
"s_mov_b32 m0, s75\n"
"s_nop 0\n"
"s_nop 0\n"
"buffer_load_dwordx4 v254, s[16:19], s36 offen lds\n"
"v_mfma_f32_16x16x32_bf16 v[148:151], v[176:179], v[36:39], 0\n"
"buffer_load_dwordx4 v255, s[16:19], s36 offen offset:1024 lds\n"
"v_mfma_f32_16x16x32_bf16 v[152:155], v[176:179], v[72:75], 0\n"
"s_add_u32 s36, s70, s69\n"
"s_nop 0\n"
"s_add_u32 m0, s75, 16384\n"
"s_nop 0\n"
"s_nop 0\n"
"buffer_load_dwordx4 v254, s[16:19], s36 offen lds\n"
"v_mfma_f32_16x16x32_bf16 v[156:159], v[176:179], v[108:111], 0\n"
"buffer_load_dwordx4 v255, s[16:19], s36 offen offset:1024 lds\n"
"v_mfma_f32_16x16x32_bf16 v[144:147], v[180:183], v[4:7], v[144:147]\n"
"ds_read_b128 v[176:179], v197 offset:8192\n"
"v_mfma_f32_16x16x32_bf16 v[148:151], v[180:183], v[40:43], v[148:151]\n"
"v_mfma_f32_16x16x32_bf16 v[152:155], v[180:183], v[76:79], v[152:155]\n"
"v_add_co_u32 v224, vcc, 147456, v224\n"
"s_nop 1\n"
"v_addc_co_u32 v225, vcc, 0, v225, vcc\n"
"s_lshl_b32 s76, s11, 10\n"
"s_nop 0\n"
"s_add_u32 m0, s76, 131072\n"
"s_nop 0\n"
"s_nop 0\n"
"global_load_lds_dwordx4 v[224:225], off\n"
"v_mfma_f32_16x16x32_bf16 v[156:159], v[180:183], v[112:115], v[156:159]\n"
"s_waitcnt lgkmcnt(1)\n"
"s_nop 0\n"
"v_mfma_f32_16x16x32_bf16 v[144:147], v[184:187], v[8:11], v[144:147]\n"
"ds_read_b128 v[180:183], v196 offset:16384\n"
"v_mfma_f32_16x16x32_bf16 v[148:151], v[184:187], v[44:47], v[148:151]\n"
"v_add_co_u32 v224, vcc, 147456, v224\n"
"s_nop 1\n"
"v_addc_co_u32 v225, vcc, 0, v225, vcc\n"
"s_lshl_b32 s76, s11, 10\n"
"s_nop 0\n"
"s_add_u32 m0, s76, 139264\n"
"s_nop 0\n"
"s_nop 0\n"
"global_load_lds_dwordx4 v[224:225], off\n"
"v_mfma_f32_16x16x32_bf16 v[152:155], v[184:187], v[80:83], v[152:155]\n"
"v_mfma_f32_16x16x32_bf16 v[156:159], v[184:187], v[116:119], v[156:159]\n"
"s_waitcnt lgkmcnt(1)\n"
"s_nop 0\n"
"v_mfma_f32_16x16x32_bf16 v[144:147], v[176:179], v[12:15], v[144:147]\n"
"ds_read_b128 v[184:187], v197 offset:16384\n"
"v_add_co_u32 v224, vcc, 147456, v224\n"
"s_nop 1\n"
"v_addc_co_u32 v225, vcc, 0, v225, vcc\n"
"s_lshl_b32 s76, s11, 10\n"
"s_nop 0\n"
"s_add_u32 m0, s76, 147456\n"
"s_nop 0\n"
"s_nop 0\n"
"global_load_lds_dwordx4 v[224:225], off\n"
"v_mfma_f32_16x16x32_bf16 v[148:151], v[176:179], v[48:51], v[148:151]\n"
"v_mfma_f32_16x16x32_bf16 v[152:155], v[176:179], v[84:87], v[152:155]\n"
"v_mfma_f32_16x16x32_bf16 v[156:159], v[176:179], v[120:123], v[156:159]\n"
"s_cmp_lt_u32 s11, 4\n"
"s_nop 0\n"
"s_cselect_b32 s77, 147456, 0\n"
"v_add_co_u32 v224, vcc, s77, v224\n"
"s_nop 1\n"
"v_addc_co_u32 v225, vcc, 0, v225, vcc\n"
"s_lshl_b32 s76, s11, 10\n"
"s_cmp_lt_u32 s11, 4\n"
"s_nop 0\n"
"s_cselect_b32 s77, 8192, 0\n"
"s_add_u32 s77, s77, 147456\n"
"s_add_u32 m0, s76, s77\n"
"s_nop 0\n"
"global_load_lds_dwordx4 v[224:225], off\n"
"s_waitcnt lgkmcnt(1)\n"
"s_nop 0\n"
"v_mfma_f32_16x16x32_bf16 v[144:147], v[180:183], v[16:19], v[144:147]\n"
"ds_read_b128 v[176:179], v196 offset:24576\n"
"v_mfma_f32_16x16x32_bf16 v[148:151], v[180:183], v[52:55], v[148:151]\n"
"v_mfma_f32_16x16x32_bf16 v[152:155], v[180:183], v[88:91], v[152:155]\n"
"s_add_u32 s70, s70, 73728\n"
"s_add_u32 s36, s70, s68\n"
"s_mov_b32 m0, s80\n"
"s_nop 0\n"
"s_nop 0\n"
"buffer_load_dwordx4 v254, s[16:19], s36 offen lds\n"
"v_mfma_f32_16x16x32_bf16 v[156:159], v[180:183], v[124:127], v[156:159]\n"
"s_waitcnt lgkmcnt(1)\n"
"s_nop 0\n"
"v_mfma_f32_16x16x32_bf16 v[144:147], v[184:187], v[20:23], v[144:147]\n"
"ds_read_b128 v[180:183], v197 offset:24576\n"
"v_mfma_f32_16x16x32_bf16 v[148:151], v[184:187], v[56:59], v[148:151]\n"
"buffer_load_dwordx4 v255, s[16:19], s36 offen offset:1024 lds\n"
"v_mfma_f32_16x16x32_bf16 v[152:155], v[184:187], v[92:95], v[152:155]\n"
"v_mfma_f32_16x16x32_bf16 v[156:159], v[184:187], v[128:131], v[156:159]\n"
"s_waitcnt lgkmcnt(1)\n"
"s_nop 0\n"
"v_mfma_f32_16x16x32_bf16 v[144:147], v[176:179], v[24:27], v[144:147]\n"
"ds_read_b128 v[184:187], v200\n"
"s_add_u32 s36, s70, s69\n"
"s_nop 0\n"
"s_add_u32 m0, s80, 16384\n"
"s_nop 0\n"
"s_nop 0\n"
"buffer_load_dwordx4 v254, s[16:19], s36 offen lds\n"
"v_mfma_f32_16x16x32_bf16 v[148:151], v[176:179], v[60:63], v[148:151]\n"
"v_mfma_f32_16x16x32_bf16 v[152:155], v[176:179], v[96:99], v[152:155]\n"
"v_mfma_f32_16x16x32_bf16 v[156:159], v[176:179], v[132:135], v[156:159]\n"
"buffer_load_dwordx4 v255, s[16:19], s36 offen offset:1024 lds\n"
"s_waitcnt vmcnt(8) lgkmcnt(0)\n"
"s_barrier\n"
"s_sub_u32 s76, s75, s74\n"
"v_add_u32 v196, s76, v196\n"
"v_add_u32_e64 v197, s76, v197\n"
"v_add_u32 v200, 4096, v200\n"
"v_mfma_f32_16x16x32_bf16 v[144:147], v[180:183], v[28:31], v[144:147]\n"
"ds_read_b128 v[176:179], v196 offset:0\n"
"v_mfma_f32_16x16x32_bf16 v[148:151], v[180:183], v[64:67], v[148:151]\n"
"v_mfma_f32_16x16x32_bf16 v[152:155], v[180:183], v[100:103], v[152:155]\n"
"v_mfma_f32_16x16x32_bf16 v[156:159], v[180:183], v[136:139], v[156:159]\n"
"v_mfma_f32_16x16x32_bf16 v[160:163], v[184:187], v[32:35], v[144:147]\n"
"ds_read_b128 v[180:183], v197 offset:0\n"
"v_mfma_f32_16x16x32_bf16 v[164:167], v[184:187], v[68:71], v[148:151]\n"
"v_mfma_f32_16x16x32_bf16 v[168:171], v[184:187], v[104:107], v[152:155]\n"
"v_mfma_f32_16x16x32_bf16 v[172:175], v[184:187], v[140:143], v[156:159]\n"
"s_mov_b32 s76, s74\n"
"s_mov_b32 s74, s75\n"
"s_mov_b32 s75, s80\n"
"s_mov_b32 s80, s76\n"
"s_waitcnt lgkmcnt(1)\n"
"s_nop 0\n"
"v_mfma_f32_16x16x32_bf16 v[144:147], v[176:179], v[0:3], 0\n"
"ds_read_b128 v[184:187], v196 offset:8192\n"
"v_mfma_f32_16x16x32_bf16 v[148:151], v[176:179], v[36:39], 0\n"
"v_mfma_f32_16x16x32_bf16 v[152:155], v[176:179], v[72:75], 0\n"
"s_add_u32 s70, s70, 73728\n"
"s_add_u32 s36, s70, s68\n"
"s_mov_b32 m0, s80\n"
"s_nop 0\n"
"s_nop 0\n"
"buffer_load_dwordx4 v254, s[16:19], s36 offen lds\n"
"v_mfma_f32_16x16x32_bf16 v[156:159], v[176:179], v[108:111], 0\n"
"s_waitcnt lgkmcnt(1)\n"
"s_nop 0\n"
"v_mfma_f32_16x16x32_bf16 v[144:147], v[180:183], v[4:7], v[144:147]\n"
"ds_read_b128 v[176:179], v197 offset:8192\n"
"v_cvt_pk_bf16_f32 v188, v160, v161\n"
"v_mfma_f32_16x16x32_bf16 v[148:151], v[180:183], v[40:43], v[148:151]\n"
"v_cvt_pk_bf16_f32 v189, v162, v163\n"
"v_mfma_f32_16x16x32_bf16 v[152:155], v[180:183], v[76:79], v[152:155]\n"
"v_cvt_pk_bf16_f32 v190, v164, v165\n"
"v_mfma_f32_16x16x32_bf16 v[156:159], v[180:183], v[112:115], v[156:159]\n"
"v_cvt_pk_bf16_f32 v191, v166, v167\n"
"s_waitcnt lgkmcnt(1)\n"
"s_nop 0\n"
"v_mfma_f32_16x16x32_bf16 v[144:147], v[184:187], v[8:11], v[144:147]\n"
"ds_read_b128 v[180:183], v196 offset:16384\n"
"v_cvt_pk_bf16_f32 v192, v168, v169\n"
"v_mfma_f32_16x16x32_bf16 v[148:151], v[184:187], v[44:47], v[148:151]\n"
"buffer_load_dwordx4 v255, s[16:19], s36 offen offset:1024 lds\n"
"v_cvt_pk_bf16_f32 v193, v170, v171\n"
"v_mfma_f32_16x16x32_bf16 v[152:155], v[184:187], v[80:83], v[152:155]\n"
"v_cvt_pk_bf16_f32 v194, v172, v173\n"
"v_mfma_f32_16x16x32_bf16 v[156:159], v[184:187], v[116:119], v[156:159]\n"
"v_cvt_pk_bf16_f32 v195, v174, v175\n"
"s_waitcnt lgkmcnt(1)\n"
"s_nop 0\n"
"v_mfma_f32_16x16x32_bf16 v[144:147], v[176:179], v[12:15], v[144:147]\n"
"ds_read_b128 v[184:187], v197 offset:16384\n"
"buffer_store_dwordx4 v[188:191], v198, s[20:23], s81 offen sc1\n"
"v_mfma_f32_16x16x32_bf16 v[148:151], v[176:179], v[48:51], v[148:151]\n"
"v_mfma_f32_16x16x32_bf16 v[152:155], v[176:179], v[84:87], v[152:155]\n"
"v_mfma_f32_16x16x32_bf16 v[156:159], v[176:179], v[120:123], v[156:159]\n"
"s_waitcnt lgkmcnt(1)\n"
"s_nop 0\n"
"v_mfma_f32_16x16x32_bf16 v[144:147], v[180:183], v[16:19], v[144:147]\n"
"ds_read_b128 v[176:179], v196 offset:24576\n"
"s_add_u32 s36, s70, s69\n"
"s_nop 0\n"
"s_add_u32 m0, s80, 16384\n"
"s_nop 0\n"
"s_nop 0\n"
"buffer_load_dwordx4 v254, s[16:19], s36 offen lds\n"
"v_mfma_f32_16x16x32_bf16 v[148:151], v[180:183], v[52:55], v[148:151]\n"
"v_mfma_f32_16x16x32_bf16 v[152:155], v[180:183], v[88:91], v[152:155]\n"
"v_mfma_f32_16x16x32_bf16 v[156:159], v[180:183], v[124:127], v[156:159]\n"
"buffer_store_dwordx4 v[192:195], v199, s[20:23], s81 offen sc1\n"
"s_add_u32 s81, s81, 131072\n"
"s_waitcnt lgkmcnt(1)\n"
"s_nop 0\n"
"v_mfma_f32_16x16x32_bf16 v[144:147], v[184:187], v[20:23], v[144:147]\n"
"ds_read_b128 v[180:183], v197 offset:24576\n"
"v_mfma_f32_16x16x32_bf16 v[148:151], v[184:187], v[56:59], v[148:151]\n"
"v_mfma_f32_16x16x32_bf16 v[152:155], v[184:187], v[92:95], v[152:155]\n"
"v_mfma_f32_16x16x32_bf16 v[156:159], v[184:187], v[128:131], v[156:159]\n"
"buffer_load_dwordx4 v255, s[16:19], s36 offen offset:1024 lds\n"
"s_waitcnt lgkmcnt(1)\n"
"s_nop 0\n"
"v_mfma_f32_16x16x32_bf16 v[144:147], v[176:179], v[24:27], v[144:147]\n"
"ds_read_b128 v[184:187], v200\n"
"v_mfma_f32_16x16x32_bf16 v[148:151], v[176:179], v[60:63], v[148:151]\n"
"v_mfma_f32_16x16x32_bf16 v[152:155], v[176:179], v[96:99], v[152:155]\n"
"v_mfma_f32_16x16x32_bf16 v[156:159], v[176:179], v[132:135], v[156:159]\n"
"s_waitcnt vmcnt(6) lgkmcnt(0)\n"
"s_barrier\n"
"s_sub_u32 s76, s75, s74\n"
"v_add_u32 v196, s76, v196\n"
"v_add_u32_e64 v197, s76, v197\n"
"v_add_u32 v200, 94208, v200\n"
"v_mfma_f32_16x16x32_bf16 v[144:147], v[180:183], v[28:31], v[144:147]\n"
"ds_read_b128 v[176:179], v196 offset:0\n"
"v_mfma_f32_16x16x32_bf16 v[148:151], v[180:183], v[64:67], v[148:151]\n"
"v_mfma_f32_16x16x32_bf16 v[152:155], v[180:183], v[100:103], v[152:155]\n"
"v_mfma_f32_16x16x32_bf16 v[156:159], v[180:183], v[136:139], v[156:159]\n"
"v_mfma_f32_16x16x32_bf16 v[160:163], v[184:187], v[32:35], v[144:147]\n"
"ds_read_b128 v[180:183], v197 offset:0\n"
"v_mfma_f32_16x16x32_bf16 v[164:167], v[184:187], v[68:71], v[148:151]\n"
"v_mfma_f32_16x16x32_bf16 v[168:171], v[184:187], v[104:107], v[152:155]\n"
"v_mfma_f32_16x16x32_bf16 v[172:175], v[184:187], v[140:143], v[156:159]\n"
"s_mov_b32 s76, s74\n"
"s_mov_b32 s74, s75\n"
"s_mov_b32 s75, s80\n"
"s_mov_b32 s80, s76\n"
"s_mov_b32 s39, 5\n"
".p2align 3\n"
"S2LOOP:\n"
"s_waitcnt lgkmcnt(1)\n"
"s_nop 0\n"
"v_mfma_f32_16x16x32_bf16 v[144:147], v[176:179], v[0:3], 0\n"
"ds_read_b128 v[184:187], v196 offset:8192\n"
"v_mfma_f32_16x16x32_bf16 v[148:151], v[176:179], v[36:39], 0\n"
"v_mfma_f32_16x16x32_bf16 v[152:155], v[176:179], v[72:75], 0\n"
"s_add_u32 s70, s70, 73728\n"
"s_add_u32 s36, s70, s68\n"
"s_mov_b32 m0, s80\n"
"s_nop 0\n"
"s_nop 0\n"
"buffer_load_dwordx4 v254, s[16:19], s36 offen lds\n"
"v_mfma_f32_16x16x32_bf16 v[156:159], v[176:179], v[108:111], 0\n"
"s_waitcnt lgkmcnt(1)\n"
"s_nop 0\n"
"v_mfma_f32_16x16x32_bf16 v[144:147], v[180:183], v[4:7], v[144:147]\n"
"ds_read_b128 v[176:179], v197 offset:8192\n"
"v_cvt_pk_bf16_f32 v188, v160, v161\n"
"v_mfma_f32_16x16x32_bf16 v[148:151], v[180:183], v[40:43], v[148:151]\n"
"v_cvt_pk_bf16_f32 v189, v162, v163\n"
"v_mfma_f32_16x16x32_bf16 v[152:155], v[180:183], v[76:79], v[152:155]\n"
"v_cvt_pk_bf16_f32 v190, v164, v165\n"
"v_mfma_f32_16x16x32_bf16 v[156:159], v[180:183], v[112:115], v[156:159]\n"
"v_cvt_pk_bf16_f32 v191, v166, v167\n"
"s_waitcnt lgkmcnt(1)\n"
"s_nop 0\n"
"v_mfma_f32_16x16x32_bf16 v[144:147], v[184:187], v[8:11], v[144:147]\n"
"ds_read_b128 v[180:183], v196 offset:16384\n"
"v_cvt_pk_bf16_f32 v192, v168, v169\n"
"v_mfma_f32_16x16x32_bf16 v[148:151], v[184:187], v[44:47], v[148:151]\n"
"buffer_load_dwordx4 v255, s[16:19], s36 offen offset:1024 lds\n"
"v_cvt_pk_bf16_f32 v193, v170, v171\n"
"v_mfma_f32_16x16x32_bf16 v[152:155], v[184:187], v[80:83], v[152:155]\n"
"v_cvt_pk_bf16_f32 v194, v172, v173\n"
"v_mfma_f32_16x16x32_bf16 v[156:159], v[184:187], v[116:119], v[156:159]\n"
"v_cvt_pk_bf16_f32 v195, v174, v175\n"
"s_waitcnt lgkmcnt(1)\n"
"s_nop 0\n"
"v_mfma_f32_16x16x32_bf16 v[144:147], v[176:179], v[12:15], v[144:147]\n"
"ds_read_b128 v[184:187], v197 offset:16384\n"
"buffer_store_dwordx4 v[188:191], v198, s[20:23], s81 offen sc1\n"
"v_mfma_f32_16x16x32_bf16 v[148:151], v[176:179], v[48:51], v[148:151]\n"
"v_mfma_f32_16x16x32_bf16 v[152:155], v[176:179], v[84:87], v[152:155]\n"
"v_mfma_f32_16x16x32_bf16 v[156:159], v[176:179], v[120:123], v[156:159]\n"
"s_waitcnt lgkmcnt(1)\n"
"s_nop 0\n"
"v_mfma_f32_16x16x32_bf16 v[144:147], v[180:183], v[16:19], v[144:147]\n"
"ds_read_b128 v[176:179], v196 offset:24576\n"
"s_add_u32 s36, s70, s69\n"
"s_nop 0\n"
"s_add_u32 m0, s80, 16384\n"
"s_nop 0\n"
"s_nop 0\n"
"buffer_load_dwordx4 v254, s[16:19], s36 offen lds\n"
"v_mfma_f32_16x16x32_bf16 v[148:151], v[180:183], v[52:55], v[148:151]\n"
"v_mfma_f32_16x16x32_bf16 v[152:155], v[180:183], v[88:91], v[152:155]\n"
"v_mfma_f32_16x16x32_bf16 v[156:159], v[180:183], v[124:127], v[156:159]\n"
"buffer_store_dwordx4 v[192:195], v199, s[20:23], s81 offen sc1\n"
"s_add_u32 s81, s81, 131072\n"
"s_waitcnt lgkmcnt(1)\n"
"s_nop 0\n"
"v_mfma_f32_16x16x32_bf16 v[144:147], v[184:187], v[20:23], v[144:147]\n"
"ds_read_b128 v[180:183], v197 offset:24576\n"
"v_mfma_f32_16x16x32_bf16 v[148:151], v[184:187], v[56:59], v[148:151]\n"
"v_mfma_f32_16x16x32_bf16 v[152:155], v[184:187], v[92:95], v[152:155]\n"
"v_mfma_f32_16x16x32_bf16 v[156:159], v[184:187], v[128:131], v[156:159]\n"
"buffer_load_dwordx4 v255, s[16:19], s36 offen offset:1024 lds\n"
"s_waitcnt lgkmcnt(1)\n"
"s_nop 0\n"
"v_mfma_f32_16x16x32_bf16 v[144:147], v[176:179], v[24:27], v[144:147]\n"
"ds_read_b128 v[184:187], v200\n"
"v_mfma_f32_16x16x32_bf16 v[148:151], v[176:179], v[60:63], v[148:151]\n"
"v_mfma_f32_16x16x32_bf16 v[152:155], v[176:179], v[96:99], v[152:155]\n"
"v_mfma_f32_16x16x32_bf16 v[156:159], v[176:179], v[132:135], v[156:159]\n"
"s_waitcnt vmcnt(6) lgkmcnt(0)\n"
"s_barrier\n"
"s_sub_u32 s76, s75, s74\n"
"v_add_u32 v196, s76, v196\n"
"v_add_u32_e64 v197, s76, v197\n"
"v_add_u32 v200, 4096, v200\n"
"v_mfma_f32_16x16x32_bf16 v[144:147], v[180:183], v[28:31], v[144:147]\n"
"ds_read_b128 v[176:179], v196 offset:0\n"
"v_mfma_f32_16x16x32_bf16 v[148:151], v[180:183], v[64:67], v[148:151]\n"
"v_mfma_f32_16x16x32_bf16 v[152:155], v[180:183], v[100:103], v[152:155]\n"
"v_mfma_f32_16x16x32_bf16 v[156:159], v[180:183], v[136:139], v[156:159]\n"
"v_mfma_f32_16x16x32_bf16 v[160:163], v[184:187], v[32:35], v[144:147]\n"
"ds_read_b128 v[180:183], v197 offset:0\n"
"v_mfma_f32_16x16x32_bf16 v[164:167], v[184:187], v[68:71], v[148:151]\n"
"v_mfma_f32_16x16x32_bf16 v[168:171], v[184:187], v[104:107], v[152:155]\n"
"v_mfma_f32_16x16x32_bf16 v[172:175], v[184:187], v[140:143], v[156:159]\n"
"s_mov_b32 s76, s74\n"
"s_mov_b32 s74, s75\n"
"s_mov_b32 s75, s80\n"
"s_mov_b32 s80, s76\n"
"s_sub_u32 s39, s39, 1\n"
"s_cmp_lg_u32 s39, 0\n"
"s_cbranch_scc1 S2LOOP\n"
"s_cmp_lg_u32 s11, 0\n"
"s_cbranch_scc1 MP_SKIP2\n"
"s_lshl_b32 s76, s34, 4\n"
"s_add_u32 s76, s76, s35\n"
"s_lshl_b32 s76, s76, 3\n"
"v_mov_b32 v232, s4\n"
"v_mov_b32 v233, s5\n"
"v_mov_b32 v234, s76\n"
"s_mov_b64 exec, 1\n"
"global_store_dwordx2 v234, v[232:233], s[100:101] sc1\n"
"s_mov_b64 exec, -1\n"
"MP_SKIP2:\n"
"s_waitcnt lgkmcnt(1)\n"
"v_mfma_f32_16x16x32_bf16 v[144:147], v[176:179], v[0:3], 0\n"
"ds_read_b128 v[184:187], v196 offset:8192\n"
"v_mfma_f32_16x16x32_bf16 v[148:151], v[176:179], v[36:39], 0\n"
"v_mfma_f32_16x16x32_bf16 v[152:155], v[176:179], v[72:75], 0\n"
"v_mfma_f32_16x16x32_bf16 v[156:159], v[176:179], v[108:111], 0\n"
"s_waitcnt lgkmcnt(1)\n"
"s_nop 0\n"
"v_mfma_f32_16x16x32_bf16 v[144:147], v[180:183], v[4:7], v[144:147]\n"
"ds_read_b128 v[176:179], v197 offset:8192\n"
"v_cvt_pk_bf16_f32 v188, v160, v161\n"
"v_mfma_f32_16x16x32_bf16 v[148:151], v[180:183], v[40:43], v[148:151]\n"
"v_cvt_pk_bf16_f32 v189, v162, v163\n"
"v_mfma_f32_16x16x32_bf16 v[152:155], v[180:183], v[76:79], v[152:155]\n"
"v_cvt_pk_bf16_f32 v190, v164, v165\n"
"v_mfma_f32_16x16x32_bf16 v[156:159], v[180:183], v[112:115], v[156:159]\n"
"v_cvt_pk_bf16_f32 v191, v166, v167\n"
"s_waitcnt lgkmcnt(1)\n"
"s_nop 0\n"
"v_mfma_f32_16x16x32_bf16 v[144:147], v[184:187], v[8:11], v[144:147]\n"
"ds_read_b128 v[180:183], v196 offset:16384\n"
"v_cvt_pk_bf16_f32 v192, v168, v169\n"
"v_mfma_f32_16x16x32_bf16 v[148:151], v[184:187], v[44:47], v[148:151]\n"
"v_cvt_pk_bf16_f32 v193, v170, v171\n"
"v_mfma_f32_16x16x32_bf16 v[152:155], v[184:187], v[80:83], v[152:155]\n"
"v_cvt_pk_bf16_f32 v194, v172, v173\n"
"v_mfma_f32_16x16x32_bf16 v[156:159], v[184:187], v[116:119], v[156:159]\n"
"v_cvt_pk_bf16_f32 v195, v174, v175\n"
"s_waitcnt lgkmcnt(1)\n"
"s_nop 0\n"
"v_mfma_f32_16x16x32_bf16 v[144:147], v[176:179], v[12:15], v[144:147]\n"
"ds_read_b128 v[184:187], v197 offset:16384\n"
"buffer_store_dwordx4 v[188:191], v198, s[20:23], s81 offen sc1\n"
"v_mfma_f32_16x16x32_bf16 v[148:151], v[176:179], v[48:51], v[148:151]\n"
"v_mfma_f32_16x16x32_bf16 v[152:155], v[176:179], v[84:87], v[152:155]\n"
"v_mfma_f32_16x16x32_bf16 v[156:159], v[176:179], v[120:123], v[156:159]\n"
"s_waitcnt lgkmcnt(1)\n"
"s_nop 0\n"
"v_mfma_f32_16x16x32_bf16 v[144:147], v[180:183], v[16:19], v[144:147]\n"
"ds_read_b128 v[176:179], v196 offset:24576\n"
"v_mfma_f32_16x16x32_bf16 v[148:151], v[180:183], v[52:55], v[148:151]\n"
"v_mfma_f32_16x16x32_bf16 v[152:155], v[180:183], v[88:91], v[152:155]\n"
"v_mfma_f32_16x16x32_bf16 v[156:159], v[180:183], v[124:127], v[156:159]\n"
"buffer_store_dwordx4 v[192:195], v199, s[20:23], s81 offen sc1\n"
"s_add_u32 s81, s81, 131072\n"
"s_waitcnt lgkmcnt(1)\n"
"s_nop 0\n"
"v_mfma_f32_16x16x32_bf16 v[144:147], v[184:187], v[20:23], v[144:147]\n"
"ds_read_b128 v[180:183], v197 offset:24576\n"
"v_mfma_f32_16x16x32_bf16 v[148:151], v[184:187], v[56:59], v[148:151]\n"
"v_mfma_f32_16x16x32_bf16 v[152:155], v[184:187], v[92:95], v[152:155]\n"
"v_mfma_f32_16x16x32_bf16 v[156:159], v[184:187], v[128:131], v[156:159]\n"
"v_and_b32 v234, 15, v248\n"
"v_lshlrev_b32 v234, 3, v234\n"
"s_lshl_b32 s76, s34, 7\n"
"v_add_u32 v234, s76, v234\n"
"global_load_dwordx2 v[232:233], v234, s[100:101] sc1\n"
"s_waitcnt lgkmcnt(1)\n"
"s_nop 0\n"
"v_mfma_f32_16x16x32_bf16 v[144:147], v[176:179], v[24:27], v[144:147]\n"
"ds_read_b128 v[184:187], v200\n"
"v_mfma_f32_16x16x32_bf16 v[148:151], v[176:179], v[60:63], v[148:151]\n"
"v_mfma_f32_16x16x32_bf16 v[152:155], v[176:179], v[96:99], v[152:155]\n"
"v_mfma_f32_16x16x32_bf16 v[156:159], v[176:179], v[132:135], v[156:159]\n"
"s_waitcnt vmcnt(3) lgkmcnt(0)\n"
"s_barrier\n"
"s_sub_u32 s76, s75, s74\n"
"v_add_u32 v196, s76, v196\n"
"v_add_u32_e64 v197, s76, v197\n"
"v_add_u32 v200, 4096, v200\n"
"v_mfma_f32_16x16x32_bf16 v[144:147], v[180:183], v[28:31], v[144:147]\n"
"ds_read_b128 v[176:179], v196 offset:0\n"
"v_mfma_f32_16x16x32_bf16 v[148:151], v[180:183], v[64:67], v[148:151]\n"
"v_mfma_f32_16x16x32_bf16 v[152:155], v[180:183], v[100:103], v[152:155]\n"
"v_mfma_f32_16x16x32_bf16 v[156:159], v[180:183], v[136:139], v[156:159]\n"
"v_mfma_f32_16x16x32_bf16 v[160:163], v[184:187], v[32:35], v[144:147]\n"
"ds_read_b128 v[180:183], v197 offset:0\n"
"v_mfma_f32_16x16x32_bf16 v[164:167], v[184:187], v[68:71], v[148:151]\n"
"v_mfma_f32_16x16x32_bf16 v[168:171], v[184:187], v[104:107], v[152:155]\n"
"v_mfma_f32_16x16x32_bf16 v[172:175], v[184:187], v[140:143], v[156:159]\n"
"s_mov_b32 s76, s74\n"
"s_mov_b32 s74, s75\n"
"s_mov_b32 s75, s80\n"
"s_mov_b32 s80, s76\n"
"s_waitcnt lgkmcnt(1)\n"
"s_nop 0\n"
"v_mfma_f32_16x16x32_bf16 v[144:147], v[176:179], v[0:3], 0\n"
"ds_read_b128 v[184:187], v196 offset:8192\n"
"v_mfma_f32_16x16x32_bf16 v[148:151], v[176:179], v[36:39], 0\n"
"v_mfma_f32_16x16x32_bf16 v[152:155], v[176:179], v[72:75], 0\n"
"v_mfma_f32_16x16x32_bf16 v[156:159], v[176:179], v[108:111], 0\n"
"s_waitcnt vmcnt(0)\n"
"s_mov_b32 s63, 0\n"
"MC2_T:\n"
"v_cmp_ne_u32 vcc, s4, v232\n"
"s_nop 0\n"
"v_cmp_ne_u32 s[76:77], s5, v233\n"
"s_nop 3\n"
"s_or_b64 vcc, vcc, s[76:77]\n"
"s_cmp_eq_u64 vcc, 0\n"
"s_cbranch_scc1 MC2_OK\n"
"s_add_u32 s63, s63, 1\n"
"s_nop 0\n"
"s_cmp_gt_u32 s63, 400000\n"
"s_cbranch_scc1 MC2_OK\n"
"s_sleep 1\n"
"global_load_dwordx2 v[232:233], v234, s[100:101] sc1\n"
"s_waitcnt vmcnt(0)\n"
"s_branch MC2_T\n"
"MC2_OK:\n"
"s_waitcnt lgkmcnt(1)\n"
"s_nop 0\n"
"v_mfma_f32_16x16x32_bf16 v[144:147], v[180:183], v[4:7], v[144:147]\n"
"ds_read_b128 v[176:179], v197 offset:8192\n"
"v_cvt_pk_bf16_f32 v188, v160, v161\n"
"v_mfma_f32_16x16x32_bf16 v[148:151], v[180:183], v[40:43], v[148:151]\n"
"s_mul_i32 s25, s35, 5\n"
"s_cmp_ge_u32 s25, 40\n"
"s_cselect_b32 s79, 32, 0\n"
"s_add_u32 s25, s25, s79\n"
"s_mul_i32 s26, s34, 144\n"
"s_add_u32 s25, s25, s26\n"
"s_lshl_b32 s25, s25, 14\n"
"s_lshl_b32 s26, s11, 11\n"
"s_add_u32 s25, s25, s26\n"
"s_add_u32 m0, s26, 0\n"
"s_nop 0\n"
"buffer_load_dwordx4 v198, s[20:23], s25 offen sc1 lds\n"
"v_cvt_pk_bf16_f32 v189, v162, v163\n"
"v_mfma_f32_16x16x32_bf16 v[152:155], v[180:183], v[76:79], v[152:155]\n"
"v_cvt_pk_bf16_f32 v190, v164, v165\n"
"v_mfma_f32_16x16x32_bf16 v[156:159], v[180:183], v[112:115], v[156:159]\n"
"v_cvt_pk_bf16_f32 v191, v166, v167\n"
"s_waitcnt lgkmcnt(1)\n"
"s_nop 0\n"
"v_mfma_f32_16x16x32_bf16 v[144:147], v[184:187], v[8:11], v[144:147]\n"
"ds_read_b128 v[180:183], v196 offset:16384\n"
"buffer_load_dwordx4 v198, s[20:23], s25 offen offset:1024 sc1 lds\n"
"v_cvt_pk_bf16_f32 v192, v168, v169\n"
"v_mfma_f32_16x16x32_bf16 v[148:151], v[184:187], v[44:47], v[148:151]\n"
"v_cvt_pk_bf16_f32 v193, v170, v171\n"
"v_mfma_f32_16x16x32_bf16 v[152:155], v[184:187], v[80:83], v[152:155]\n"
"v_cvt_pk_bf16_f32 v194, v172, v173\n"
"v_mfma_f32_16x16x32_bf16 v[156:159], v[184:187], v[116:119], v[156:159]\n"
"s_mul_i32 s25, s35, 5\n"
"s_add_u32 s25, s25, 1\n"
"s_cmp_ge_u32 s25, 40\n"
"s_cselect_b32 s79, 32, 0\n"
"s_add_u32 s25, s25, s79\n"
"s_nop 0\n"
"s_mul_i32 s26, s34, 144\n"
"s_add_u32 s25, s25, s26\n"
"s_lshl_b32 s25, s25, 14\n"
"s_lshl_b32 s26, s11, 11\n"
"s_add_u32 s25, s25, s26\n"
"s_add_u32 m0, s26, 16384\n"
"s_nop 0\n"
"s_nop 0\n"
"buffer_load_dwordx4 v198, s[20:23], s25 offen sc1 lds\n"
"v_cvt_pk_bf16_f32 v195, v174, v175\n"
"s_waitcnt lgkmcnt(1)\n"
"s_nop 0\n"
"v_mfma_f32_16x16x32_bf16 v[144:147], v[176:179], v[12:15], v[144:147]\n"
"ds_read_b128 v[184:187], v197 offset:16384\n"
"buffer_store_dwordx4 v[188:191], v198, s[20:23], s81 offen sc1\n"
"v_mfma_f32_16x16x32_bf16 v[148:151], v[176:179], v[48:51], v[148:151]\n"
"v_mfma_f32_16x16x32_bf16 v[152:155], v[176:179], v[84:87], v[152:155]\n"
"buffer_load_dwordx4 v198, s[20:23], s25 offen offset:1024 sc1 lds\n"
"v_mfma_f32_16x16x32_bf16 v[156:159], v[176:179], v[120:123], v[156:159]\n"
"s_waitcnt lgkmcnt(1)\n"
"s_nop 0\n"
"v_mfma_f32_16x16x32_bf16 v[144:147], v[180:183], v[16:19], v[144:147]\n"
"ds_read_b128 v[176:179], v196 offset:24576\n"
"v_mfma_f32_16x16x32_bf16 v[148:151], v[180:183], v[52:55], v[148:151]\n"
"s_mul_i32 s25, s35, 5\n"
"s_add_u32 s25, s25, 2\n"
"s_cmp_ge_u32 s25, 40\n"
"s_cselect_b32 s79, 32, 0\n"
"s_add_u32 s25, s25, s79\n"
"s_nop 0\n"
"s_mul_i32 s26, s34, 144\n"
"s_add_u32 s25, s25, s26\n"
"s_lshl_b32 s25, s25, 14\n"
"s_lshl_b32 s26, s11, 11\n"
"s_add_u32 s25, s25, s26\n"
"s_add_u32 m0, s26, 65536\n"
"s_nop 0\n"
"s_nop 0\n"
"buffer_load_dwordx4 v198, s[20:23], s25 offen sc1 lds\n"
"v_mfma_f32_16x16x32_bf16 v[152:155], v[180:183], v[88:91], v[152:155]\n"
"v_mfma_f32_16x16x32_bf16 v[156:159], v[180:183], v[124:127], v[156:159]\n"
"buffer_store_dwordx4 v[192:195], v199, s[20:23], s81 offen sc1\n"
"s_add_u32 s81, s81, 131072\n"
"s_waitcnt lgkmcnt(1)\n"
"s_nop 0\n"
"v_mfma_f32_16x16x32_bf16 v[144:147], v[184:187], v[20:23], v[144:147]\n"
"ds_read_b128 v[180:183], v197 offset:24576\n"
"buffer_load_dwordx4 v198, s[20:23], s25 offen offset:1024 sc1 lds\n"
"v_mfma_f32_16x16x32_bf16 v[148:151], v[184:187], v[56:59], v[148:151]\n"
"v_mfma_f32_16x16x32_bf16 v[152:155], v[184:187], v[92:95], v[152:155]\n"
"v_mfma_f32_16x16x32_bf16 v[156:159], v[184:187], v[128:131], v[156:159]\n"
"s_mul_i32 s25, s35, 5\n"
"s_add_u32 s25, s25, 3\n"
"s_cmp_ge_u32 s25, 40\n"
"s_cselect_b32 s79, 32, 0\n"
"s_add_u32 s25, s25, s79\n"
"s_nop 0\n"
"s_mul_i32 s26, s34, 144\n"
"s_add_u32 s25, s25, s26\n"
"s_lshl_b32 s25, s25, 14\n"
"s_lshl_b32 s26, s11, 11\n"
"s_add_u32 s25, s25, s26\n"
"s_add_u32 m0, s26, 81920\n"
"s_nop 0\n"
"s_nop 0\n"
"buffer_load_dwordx4 v198, s[20:23], s25 offen sc1 lds\n"
"s_waitcnt lgkmcnt(1)\n"
"s_nop 0\n"
"v_mfma_f32_16x16x32_bf16 v[144:147], v[176:179], v[24:27], v[144:147]\n"
"ds_read_b128 v[184:187], v200\n"
"v_mfma_f32_16x16x32_bf16 v[148:151], v[176:179], v[60:63], v[148:151]\n"
"v_mfma_f32_16x16x32_bf16 v[152:155], v[176:179], v[96:99], v[152:155]\n"
"buffer_load_dwordx4 v198, s[20:23], s25 offen offset:1024 sc1 lds\n"
"v_mfma_f32_16x16x32_bf16 v[156:159], v[176:179], v[132:135], v[156:159]\n"
"s_waitcnt lgkmcnt(1)\n"
"s_nop 0\n"
"v_mfma_f32_16x16x32_bf16 v[144:147], v[180:183], v[28:31], v[144:147]\n"
"v_mfma_f32_16x16x32_bf16 v[148:151], v[180:183], v[64:67], v[148:151]\n"
"s_mul_i32 s25, s35, 5\n"
"s_add_u32 s25, s25, 4\n"
"s_cmp_ge_u32 s25, 40\n"
"s_cselect_b32 s79, 32, 0\n"
"s_add_u32 s25, s25, s79\n"
"s_nop 0\n"
"s_mul_i32 s26, s34, 144\n"
"s_add_u32 s25, s25, s26\n"
"s_lshl_b32 s25, s25, 14\n"
"s_lshl_b32 s26, s11, 11\n"
"s_add_u32 s25, s25, s26\n"
"s_add_u32 m0, s26, 131072\n"
"s_nop 0\n"
"s_nop 0\n"
"buffer_load_dwordx4 v198, s[20:23], s25 offen sc1 lds\n"
"v_mfma_f32_16x16x32_bf16 v[152:155], v[180:183], v[100:103], v[152:155]\n"
"v_mfma_f32_16x16x32_bf16 v[156:159], v[180:183], v[136:139], v[156:159]\n"
"s_waitcnt lgkmcnt(0)\n"
"s_nop 0\n"
"v_mfma_f32_16x16x32_bf16 v[160:163], v[184:187], v[32:35], v[144:147]\n"
"buffer_load_dwordx4 v198, s[20:23], s25 offen offset:1024 sc1 lds\n"
"v_mfma_f32_16x16x32_bf16 v[164:167], v[184:187], v[68:71], v[148:151]\n"
"v_mfma_f32_16x16x32_bf16 v[168:171], v[184:187], v[104:107], v[152:155]\n"
"v_mfma_f32_16x16x32_bf16 v[172:175], v[184:187], v[140:143], v[156:159]\n"
"s_nop 7\n"
"s_nop 0\n"
"v_cvt_pk_bf16_f32 v188, v160, v161\n"
"v_cvt_pk_bf16_f32 v189, v162, v163\n"
"v_cvt_pk_bf16_f32 v190, v164, v165\n"
"v_cvt_pk_bf16_f32 v191, v166, v167\n"
"v_cvt_pk_bf16_f32 v192, v168, v169\n"
"v_cvt_pk_bf16_f32 v193, v170, v171\n"
"v_cvt_pk_bf16_f32 v194, v172, v173\n"
"v_cvt_pk_bf16_f32 v195, v174, v175\n"
"buffer_store_dwordx4 v[188:191], v198, s[20:23], s81 offen sc1\n"
"buffer_store_dwordx4 v[192:195], v199, s[20:23], s81 offen sc1\n"
"s_add_u32 s81, s81, 131072\n"
"s_waitcnt vmcnt(7)\n"
"s_barrier\n"
"s_cmp_lg_u32 s11, 6\n"
"s_cbranch_scc1 ML_SKIP2\n"
"s_lshl_b32 s76, s34, 4\n"
"s_add_u32 s76, s76, s35\n"
"s_lshl_b32 s76, s76, 3\n"
"v_mov_b32 v232, s4\n"
"v_mov_b32 v233, s5\n"
"v_mov_b32 v234, s76\n"
"s_mov_b64 exec, 1\n"
"s_nop 0\n"
"global_store_dwordx2 v234, v[232:233], s[100:101] offset:2048 sc1\n"
"s_mov_b64 exec, -1\n"
"ML_SKIP2:\n"
"s_mov_b32 s12, 0\n"
"s_mov_b32 s13, 16384\n"
"s_mov_b32 s14, 65536\n"
"s_mov_b32 s15, 81920\n"
"s_mov_b32 s24, 131072\n"
"s_mov_b32 s6, 53760\n"
"s_branch TAIL\n"
".p2align 6\n"
"TAIL:\n"
"s_load_dwordx2 s[44:45], s[8:9], 0\n"
"s_load_dwordx2 s[46:47], s[8:9], 64\n"
"s_load_dwordx2 s[48:49], s[8:9], 96\n"
"s_load_dwordx2 s[52:53], s[8:9], 104\n"
"s_waitcnt vmcnt(0)\n"
"s_barrier\n"
"s_cmp_lg_u32 s11, 7\n"
"s_cbranch_scc1 TL_NOPUB\n"
"s_lshl_b32 s76, s34, 4\n"
"s_add_u32 s76, s76, s35\n"
"s_lshl_b32 s76, s76, 3\n"
"v_mov_b32 v80, s4\n"
"v_mov_b32 v81, s5\n"
"v_mov_b32 v82, s76\n"
"s_mov_b64 exec, 1\n"
"s_nop 0\n"
"global_store_dwordx2 v82, v[80:81], s[100:101] offset:-2048 sc1\n"
"s_mov_b64 exec, -1\n"
"TL_NOPUB:\n"
"s_nop 0\n"
"s_mov_b32 s66, 4096\n"
"s_mov_b32 s67, 8192\n"
"s_mov_b32 s68, 12288\n"
"v_mov_b32 v161, 0x00003f80\n"
"v_mov_b32_e64 v162, 0x3f800000\n"
"v_mov_b32 v163, 0x40088e99\n"
"s_cmp_ge_u32 s11, 4\n"
"s_cbranch_scc1 TL_LATE0\n"
"s_mul_i32 s58, s35, 5\n"
"s_add_u32 s58, s58, s11\n"
"s_cmp_ge_u32 s58, 40\n"
"s_cselect_b32 s79, 32, 0\n"
"s_add_u32 s58, s58, s79\n"
"s_branch TL_J0\n"
"TL_LATE0:\n"
"s_cmp_eq_u32 s11, 7\n"
"s_cbranch_scc1 TL_LAST0\n"
"s_sub_u32 s63, s11, 4\n"
"s_mul_i32 s58, s35, 3\n"
"s_add_u32 s58, s58, s63\n"
"s_cmp_ge_u32 s58, 24\n"
"s_cselect_b32 s79, 48, 0\n"
"s_add_u32 s58, s58, s79\n"
"s_add_u32 s58, s58, 40\n"
"s_branch TL_J0\n"
"TL_LAST0:\n"
"s_cmp_ge_u32 s35, 8\n"
"s_cselect_b32 s79, 64, 0\n"
"s_add_u32 s58, s35, s79\n"
"s_add_u32 s58, s58, 64\n"
"TL_J0:\n"
"s_waitcnt lgkmcnt(0)\n"
"s_lshr_b32 s76, s58, 1\n"
"s_cmp_ge_u32 s76, 36\n"
"s_cselect_b32 s77, 1, 0\n"
"s_mul_i32 s78, s77, 36\n"
"s_sub_u32 s76, s76, s78\n"
"s_and_b32 s78, s58, 1\n"
"v_lshrrev_b32 v96, 4, v248\n"
"v_and_b32_e64 v97, 15, v248\n"
"s_mul_i32 s79, s34, 144\n"
"s_add_u32 s79, s79, s58\n"
"s_lshl_b32 s79, s79, 14\n"
"v_lshlrev_b32 v99, 4, v248\n"
"v_add_u32 v99, s79, v99\n"
"s_lshl_b32 s77, s77, 6\n"
"s_lshl_b32 s78, s78, 5\n"
"s_add_u32 s77, s77, s78\n"
"v_add_u32 v98, s77, v97\n"
"v_lshlrev_b32 v102, 3, v98\n"
"v_add_u32 v102, s6, v102\n"
"s_lshl_b32 s76, s76, 4\n"
"v_lshlrev_b32 v103, 2, v96\n"
"v_add_u32 v103, s76, v103\n"
"s_lshl_b32 s77, s34, 7\n"
"v_add_u32_e64 v98, s77, v98\n"
"v_mul_u32_u24 v98, 576, v98\n"
"v_add_u32 v98, v98, v103\n"
"v_lshlrev_b32 v100, 1, v98\n"
"v_lshlrev_b32 v101, 2, v98\n"
"v_lshlrev_b32 v103, 1, v103\n"
"global_load_dwordx2 v[112:113], v100, s[44:45]\n"
"v_add_u32 v124, 18432, v100\n"
"global_load_dwordx2 v[114:115], v124, s[44:45]\n"
"global_load_dwordx2 v[116:117], v103, s[48:49]\n"
"global_load_dwordx2 v[118:119], v103, s[46:47]\n"
"ds_read_b64 v[120:121], v102\n"
"ds_read_b64 v[122:123], v102 offset:128\n"
"s_cmp_ge_u32 s11, 4\n"
"s_cbranch_scc1 TL_LATE\n"
"s_mov_b32 s64, s12\n"
"s_cmp_eq_u32 s11, 1\n"
"s_cselect_b32 s64, s13, s64\n"
"s_cmp_eq_u32 s11, 2\n"
"s_cselect_b32 s64, s14, s64\n"
"s_cmp_eq_u32 s11, 3\n"
"s_cselect_b32 s64, s15, s64\n"
"v_lshlrev_b32 v160, 4, v248\n"
"v_add_u32_e64 v160, s64, v160\n"
"ds_read_b128 v[0:3], v160 offset:0\n"
"ds_read_b128 v[4:7], v160 offset:1024\n"
"ds_read_b128 v[8:11], v160 offset:2048\n"
"ds_read_b128 v[12:15], v160 offset:3072\n"
"ds_read_b128 v[16:19], v160 offset:4096\n"
"ds_read_b128 v[20:23], v160 offset:5120\n"
"ds_read_b128 v[24:27], v160 offset:6144\n"
"ds_read_b128 v[28:31], v160 offset:7168\n"
"ds_read_b128 v[32:35], v160 offset:8192\n"
"ds_read_b128 v[36:39], v160 offset:9216\n"
"ds_read_b128 v[40:43], v160 offset:10240\n"
"ds_read_b128 v[44:47], v160 offset:11264\n"
"ds_read_b128 v[48:51], v160 offset:12288\n"
"ds_read_b128 v[52:55], v160 offset:13312\n"
"ds_read_b128 v[56:59], v160 offset:14336\n"
"ds_read_b128 v[60:63], v160 offset:15360\n"
"s_waitcnt lgkmcnt(15)\n"
"v_lshlrev_b32 v144, 16, v0\n"
"v_and_b32 v145, 0xffff0000, v0\n"
"v_lshlrev_b32_e64 v146, 16, v1\n"
"v_and_b32 v147, 0xffff0000, v1\n"
"v_lshlrev_b32_e64 v148, 16, v2\n"
"v_and_b32 v149, 0xffff0000, v2\n"
"v_lshlrev_b32_e64 v150, 16, v3\n"
"v_and_b32 v151, 0xffff0000, v3\n"
"s_nop 0\n"
"s_waitcnt lgkmcnt(14)\n"
"v_dot2c_f32_bf16 v144, v161, v4\n"
"v_dot2c_f32_bf16 v145, v162, v4\n"
"v_dot2c_f32_bf16 v146, v161, v5\n"
"v_dot2c_f32_bf16 v147, v162, v5\n"
"v_dot2c_f32_bf16 v148, v161, v6\n"
"v_dot2c_f32_bf16 v149, v162, v6\n"
"v_dot2c_f32_bf16 v150, v161, v7\n"
"v_dot2c_f32_bf16 v151, v162, v7\n"
"s_waitcnt lgkmcnt(13)\n"
"v_dot2c_f32_bf16 v144, v161, v8\n"
"v_dot2c_f32_bf16 v145, v162, v8\n"
"v_dot2c_f32_bf16 v146, v161, v9\n"
"v_dot2c_f32_bf16 v147, v162, v9\n"
"v_dot2c_f32_bf16 v148, v161, v10\n"
"v_dot2c_f32_bf16 v149, v162, v10\n"
"v_dot2c_f32_bf16 v150, v161, v11\n"
"v_dot2c_f32_bf16 v151, v162, v11\n"
"s_waitcnt lgkmcnt(12)\n"
"v_dot2c_f32_bf16 v144, v161, v12\n"
"v_dot2c_f32_bf16 v145, v162, v12\n"
"v_dot2c_f32_bf16 v146, v161, v13\n"
"v_dot2c_f32_bf16 v147, v162, v13\n"
"v_dot2c_f32_bf16 v148, v161, v14\n"
"v_dot2c_f32_bf16 v149, v162, v14\n"
"v_dot2c_f32_bf16 v150, v161, v15\n"
"v_dot2c_f32_bf16 v151, v162, v15\n"
"s_waitcnt lgkmcnt(11)\n"
"v_dot2c_f32_bf16 v144, v161, v16\n"
"v_dot2c_f32_bf16 v145, v162, v16\n"
"v_dot2c_f32_bf16 v146, v161, v17\n"
"v_dot2c_f32_bf16 v147, v162, v17\n"
"v_dot2c_f32_bf16 v148, v161, v18\n"
"v_dot2c_f32_bf16 v149, v162, v18\n"
"v_dot2c_f32_bf16 v150, v161, v19\n"
"v_dot2c_f32_bf16 v151, v162, v19\n"
"s_waitcnt lgkmcnt(10)\n"
"v_dot2c_f32_bf16 v144, v161, v20\n"
"v_dot2c_f32_bf16 v145, v162, v20\n"
"v_dot2c_f32_bf16 v146, v161, v21\n"
"v_dot2c_f32_bf16 v147, v162, v21\n"
"v_dot2c_f32_bf16 v148, v161, v22\n"
"v_dot2c_f32_bf16 v149, v162, v22\n"
"v_dot2c_f32_bf16 v150, v161, v23\n"
"v_dot2c_f32_bf16 v151, v162, v23\n"
"s_waitcnt lgkmcnt(9)\n"
"v_dot2c_f32_bf16 v144, v161, v24\n"
"v_dot2c_f32_bf16 v145, v162, v24\n"
"v_dot2c_f32_bf16 v146, v161, v25\n"
"v_dot2c_f32_bf16 v147, v162, v25\n"
"v_dot2c_f32_bf16 v148, v161, v26\n"
"v_dot2c_f32_bf16 v149, v162, v26\n"
"v_dot2c_f32_bf16 v150, v161, v27\n"
"v_dot2c_f32_bf16 v151, v162, v27\n"
"s_waitcnt lgkmcnt(8)\n"
"v_dot2c_f32_bf16 v144, v161, v28\n"
"v_dot2c_f32_bf16 v145, v162, v28\n"
"v_dot2c_f32_bf16 v146, v161, v29\n"
"v_dot2c_f32_bf16 v147, v162, v29\n"
"v_dot2c_f32_bf16 v148, v161, v30\n"
"v_dot2c_f32_bf16 v149, v162, v30\n"
"v_dot2c_f32_bf16 v150, v161, v31\n"
"v_dot2c_f32_bf16 v151, v162, v31\n"
"s_waitcnt lgkmcnt(7)\n"
"v_dot2c_f32_bf16 v144, v161, v32\n"
"v_dot2c_f32_bf16 v145, v162, v32\n"
"v_dot2c_f32_bf16 v146, v161, v33\n"
"v_dot2c_f32_bf16 v147, v162, v33\n"
"v_dot2c_f32_bf16 v148, v161, v34\n"
"v_dot2c_f32_bf16 v149, v162, v34\n"
"v_dot2c_f32_bf16 v150, v161, v35\n"
"v_dot2c_f32_bf16 v151, v162, v35\n"
"s_waitcnt lgkmcnt(6)\n"
"v_dot2c_f32_bf16 v144, v161, v36\n"
"v_dot2c_f32_bf16 v145, v162, v36\n"
"v_dot2c_f32_bf16 v146, v161, v37\n"
"v_dot2c_f32_bf16 v147, v162, v37\n"
"v_dot2c_f32_bf16 v148, v161, v38\n"
"v_dot2c_f32_bf16 v149, v162, v38\n"
"v_dot2c_f32_bf16 v150, v161, v39\n"
"v_dot2c_f32_bf16 v151, v162, v39\n"
"s_waitcnt lgkmcnt(5)\n"
"v_dot2c_f32_bf16 v144, v161, v40\n"
"v_dot2c_f32_bf16 v145, v162, v40\n"
"v_dot2c_f32_bf16 v146, v161, v41\n"
"v_dot2c_f32_bf16 v147, v162, v41\n"
"v_dot2c_f32_bf16 v148, v161, v42\n"
"v_dot2c_f32_bf16 v149, v162, v42\n"
"v_dot2c_f32_bf16 v150, v161, v43\n"
"v_dot2c_f32_bf16 v151, v162, v43\n"
"s_waitcnt lgkmcnt(4)\n"
"v_dot2c_f32_bf16 v144, v161, v44\n"
"v_dot2c_f32_bf16 v145, v162, v44\n"
"v_dot2c_f32_bf16 v146, v161, v45\n"
"v_dot2c_f32_bf16 v147, v162, v45\n"
"v_dot2c_f32_bf16 v148, v161, v46\n"
"v_dot2c_f32_bf16 v149, v162, v46\n"
"v_dot2c_f32_bf16 v150, v161, v47\n"
"v_dot2c_f32_bf16 v151, v162, v47\n"
"s_waitcnt lgkmcnt(3)\n"
"v_lshlrev_b32 v152, 16, v48\n"
"v_and_b32 v153, 0xffff0000, v48\n"
"v_lshlrev_b32_e64 v154, 16, v49\n"
"v_and_b32 v155, 0xffff0000, v49\n"
"v_lshlrev_b32_e64 v156, 16, v50\n"
"v_and_b32 v157, 0xffff0000, v50\n"
"v_lshlrev_b32_e64 v158, 16, v51\n"
"v_and_b32 v159, 0xffff0000, v51\n"
"s_nop 0\n"
"s_waitcnt lgkmcnt(2)\n"
"v_dot2c_f32_bf16 v152, v161, v52\n"
"v_dot2c_f32_bf16 v153, v162, v52\n"
"v_dot2c_f32_bf16 v154, v161, v53\n"
"v_dot2c_f32_bf16 v155, v162, v53\n"
"v_dot2c_f32_bf16 v156, v161, v54\n"
"v_dot2c_f32_bf16 v157, v162, v54\n"
"v_dot2c_f32_bf16 v158, v161, v55\n"
"v_dot2c_f32_bf16 v159, v162, v55\n"
"s_waitcnt lgkmcnt(1)\n"
"v_dot2c_f32_bf16 v152, v161, v56\n"
"v_dot2c_f32_bf16 v153, v162, v56\n"
"v_dot2c_f32_bf16 v154, v161, v57\n"
"v_dot2c_f32_bf16 v155, v162, v57\n"
"v_dot2c_f32_bf16 v156, v161, v58\n"
"v_dot2c_f32_bf16 v157, v162, v58\n"
"v_dot2c_f32_bf16 v158, v161, v59\n"
"v_dot2c_f32_bf16 v159, v162, v59\n"
"s_waitcnt lgkmcnt(0)\n"
"v_dot2c_f32_bf16 v152, v161, v60\n"
"v_dot2c_f32_bf16 v153, v162, v60\n"
"v_dot2c_f32_bf16 v154, v161, v61\n"
"v_dot2c_f32_bf16 v155, v162, v61\n"
"v_dot2c_f32_bf16 v156, v161, v62\n"
"v_dot2c_f32_bf16 v157, v162, v62\n"
"v_dot2c_f32_bf16 v158, v161, v63\n"
"v_dot2c_f32_bf16 v159, v162, v63\n"
"s_waitcnt vmcnt(0)\n"
"v_lshlrev_b32 v164, 16, v116\n"
"v_and_b32 v165, 0xffff0000, v116\n"
"v_lshlrev_b32_e64 v166, 16, v117\n"
"v_and_b32 v167, 0xffff0000, v117\n"
"v_lshlrev_b32_e64 v168, 16, v118\n"
"v_and_b32 v169, 0xffff0000, v118\n"
"v_lshlrev_b32_e64 v170, 16, v119\n"
"v_and_b32 v171, 0xffff0000, v119\n"
"v_lshlrev_b32_e64 v172, 16, v112\n"
"v_and_b32 v173, 0xffff0000, v112\n"
"v_lshlrev_b32_e64 v174, 16, v113\n"
"v_and_b32 v175, 0xffff0000, v113\n"
"v_lshlrev_b32_e64 v176, 16, v114\n"
"v_and_b32 v177, 0xffff0000, v114\n"
"v_lshlrev_b32_e64 v178, 16, v115\n"
"v_and_b32 v179, 0xffff0000, v115\n"
"v_fma_f32 v180, v163, v144, v164\n"
"v_fma_f32 v181, v163, v145, v165\n"
"v_fma_f32 v182, v163, v146, v166\n"
"v_fma_f32 v183, v163, v147, v167\n"
"v_fma_f32 v184, v163, v148, v164\n"
"v_fma_f32 v185, v163, v149, v165\n"
"v_fma_f32 v186, v163, v150, v166\n"
"v_fma_f32 v187, v163, v151, v167\n"
"v_mul_f32 v188, 0xbfb8aa3b, v180\n"
"v_mul_f32 v189, 0xbfb8aa3b, v181\n"
"v_mul_f32 v190, 0xbfb8aa3b, v182\n"
"v_mul_f32 v191, 0xbfb8aa3b, v183\n"
"v_mul_f32 v192, 0xbfb8aa3b, v184\n"
"v_mul_f32 v193, 0xbfb8aa3b, v185\n"
"v_mul_f32 v194, 0xbfb8aa3b, v186\n"
"v_mul_f32 v195, 0xbfb8aa3b, v187\n"
"v_exp_f32 v188, v188\n"
"v_exp_f32 v189, v189\n"
"v_exp_f32 v190, v190\n"
"v_exp_f32 v191, v191\n"
"v_exp_f32 v192, v192\n"
"v_exp_f32 v193, v193\n"
"v_exp_f32 v194, v194\n"
"v_exp_f32 v195, v195\n"
"v_sub_f32 v196, v172, v120\n"
"v_sub_f32 v197, v173, v120\n"
"v_sub_f32 v198, v174, v120\n"
"v_sub_f32 v199, v175, v120\n"
"v_sub_f32 v200, v176, v122\n"
"v_sub_f32 v201, v177, v122\n"
"v_sub_f32 v202, v178, v122\n"
"v_sub_f32 v203, v179, v122\n"
"v_add_f32 v188, 1.0, v188\n"
"v_add_f32 v189, 1.0, v189\n"
"v_add_f32 v190, 1.0, v190\n"
"v_add_f32 v191, 1.0, v191\n"
"v_add_f32 v192, 1.0, v192\n"
"v_add_f32 v193, 1.0, v193\n"
"v_add_f32 v194, 1.0, v194\n"
"v_add_f32 v195, 1.0, v195\n"
"v_mul_f32 v196, v196, v121\n"
"v_mul_f32 v197, v197, v121\n"
"v_mul_f32 v198, v198, v121\n"
"v_mul_f32 v199, v199, v121\n"
"v_mul_f32 v200, v200, v123\n"
"v_mul_f32 v201, v201, v123\n"
"v_mul_f32 v202, v202, v123\n"
"v_mul_f32 v203, v203, v123\n"
"v_rcp_f32 v188, v188\n"
"v_rcp_f32 v189, v189\n"
"v_rcp_f32 v190, v190\n"
"v_rcp_f32 v191, v191\n"
"v_rcp_f32 v192, v192\n"
"v_rcp_f32 v193, v193\n"
"v_rcp_f32 v194, v194\n"
"v_rcp_f32 v195, v195\n"
"v_fmac_f32 v152, v196, v168\n"
"v_fmac_f32 v153, v197, v169\n"
"v_fmac_f32 v154, v198, v170\n"
"v_fmac_f32 v155, v199, v171\n"
"v_fmac_f32 v156, v200, v168\n"
"v_fmac_f32 v157, v201, v169\n"
"v_fmac_f32 v158, v202, v170\n"
"v_fmac_f32 v159, v203, v171\n"
"v_mul_f32 v180, v180, v188\n"
"v_mul_f32 v181, v181, v189\n"
"v_mul_f32 v182, v182, v190\n"
"v_mul_f32 v183, v183, v191\n"
"v_mul_f32 v184, v184, v192\n"
"v_mul_f32 v185, v185, v193\n"
"v_mul_f32 v186, v186, v194\n"
"v_mul_f32 v187, v187, v195\n"
"v_fmac_f32 v172, v152, v180\n"
"v_fmac_f32 v173, v153, v181\n"
"v_fmac_f32 v174, v154, v182\n"
"v_fmac_f32 v175, v155, v183\n"
"v_fmac_f32 v176, v156, v184\n"
"v_fmac_f32 v177, v157, v185\n"
"v_fmac_f32 v178, v158, v186\n"
"v_fmac_f32 v179, v159, v187\n"
"global_store_dwordx4 v101, v[172:175], s[52:53]\n"
"v_add_u32 v101, 36864, v101\n"
"global_store_dwordx4 v101, v[176:179], s[52:53]\n"
"s_cmp_lg_u32 s11, 1\n"
"s_cbranch_scc1 TL_END\n"
"s_mul_i32 s58, s35, 5\n"
"s_add_u32 s58, s58, 4\n"
"s_cmp_ge_u32 s58, 40\n"
"s_cselect_b32 s79, 32, 0\n"
"s_add_u32 s58, s58, s79\n"
"s_lshr_b32 s76, s58, 1\n"
"s_cmp_ge_u32 s76, 36\n"
"s_cselect_b32 s77, 1, 0\n"
"s_mul_i32 s78, s77, 36\n"
"s_sub_u32 s76, s76, s78\n"
"s_and_b32 s78, s58, 1\n"
"v_lshrrev_b32 v96, 4, v248\n"
"v_and_b32_e64 v97, 15, v248\n"
"s_mul_i32 s79, s34, 144\n"
"s_add_u32 s79, s79, s58\n"
"s_lshl_b32 s79, s79, 14\n"
"v_lshlrev_b32 v99, 4, v248\n"
"v_add_u32 v99, s79, v99\n"
"s_lshl_b32 s77, s77, 6\n"
"s_lshl_b32 s78, s78, 5\n"
"s_add_u32 s77, s77, s78\n"
"v_add_u32 v98, s77, v97\n"
"v_lshlrev_b32 v102, 3, v98\n"
"v_add_u32 v102, s6, v102\n"
"s_lshl_b32 s76, s76, 4\n"
"v_lshlrev_b32 v103, 2, v96\n"
"v_add_u32 v103, s76, v103\n"
"s_lshl_b32 s77, s34, 7\n"
"v_add_u32_e64 v98, s77, v98\n"
"v_mul_u32_u24 v98, 576, v98\n"
"v_add_u32 v98, v98, v103\n"
"v_lshlrev_b32 v100, 1, v98\n"
"v_lshlrev_b32 v101, 2, v98\n"
"v_lshlrev_b32 v103, 1, v103\n"
"global_load_dwordx2 v[112:113], v100, s[44:45]\n"
"v_add_u32 v124, 18432, v100\n"
"global_load_dwordx2 v[114:115], v124, s[44:45]\n"
"global_load_dwordx2 v[116:117], v103, s[48:49]\n"
"global_load_dwordx2 v[118:119], v103, s[46:47]\n"
"ds_read_b64 v[120:121], v102\n"
"ds_read_b64 v[122:123], v102 offset:128\n"
"v_lshlrev_b32 v160, 4, v248\n"
"v_add_u32 v160, s24, v160\n"
"ds_read_b128 v[0:3], v160 offset:0\n"
"ds_read_b128 v[4:7], v160 offset:1024\n"
"ds_read_b128 v[8:11], v160 offset:2048\n"
"ds_read_b128 v[12:15], v160 offset:3072\n"
"ds_read_b128 v[16:19], v160 offset:4096\n"
"ds_read_b128 v[20:23], v160 offset:5120\n"
"ds_read_b128 v[24:27], v160 offset:6144\n"
"ds_read_b128 v[28:31], v160 offset:7168\n"
"ds_read_b128 v[32:35], v160 offset:8192\n"
"ds_read_b128 v[36:39], v160 offset:9216\n"
"ds_read_b128 v[40:43], v160 offset:10240\n"
"ds_read_b128 v[44:47], v160 offset:11264\n"
"ds_read_b128 v[48:51], v160 offset:12288\n"
"ds_read_b128 v[52:55], v160 offset:13312\n"
"ds_read_b128 v[56:59], v160 offset:14336\n"
"ds_read_b128 v[60:63], v160 offset:15360\n"
"s_waitcnt lgkmcnt(15)\n"
"v_lshlrev_b32 v144, 16, v0\n"
"v_and_b32 v145, 0xffff0000, v0\n"
"v_lshlrev_b32_e64 v146, 16, v1\n"
"v_and_b32 v147, 0xffff0000, v1\n"
"v_lshlrev_b32_e64 v148, 16, v2\n"
"v_and_b32 v149, 0xffff0000, v2\n"
"v_lshlrev_b32_e64 v150, 16, v3\n"
"v_and_b32 v151, 0xffff0000, v3\n"
"s_nop 0\n"
"s_waitcnt lgkmcnt(14)\n"
"v_dot2c_f32_bf16 v144, v161, v4\n"
"v_dot2c_f32_bf16 v145, v162, v4\n"
"v_dot2c_f32_bf16 v146, v161, v5\n"
"v_dot2c_f32_bf16 v147, v162, v5\n"
"v_dot2c_f32_bf16 v148, v161, v6\n"
"v_dot2c_f32_bf16 v149, v162, v6\n"
"v_dot2c_f32_bf16 v150, v161, v7\n"
"v_dot2c_f32_bf16 v151, v162, v7\n"
"s_waitcnt lgkmcnt(13)\n"
"v_dot2c_f32_bf16 v144, v161, v8\n"
"v_dot2c_f32_bf16 v145, v162, v8\n"
"v_dot2c_f32_bf16 v146, v161, v9\n"
"v_dot2c_f32_bf16 v147, v162, v9\n"
"v_dot2c_f32_bf16 v148, v161, v10\n"
"v_dot2c_f32_bf16 v149, v162, v10\n"
"v_dot2c_f32_bf16 v150, v161, v11\n"
"v_dot2c_f32_bf16 v151, v162, v11\n"
"s_waitcnt lgkmcnt(12)\n"
"v_dot2c_f32_bf16 v144, v161, v12\n"
"v_dot2c_f32_bf16 v145, v162, v12\n"
"v_dot2c_f32_bf16 v146, v161, v13\n"
"v_dot2c_f32_bf16 v147, v162, v13\n"
"v_dot2c_f32_bf16 v148, v161, v14\n"
"v_dot2c_f32_bf16 v149, v162, v14\n"
"v_dot2c_f32_bf16 v150, v161, v15\n"
"v_dot2c_f32_bf16 v151, v162, v15\n"
"s_waitcnt lgkmcnt(11)\n"
"v_dot2c_f32_bf16 v144, v161, v16\n"
"v_dot2c_f32_bf16 v145, v162, v16\n"
"v_dot2c_f32_bf16 v146, v161, v17\n"
"v_dot2c_f32_bf16 v147, v162, v17\n"
"v_dot2c_f32_bf16 v148, v161, v18\n"
"v_dot2c_f32_bf16 v149, v162, v18\n"
"v_dot2c_f32_bf16 v150, v161, v19\n"
"v_dot2c_f32_bf16 v151, v162, v19\n"
"s_waitcnt lgkmcnt(10)\n"
"v_dot2c_f32_bf16 v144, v161, v20\n"
"v_dot2c_f32_bf16 v145, v162, v20\n"
"v_dot2c_f32_bf16 v146, v161, v21\n"
"v_dot2c_f32_bf16 v147, v162, v21\n"
"v_dot2c_f32_bf16 v148, v161, v22\n"
"v_dot2c_f32_bf16 v149, v162, v22\n"
"v_dot2c_f32_bf16 v150, v161, v23\n"
"v_dot2c_f32_bf16 v151, v162, v23\n"
"s_waitcnt lgkmcnt(9)\n"
"v_dot2c_f32_bf16 v144, v161, v24\n"
"v_dot2c_f32_bf16 v145, v162, v24\n"
"v_dot2c_f32_bf16 v146, v161, v25\n"
"v_dot2c_f32_bf16 v147, v162, v25\n"
"v_dot2c_f32_bf16 v148, v161, v26\n"
"v_dot2c_f32_bf16 v149, v162, v26\n"
"v_dot2c_f32_bf16 v150, v161, v27\n"
"v_dot2c_f32_bf16 v151, v162, v27\n"
"s_waitcnt lgkmcnt(8)\n"
"v_dot2c_f32_bf16 v144, v161, v28\n"
"v_dot2c_f32_bf16 v145, v162, v28\n"
"v_dot2c_f32_bf16 v146, v161, v29\n"
"v_dot2c_f32_bf16 v147, v162, v29\n"
"v_dot2c_f32_bf16 v148, v161, v30\n"
"v_dot2c_f32_bf16 v149, v162, v30\n"
"v_dot2c_f32_bf16 v150, v161, v31\n"
"v_dot2c_f32_bf16 v151, v162, v31\n"
"s_waitcnt lgkmcnt(7)\n"
"v_dot2c_f32_bf16 v144, v161, v32\n"
"v_dot2c_f32_bf16 v145, v162, v32\n"
"v_dot2c_f32_bf16 v146, v161, v33\n"
"v_dot2c_f32_bf16 v147, v162, v33\n"
"v_dot2c_f32_bf16 v148, v161, v34\n"
"v_dot2c_f32_bf16 v149, v162, v34\n"
"v_dot2c_f32_bf16 v150, v161, v35\n"
"v_dot2c_f32_bf16 v151, v162, v35\n"
"s_waitcnt lgkmcnt(6)\n"
"v_dot2c_f32_bf16 v144, v161, v36\n"
"v_dot2c_f32_bf16 v145, v162, v36\n"
"v_dot2c_f32_bf16 v146, v161, v37\n"
"v_dot2c_f32_bf16 v147, v162, v37\n"
"v_dot2c_f32_bf16 v148, v161, v38\n"
"v_dot2c_f32_bf16 v149, v162, v38\n"
"v_dot2c_f32_bf16 v150, v161, v39\n"
"v_dot2c_f32_bf16 v151, v162, v39\n"
"s_waitcnt lgkmcnt(5)\n"
"v_dot2c_f32_bf16 v144, v161, v40\n"
"v_dot2c_f32_bf16 v145, v162, v40\n"
"v_dot2c_f32_bf16 v146, v161, v41\n"
"v_dot2c_f32_bf16 v147, v162, v41\n"
"v_dot2c_f32_bf16 v148, v161, v42\n"
"v_dot2c_f32_bf16 v149, v162, v42\n"
"v_dot2c_f32_bf16 v150, v161, v43\n"
"v_dot2c_f32_bf16 v151, v162, v43\n"
"s_waitcnt lgkmcnt(4)\n"
"v_dot2c_f32_bf16 v144, v161, v44\n"
"v_dot2c_f32_bf16 v145, v162, v44\n"
"v_dot2c_f32_bf16 v146, v161, v45\n"
"v_dot2c_f32_bf16 v147, v162, v45\n"
"v_dot2c_f32_bf16 v148, v161, v46\n"
"v_dot2c_f32_bf16 v149, v162, v46\n"
"v_dot2c_f32_bf16 v150, v161, v47\n"
"v_dot2c_f32_bf16 v151, v162, v47\n"
"s_waitcnt lgkmcnt(3)\n"
"v_lshlrev_b32 v152, 16, v48\n"
"v_and_b32 v153, 0xffff0000, v48\n"
"v_lshlrev_b32_e64 v154, 16, v49\n"
"v_and_b32 v155, 0xffff0000, v49\n"
"v_lshlrev_b32_e64 v156, 16, v50\n"
"v_and_b32 v157, 0xffff0000, v50\n"
"v_lshlrev_b32_e64 v158, 16, v51\n"
"v_and_b32 v159, 0xffff0000, v51\n"
"s_nop 0\n"
"s_waitcnt lgkmcnt(2)\n"
"v_dot2c_f32_bf16 v152, v161, v52\n"
"v_dot2c_f32_bf16 v153, v162, v52\n"
"v_dot2c_f32_bf16 v154, v161, v53\n"
"v_dot2c_f32_bf16 v155, v162, v53\n"
"v_dot2c_f32_bf16 v156, v161, v54\n"
"v_dot2c_f32_bf16 v157, v162, v54\n"
"v_dot2c_f32_bf16 v158, v161, v55\n"
"v_dot2c_f32_bf16 v159, v162, v55\n"
"s_waitcnt lgkmcnt(1)\n"
"v_dot2c_f32_bf16 v152, v161, v56\n"
"v_dot2c_f32_bf16 v153, v162, v56\n"
"v_dot2c_f32_bf16 v154, v161, v57\n"
"v_dot2c_f32_bf16 v155, v162, v57\n"
"v_dot2c_f32_bf16 v156, v161, v58\n"
"v_dot2c_f32_bf16 v157, v162, v58\n"
"v_dot2c_f32_bf16 v158, v161, v59\n"
"v_dot2c_f32_bf16 v159, v162, v59\n"
"s_waitcnt lgkmcnt(0)\n"
"v_dot2c_f32_bf16 v152, v161, v60\n"
"v_dot2c_f32_bf16 v153, v162, v60\n"
"v_dot2c_f32_bf16 v154, v161, v61\n"
"v_dot2c_f32_bf16 v155, v162, v61\n"
"v_dot2c_f32_bf16 v156, v161, v62\n"
"v_dot2c_f32_bf16 v157, v162, v62\n"
"v_dot2c_f32_bf16 v158, v161, v63\n"
"v_dot2c_f32_bf16 v159, v162, v63\n"
"s_waitcnt vmcnt(0)\n"
"v_lshlrev_b32 v164, 16, v116\n"
"v_and_b32 v165, 0xffff0000, v116\n"
"v_lshlrev_b32_e64 v166, 16, v117\n"
"v_and_b32 v167, 0xffff0000, v117\n"
"v_lshlrev_b32_e64 v168, 16, v118\n"
"v_and_b32 v169, 0xffff0000, v118\n"
"v_lshlrev_b32_e64 v170, 16, v119\n"
"v_and_b32 v171, 0xffff0000, v119\n"
"v_lshlrev_b32_e64 v172, 16, v112\n"
"v_and_b32 v173, 0xffff0000, v112\n"
"v_lshlrev_b32_e64 v174, 16, v113\n"
"v_and_b32 v175, 0xffff0000, v113\n"
"v_lshlrev_b32_e64 v176, 16, v114\n"
"v_and_b32 v177, 0xffff0000, v114\n"
"v_lshlrev_b32_e64 v178, 16, v115\n"
"v_and_b32 v179, 0xffff0000, v115\n"
"v_fma_f32 v180, v163, v144, v164\n"
"v_fma_f32 v181, v163, v145, v165\n"
"v_fma_f32 v182, v163, v146, v166\n"
"v_fma_f32 v183, v163, v147, v167\n"
"v_fma_f32 v184, v163, v148, v164\n"
"v_fma_f32 v185, v163, v149, v165\n"
"v_fma_f32 v186, v163, v150, v166\n"
"v_fma_f32 v187, v163, v151, v167\n"
"v_mul_f32 v188, 0xbfb8aa3b, v180\n"
"v_mul_f32 v189, 0xbfb8aa3b, v181\n"
"v_mul_f32 v190, 0xbfb8aa3b, v182\n"
"v_mul_f32 v191, 0xbfb8aa3b, v183\n"
"v_mul_f32 v192, 0xbfb8aa3b, v184\n"
"v_mul_f32 v193, 0xbfb8aa3b, v185\n"
"v_mul_f32 v194, 0xbfb8aa3b, v186\n"
"v_mul_f32 v195, 0xbfb8aa3b, v187\n"
"v_exp_f32 v188, v188\n"
"v_exp_f32 v189, v189\n"
"v_exp_f32 v190, v190\n"
"v_exp_f32 v191, v191\n"
"v_exp_f32 v192, v192\n"
"v_exp_f32 v193, v193\n"
"v_exp_f32 v194, v194\n"
"v_exp_f32 v195, v195\n"
"v_sub_f32 v196, v172, v120\n"
"v_sub_f32 v197, v173, v120\n"
"v_sub_f32 v198, v174, v120\n"
"v_sub_f32 v199, v175, v120\n"
"v_sub_f32 v200, v176, v122\n"
"v_sub_f32 v201, v177, v122\n"
"v_sub_f32 v202, v178, v122\n"
"v_sub_f32 v203, v179, v122\n"
"v_add_f32 v188, 1.0, v188\n"
"v_add_f32 v189, 1.0, v189\n"
"v_add_f32 v190, 1.0, v190\n"
"v_add_f32 v191, 1.0, v191\n"
"v_add_f32 v192, 1.0, v192\n"
"v_add_f32 v193, 1.0, v193\n"
"v_add_f32 v194, 1.0, v194\n"
"v_add_f32 v195, 1.0, v195\n"
"v_mul_f32 v196, v196, v121\n"
"v_mul_f32 v197, v197, v121\n"
"v_mul_f32 v198, v198, v121\n"
"v_mul_f32 v199, v199, v121\n"
"v_mul_f32 v200, v200, v123\n"
"v_mul_f32 v201, v201, v123\n"
"v_mul_f32 v202, v202, v123\n"
"v_mul_f32 v203, v203, v123\n"
"v_rcp_f32 v188, v188\n"
"v_rcp_f32 v189, v189\n"
"v_rcp_f32 v190, v190\n"
"v_rcp_f32 v191, v191\n"
"v_rcp_f32 v192, v192\n"
"v_rcp_f32 v193, v193\n"
"v_rcp_f32 v194, v194\n"
"v_rcp_f32 v195, v195\n"
"v_fmac_f32 v152, v196, v168\n"
"v_fmac_f32 v153, v197, v169\n"
"v_fmac_f32 v154, v198, v170\n"
"v_fmac_f32 v155, v199, v171\n"
"v_fmac_f32 v156, v200, v168\n"
"v_fmac_f32 v157, v201, v169\n"
"v_fmac_f32 v158, v202, v170\n"
"v_fmac_f32 v159, v203, v171\n"
"v_mul_f32 v180, v180, v188\n"
"v_mul_f32 v181, v181, v189\n"
"v_mul_f32 v182, v182, v190\n"
"v_mul_f32 v183, v183, v191\n"
"v_mul_f32 v184, v184, v192\n"
"v_mul_f32 v185, v185, v193\n"
"v_mul_f32 v186, v186, v194\n"
"v_mul_f32 v187, v187, v195\n"
"v_fmac_f32 v172, v152, v180\n"
"v_fmac_f32 v173, v153, v181\n"
"v_fmac_f32 v174, v154, v182\n"
"v_fmac_f32 v175, v155, v183\n"
"v_fmac_f32 v176, v156, v184\n"
"v_fmac_f32 v177, v157, v185\n"
"v_fmac_f32 v178, v158, v186\n"
"v_fmac_f32 v179, v159, v187\n"
"global_store_dwordx4 v101, v[172:175], s[52:53]\n"
"v_add_u32 v101, 36864, v101\n"
"global_store_dwordx4 v101, v[176:179], s[52:53]\n"
"s_branch TL_END\n"
"TL_LATE:\n"
"s_cmp_eq_u32 s11, 7\n"
"s_cbranch_scc1 TL_P7\n"
"v_and_b32 v82, 15, v248\n"
"v_lshlrev_b32 v82, 3, v82\n"
"s_lshl_b32 s76, s34, 7\n"
"v_add_u32 v82, s76, v82\n"
"s_mov_b32 s63, 0\n"
"TL_POLLM:\n"
"global_load_dwordx2 v[80:81], v82, s[100:101] offset:2048 sc1\n"
"s_waitcnt vmcnt(0)\n"
"v_cmp_ne_u32 vcc, s4, v80\n"
"v_cmp_ne_u32 s[76:77], s5, v81\n"
"s_nop 3\n"
"s_or_b64 vcc, vcc, s[76:77]\n"
"s_cmp_eq_u64 vcc, 0\n"
"s_cbranch_scc1 TL_POLLM_OK\n"
"s_add_u32 s63, s63, 1\n"
"s_nop 0\n"
"s_cmp_gt_u32 s63, 400000\n"
"s_cbranch_scc1 TL_POLLM_OK\n"
"s_sleep 2\n"
"s_branch TL_POLLM\n"
"TL_POLLM_OK:\n"
"s_branch TL_LOADS\n"
"TL_P7:\n"
"v_and_b32 v82, 15, v248\n"
"v_lshlrev_b32 v82, 3, v82\n"
"s_lshl_b32 s76, s34, 7\n"
"v_add_u32 v82, s76, v82\n"
"s_mov_b32 s63, 0\n"
"TL_POLLF:\n"
"s_nop 0\n"
"global_load_dwordx2 v[80:81], v82, s[100:101] offset:-2048 sc1\n"
"s_waitcnt vmcnt(0)\n"
"v_cmp_ne_u32 vcc, s4, v80\n"
"v_cmp_ne_u32 s[76:77], s5, v81\n"
"s_nop 3\n"
"s_or_b64 vcc, vcc, s[76:77]\n"
"s_cmp_eq_u64 vcc, 0\n"
"s_cbranch_scc1 TL_POLLF_OK\n"
"s_add_u32 s63, s63, 1\n"
"s_nop 0\n"
"s_cmp_gt_u32 s63, 400000\n"
"s_cbranch_scc1 TL_POLLF_OK\n"
"s_sleep 2\n"
"s_branch TL_POLLF\n"
"TL_POLLF_OK:\n"
"TL_LOADS:\n"
"s_nop 0\n"
"buffer_load_dwordx4 v[0:3], v99, s[20:23], 0 offen offset:0 sc1\n"
"buffer_load_dwordx4 v[4:7], v99, s[20:23], 0 offen offset:1024 sc1\n"
"buffer_load_dwordx4 v[8:11], v99, s[20:23], 0 offen offset:2048 sc1\n"
"buffer_load_dwordx4 v[12:15], v99, s[20:23], 0 offen offset:3072 sc1\n"
"buffer_load_dwordx4 v[16:19], v99, s[20:23], s66 offen offset:0 sc1\n"
"buffer_load_dwordx4 v[20:23], v99, s[20:23], s66 offen offset:1024 sc1\n"
"buffer_load_dwordx4 v[24:27], v99, s[20:23], s66 offen offset:2048 sc1\n"
"buffer_load_dwordx4 v[28:31], v99, s[20:23], s66 offen offset:3072 sc1\n"
"buffer_load_dwordx4 v[32:35], v99, s[20:23], s67 offen offset:0 sc1\n"
"buffer_load_dwordx4 v[36:39], v99, s[20:23], s67 offen offset:1024 sc1\n"
"buffer_load_dwordx4 v[40:43], v99, s[20:23], s67 offen offset:2048 sc1\n"
"buffer_load_dwordx4 v[44:47], v99, s[20:23], s67 offen offset:3072 sc1\n"
"buffer_load_dwordx4 v[48:51], v99, s[20:23], s68 offen offset:0 sc1\n"
"buffer_load_dwordx4 v[52:55], v99, s[20:23], s68 offen offset:1024 sc1\n"
"buffer_load_dwordx4 v[56:59], v99, s[20:23], s68 offen offset:2048 sc1\n"
"buffer_load_dwordx4 v[60:63], v99, s[20:23], s68 offen offset:3072 sc1\n"
"s_waitcnt vmcnt(15)\n"
"v_lshlrev_b32 v144, 16, v0\n"
"v_and_b32 v145, 0xffff0000, v0\n"
"v_lshlrev_b32_e64 v146, 16, v1\n"
"v_and_b32 v147, 0xffff0000, v1\n"
"v_lshlrev_b32_e64 v148, 16, v2\n"
"v_and_b32 v149, 0xffff0000, v2\n"
"v_lshlrev_b32_e64 v150, 16, v3\n"
"v_and_b32 v151, 0xffff0000, v3\n"
"s_nop 0\n"
"s_waitcnt vmcnt(14)\n"
"v_dot2c_f32_bf16 v144, v161, v4\n"
"v_dot2c_f32_bf16 v145, v162, v4\n"
"v_dot2c_f32_bf16 v146, v161, v5\n"
"v_dot2c_f32_bf16 v147, v162, v5\n"
"v_dot2c_f32_bf16 v148, v161, v6\n"
"v_dot2c_f32_bf16 v149, v162, v6\n"
"v_dot2c_f32_bf16 v150, v161, v7\n"
"v_dot2c_f32_bf16 v151, v162, v7\n"
"s_waitcnt vmcnt(13)\n"
"v_dot2c_f32_bf16 v144, v161, v8\n"
"v_dot2c_f32_bf16 v145, v162, v8\n"
"v_dot2c_f32_bf16 v146, v161, v9\n"
"v_dot2c_f32_bf16 v147, v162, v9\n"
"v_dot2c_f32_bf16 v148, v161, v10\n"
"v_dot2c_f32_bf16 v149, v162, v10\n"
"v_dot2c_f32_bf16 v150, v161, v11\n"
"v_dot2c_f32_bf16 v151, v162, v11\n"
"s_waitcnt vmcnt(12)\n"
"v_dot2c_f32_bf16 v144, v161, v12\n"
"v_dot2c_f32_bf16 v145, v162, v12\n"
"v_dot2c_f32_bf16 v146, v161, v13\n"
"v_dot2c_f32_bf16 v147, v162, v13\n"
"v_dot2c_f32_bf16 v148, v161, v14\n"
"v_dot2c_f32_bf16 v149, v162, v14\n"
"v_dot2c_f32_bf16 v150, v161, v15\n"
"v_dot2c_f32_bf16 v151, v162, v15\n"
"s_waitcnt vmcnt(11)\n"
"v_dot2c_f32_bf16 v144, v161, v16\n"
"v_dot2c_f32_bf16 v145, v162, v16\n"
"v_dot2c_f32_bf16 v146, v161, v17\n"
"v_dot2c_f32_bf16 v147, v162, v17\n"
"v_dot2c_f32_bf16 v148, v161, v18\n"
"v_dot2c_f32_bf16 v149, v162, v18\n"
"v_dot2c_f32_bf16 v150, v161, v19\n"
"v_dot2c_f32_bf16 v151, v162, v19\n"
"s_waitcnt vmcnt(10)\n"
"v_dot2c_f32_bf16 v144, v161, v20\n"
"v_dot2c_f32_bf16 v145, v162, v20\n"
"v_dot2c_f32_bf16 v146, v161, v21\n"
"v_dot2c_f32_bf16 v147, v162, v21\n"
"v_dot2c_f32_bf16 v148, v161, v22\n"
"v_dot2c_f32_bf16 v149, v162, v22\n"
"v_dot2c_f32_bf16 v150, v161, v23\n"
"v_dot2c_f32_bf16 v151, v162, v23\n"
"s_waitcnt vmcnt(9)\n"
"v_dot2c_f32_bf16 v144, v161, v24\n"
"v_dot2c_f32_bf16 v145, v162, v24\n"
"v_dot2c_f32_bf16 v146, v161, v25\n"
"v_dot2c_f32_bf16 v147, v162, v25\n"
"v_dot2c_f32_bf16 v148, v161, v26\n"
"v_dot2c_f32_bf16 v149, v162, v26\n"
"v_dot2c_f32_bf16 v150, v161, v27\n"
"v_dot2c_f32_bf16 v151, v162, v27\n"
"s_waitcnt vmcnt(8)\n"
"v_dot2c_f32_bf16 v144, v161, v28\n"
"v_dot2c_f32_bf16 v145, v162, v28\n"
"v_dot2c_f32_bf16 v146, v161, v29\n"
"v_dot2c_f32_bf16 v147, v162, v29\n"
"v_dot2c_f32_bf16 v148, v161, v30\n"
"v_dot2c_f32_bf16 v149, v162, v30\n"
"v_dot2c_f32_bf16 v150, v161, v31\n"
"v_dot2c_f32_bf16 v151, v162, v31\n"
"s_waitcnt vmcnt(7)\n"
"v_dot2c_f32_bf16 v144, v161, v32\n"
"v_dot2c_f32_bf16 v145, v162, v32\n"
"v_dot2c_f32_bf16 v146, v161, v33\n"
"v_dot2c_f32_bf16 v147, v162, v33\n"
"v_dot2c_f32_bf16 v148, v161, v34\n"
"v_dot2c_f32_bf16 v149, v162, v34\n"
"v_dot2c_f32_bf16 v150, v161, v35\n"
"v_dot2c_f32_bf16 v151, v162, v35\n"
"s_waitcnt vmcnt(6)\n"
"v_dot2c_f32_bf16 v144, v161, v36\n"
"v_dot2c_f32_bf16 v145, v162, v36\n"
"v_dot2c_f32_bf16 v146, v161, v37\n"
"v_dot2c_f32_bf16 v147, v162, v37\n"
"v_dot2c_f32_bf16 v148, v161, v38\n"
"v_dot2c_f32_bf16 v149, v162, v38\n"
"v_dot2c_f32_bf16 v150, v161, v39\n"
"v_dot2c_f32_bf16 v151, v162, v39\n"
"s_waitcnt vmcnt(5)\n"
"v_dot2c_f32_bf16 v144, v161, v40\n"
"v_dot2c_f32_bf16 v145, v162, v40\n"
"v_dot2c_f32_bf16 v146, v161, v41\n"
"v_dot2c_f32_bf16 v147, v162, v41\n"
"v_dot2c_f32_bf16 v148, v161, v42\n"
"v_dot2c_f32_bf16 v149, v162, v42\n"
"v_dot2c_f32_bf16 v150, v161, v43\n"
"v_dot2c_f32_bf16 v151, v162, v43\n"
"s_waitcnt vmcnt(4)\n"
"v_dot2c_f32_bf16 v144, v161, v44\n"
"v_dot2c_f32_bf16 v145, v162, v44\n"
"v_dot2c_f32_bf16 v146, v161, v45\n"
"v_dot2c_f32_bf16 v147, v162, v45\n"
"v_dot2c_f32_bf16 v148, v161, v46\n"
"v_dot2c_f32_bf16 v149, v162, v46\n"
"v_dot2c_f32_bf16 v150, v161, v47\n"
"v_dot2c_f32_bf16 v151, v162, v47\n"
"s_waitcnt vmcnt(3)\n"
"v_lshlrev_b32 v152, 16, v48\n"
"v_and_b32 v153, 0xffff0000, v48\n"
"v_lshlrev_b32_e64 v154, 16, v49\n"
"v_and_b32 v155, 0xffff0000, v49\n"
"v_lshlrev_b32_e64 v156, 16, v50\n"
"v_and_b32 v157, 0xffff0000, v50\n"
"v_lshlrev_b32_e64 v158, 16, v51\n"
"v_and_b32 v159, 0xffff0000, v51\n"
"s_nop 0\n"
"s_waitcnt vmcnt(2)\n"
"v_dot2c_f32_bf16 v152, v161, v52\n"
"v_dot2c_f32_bf16 v153, v162, v52\n"
"v_dot2c_f32_bf16 v154, v161, v53\n"
"v_dot2c_f32_bf16 v155, v162, v53\n"
"v_dot2c_f32_bf16 v156, v161, v54\n"
"v_dot2c_f32_bf16 v157, v162, v54\n"
"v_dot2c_f32_bf16 v158, v161, v55\n"
"v_dot2c_f32_bf16 v159, v162, v55\n"
"s_waitcnt vmcnt(1)\n"
"v_dot2c_f32_bf16 v152, v161, v56\n"
"v_dot2c_f32_bf16 v153, v162, v56\n"
"v_dot2c_f32_bf16 v154, v161, v57\n"
"v_dot2c_f32_bf16 v155, v162, v57\n"
"v_dot2c_f32_bf16 v156, v161, v58\n"
"v_dot2c_f32_bf16 v157, v162, v58\n"
"v_dot2c_f32_bf16 v158, v161, v59\n"
"v_dot2c_f32_bf16 v159, v162, v59\n"
"s_waitcnt vmcnt(0)\n"
"v_dot2c_f32_bf16 v152, v161, v60\n"
"v_dot2c_f32_bf16 v153, v162, v60\n"
"v_dot2c_f32_bf16 v154, v161, v61\n"
"v_dot2c_f32_bf16 v155, v162, v61\n"
"v_dot2c_f32_bf16 v156, v161, v62\n"
"v_dot2c_f32_bf16 v157, v162, v62\n"
"v_dot2c_f32_bf16 v158, v161, v63\n"
"v_dot2c_f32_bf16 v159, v162, v63\n"
"v_lshlrev_b32_e64 v164, 16, v116\n"
"v_and_b32 v165, 0xffff0000, v116\n"
"v_lshlrev_b32_e64 v166, 16, v117\n"
"v_and_b32 v167, 0xffff0000, v117\n"
"v_lshlrev_b32_e64 v168, 16, v118\n"
"v_and_b32 v169, 0xffff0000, v118\n"
"v_lshlrev_b32_e64 v170, 16, v119\n"
"v_and_b32 v171, 0xffff0000, v119\n"
"v_lshlrev_b32_e64 v172, 16, v112\n"
"v_and_b32 v173, 0xffff0000, v112\n"
"v_lshlrev_b32_e64 v174, 16, v113\n"
"v_and_b32 v175, 0xffff0000, v113\n"
"v_lshlrev_b32_e64 v176, 16, v114\n"
"v_and_b32 v177, 0xffff0000, v114\n"
"v_lshlrev_b32_e64 v178, 16, v115\n"
"v_and_b32 v179, 0xffff0000, v115\n"
"v_fma_f32 v180, v163, v144, v164\n"
"v_fma_f32 v181, v163, v145, v165\n"
"v_fma_f32 v182, v163, v146, v166\n"
"v_fma_f32 v183, v163, v147, v167\n"
"v_fma_f32 v184, v163, v148, v164\n"
"v_fma_f32 v185, v163, v149, v165\n"
"v_fma_f32 v186, v163, v150, v166\n"
"v_fma_f32 v187, v163, v151, v167\n"
"v_mul_f32 v188, 0xbfb8aa3b, v180\n"
"v_mul_f32 v189, 0xbfb8aa3b, v181\n"
"v_mul_f32 v190, 0xbfb8aa3b, v182\n"
"v_mul_f32 v191, 0xbfb8aa3b, v183\n"
"v_mul_f32 v192, 0xbfb8aa3b, v184\n"
"v_mul_f32 v193, 0xbfb8aa3b, v185\n"
"v_mul_f32 v194, 0xbfb8aa3b, v186\n"
"v_mul_f32 v195, 0xbfb8aa3b, v187\n"
"v_exp_f32 v188, v188\n"
"v_exp_f32 v189, v189\n"
"v_exp_f32 v190, v190\n"
"v_exp_f32 v191, v191\n"
"v_exp_f32 v192, v192\n"
"v_exp_f32 v193, v193\n"
"v_exp_f32 v194, v194\n"
"v_exp_f32 v195, v195\n"
"v_sub_f32 v196, v172, v120\n"
"v_sub_f32 v197, v173, v120\n"
"v_sub_f32 v198, v174, v120\n"
"v_sub_f32 v199, v175, v120\n"
"v_sub_f32 v200, v176, v122\n"
"v_sub_f32 v201, v177, v122\n"
"v_sub_f32 v202, v178, v122\n"
"v_sub_f32 v203, v179, v122\n"
"v_add_f32 v188, 1.0, v188\n"
"v_add_f32 v189, 1.0, v189\n"
"v_add_f32 v190, 1.0, v190\n"
"v_add_f32 v191, 1.0, v191\n"
"v_add_f32 v192, 1.0, v192\n"
"v_add_f32 v193, 1.0, v193\n"
"v_add_f32 v194, 1.0, v194\n"
"v_add_f32 v195, 1.0, v195\n"
"v_mul_f32 v196, v196, v121\n"
"v_mul_f32 v197, v197, v121\n"
"v_mul_f32 v198, v198, v121\n"
"v_mul_f32 v199, v199, v121\n"
"v_mul_f32 v200, v200, v123\n"
"v_mul_f32 v201, v201, v123\n"
"v_mul_f32 v202, v202, v123\n"
"v_mul_f32 v203, v203, v123\n"
"v_rcp_f32 v188, v188\n"
"v_rcp_f32 v189, v189\n"
"v_rcp_f32 v190, v190\n"
"v_rcp_f32 v191, v191\n"
"v_rcp_f32 v192, v192\n"
"v_rcp_f32 v193, v193\n"
"v_rcp_f32 v194, v194\n"
"v_rcp_f32 v195, v195\n"
"v_fmac_f32 v152, v196, v168\n"
"v_fmac_f32 v153, v197, v169\n"
"v_fmac_f32 v154, v198, v170\n"
"v_fmac_f32 v155, v199, v171\n"
"v_fmac_f32 v156, v200, v168\n"
"v_fmac_f32 v157, v201, v169\n"
"v_fmac_f32 v158, v202, v170\n"
"v_fmac_f32 v159, v203, v171\n"
"v_mul_f32 v180, v180, v188\n"
"v_mul_f32 v181, v181, v189\n"
"v_mul_f32 v182, v182, v190\n"
"v_mul_f32 v183, v183, v191\n"
"v_mul_f32 v184, v184, v192\n"
"v_mul_f32 v185, v185, v193\n"
"v_mul_f32 v186, v186, v194\n"
"v_mul_f32 v187, v187, v195\n"
"v_fmac_f32 v172, v152, v180\n"
"v_fmac_f32 v173, v153, v181\n"
"v_fmac_f32 v174, v154, v182\n"
"v_fmac_f32 v175, v155, v183\n"
"v_fmac_f32 v176, v156, v184\n"
"v_fmac_f32 v177, v157, v185\n"
"v_fmac_f32 v178, v158, v186\n"
"v_fmac_f32 v179, v159, v187\n"
"global_store_dwordx4 v101, v[172:175], s[52:53]\n"
"v_add_u32 v101, 36864, v101\n"
"global_store_dwordx4 v101, v[176:179], s[52:53]\n"
"s_cmp_lg_u32 s10, 0\n"
"s_cbranch_scc1 TL_END\n"
"s_cmp_lg_u32 s11, 7\n"
"s_cbranch_scc1 TL_END\n"
"v_lshlrev_b32 v80, 3, v248\n"
"s_mov_b32 s63, 0\n"
"TL_POLL2:\n"
"global_load_dwordx2 v[82:83], v80, s[100:101] offset:-2048 sc1\n"
"global_load_dwordx2 v[84:85], v80, s[100:101] offset:-1536 sc1\n"
"global_load_dwordx2 v[86:87], v80, s[100:101] offset:-1024 sc1\n"
"global_load_dwordx2 v[88:89], v80, s[100:101] offset:-512 sc1\n"
"s_waitcnt vmcnt(0)\n"
"s_mov_b64 s[78:79], 0\n"
"v_cmp_ne_u32 vcc, s4, v82\n"
"s_nop 0\n"
"v_cmp_ne_u32 s[76:77], s5, v83\n"
"s_nop 3\n"
"s_or_b64 s[78:79], s[78:79], vcc\n"
"s_or_b64 s[78:79], s[78:79], s[76:77]\n"
"v_cmp_ne_u32 vcc, s4, v84\n"
"v_cmp_ne_u32 s[76:77], s5, v85\n"
"s_nop 3\n"
"s_or_b64 s[78:79], s[78:79], vcc\n"
"s_or_b64 s[78:79], s[78:79], s[76:77]\n"
"v_cmp_ne_u32 vcc, s4, v86\n"
"v_cmp_ne_u32 s[76:77], s5, v87\n"
"s_nop 3\n"
"s_or_b64 s[78:79], s[78:79], vcc\n"
"s_or_b64 s[78:79], s[78:79], s[76:77]\n"
"v_cmp_ne_u32 vcc, s4, v88\n"
"v_cmp_ne_u32 s[76:77], s5, v89\n"
"s_nop 3\n"
"s_or_b64 s[78:79], s[78:79], vcc\n"
"s_or_b64 s[78:79], s[78:79], s[76:77]\n"
"s_cmp_eq_u64 s[78:79], 0\n"
"s_cbranch_scc1 TL_BUMP\n"
"s_add_u32 s63, s63, 1\n"
"s_cmp_gt_u32 s63, 400000\n"
"s_cbranch_scc1 TL_BUMP\n"
"s_sleep 1\n"
"s_branch TL_POLL2\n"
"TL_BUMP:\n"
"s_mov_b64 exec, 1\n"
"v_mov_b32 v80, 6144\n"
"v_mov_b32_e64 v82, s4\n"
"global_store_dword v80, v82, s[100:101] sc1\n"
"s_mov_b64 exec, -1\n"
"TL_END:\n"
"s_waitcnt vmcnt(0)\n"
"s_endpgm\n"
"KEND:\n"
    : "+v"(tid) : "s"(ka), "s"(blockIdx.x) :
      "v1", "v2", "v3", "v4", "v5", "v6", "v7", "v8", "v9", "v10", "v11", "v12", "v13", "v14", "v15", "v16", "v17", "v18", "v19", "v20", "v21", "v22", "v23",
      "v24", "v25", "v26", "v27", "v28", "v29", "v30", "v31", "v32", "v33", "v34", "v35", "v36", "v37", "v38", "v39", "v40", "v41", "v42", "v43", "v44",
      "v45", "v46", "v47", "v48", "v49", "v50", "v51", "v52", "v53", "v54", "v55", "v56", "v57", "v58", "v59", "v60", "v61", "v62", "v63", "v64", "v65",
      "v66", "v67", "v68", "v69", "v70", "v71", "v72", "v73", "v74", "v75", "v76", "v77", "v78", "v79", "v80", "v81", "v82", "v83", "v84", "v85", "v86",
      "v87", "v88", "v89", "v90", "v91", "v92", "v93", "v94", "v95", "v96", "v97", "v98", "v99", "v100", "v101", "v102", "v103", "v104", "v105", "v106",
      "v107", "v108", "v109", "v110", "v111", "v112", "v113", "v114", "v115", "v116", "v117", "v118", "v119", "v120", "v121", "v122", "v123", "v124", "v125",
      "v126", "v127", "v128", "v129", "v130", "v131", "v132", "v133", "v134", "v135", "v136", "v137", "v138", "v139", "v140", "v141", "v142", "v143", "v144",
      "v145", "v146", "v147", "v148", "v149", "v150", "v151", "v152", "v153", "v154", "v155", "v156", "v157", "v158", "v159", "v160", "v161", "v162", "v163",
      "v164", "v165", "v166", "v167", "v168", "v169", "v170", "v171", "v172", "v173", "v174", "v175", "v176", "v177", "v178", "v179", "v180", "v181", "v182",
      "v183", "v184", "v185", "v186", "v187", "v188", "v189", "v190", "v191", "v192", "v193", "v194", "v195", "v196", "v197", "v198", "v199", "v200", "v201",
      "v202", "v203", "v204", "v205", "v206", "v207", "v208", "v209", "v210", "v211", "v212", "v213", "v214", "v215", "v216", "v217", "v218", "v219", "v220",
      "v221", "v222", "v223", "v224", "v225", "v226", "v227", "v228", "v229", "v230", "v231", "v232", "v233", "v234", "v235", "v236", "v237", "v238", "v239",
      "v240", "v241", "v242", "v243", "v244", "v245", "v246", "v247", "v248", "v249", "v250", "v251", "v252", "v253", "v254", "v255", "s3", "s4", "s5", "s6",
      "s7", "s8", "s9", "s10", "s11", "s12", "s13", "s14", "s15", "s16", "s17", "s18", "s19", "s20", "s21", "s22", "s23", "s24", "s25", "s26", "s27", "s28",
      "s29", "s30", "s31", "s34", "s35", "s36", "s37", "s38", "s39", "s40", "s41", "s42", "s43", "s44", "s45", "s46", "s47", "s48", "s49", "s50", "s51",
      "s52", "s53", "s54", "s55", "s56", "s57", "s58", "s59", "s60", "s61", "s62", "s63", "s64", "s65", "s66", "s67", "s68", "s69", "s70", "s71", "s72",
      "s73", "s74", "s75", "s76", "s77", "s78", "s79", "s80", "s81", "s82", "s83", "s84", "s85", "s86", "s87", "s88", "s89", "s90", "s91", "s92", "s93",
      "s94", "s95", "s96", "s97", "s98", "s99", "s100", "s101", "vcc", "scc", "memory", "m0");
}

extern "C" void kernel_launch(void* const* d_in, const int* in_sizes, int n_in, void* d_out, int out_size, void* d_ws, size_t ws_size, hipStream_t stream) {
  const u16* x = (const u16*)d_in[0];
  const u16 *lre = (const u16*)d_in[1], *lim = (const u16*)d_in[2], *bre = (const u16*)d_in[3], *bim = (const u16*)d_in[4];
  const u16 *cre = (const u16*)d_in[5], *cim = (const u16*)d_in[6], *ldt = (const u16*)d_in[7], *dsk = (const u16*)d_in[8];
  const u16 *w1 = (const u16*)d_in[10], *b1 = (const u16*)d_in[11], *w2 = (const u16*)d_in[12], *b2 = (const u16*)d_in[13];
  hipFuncSetAttribute((const void*)fused_main, hipFuncAttributeMaxDynamicSharedMemorySize, 163840);
  fused_main<<<256, 512, 163840, stream>>>(x, lre, lim, bre, bim, cre, cim, ldt, dsk, w1, b1, w2, b2, (float*)d_out, (char*)d_ws);
}
```
